```python
import torch

def _softmax(x, axis=-1):
    return torch.softmax(x, dim=axis)
def _gelu(x):
    return torch.nn.functional.gelu(x, approximate="tanh")
def _silu(x):
    return torch.nn.functional.silu(x)
def _sigmoid(x):
    return torch.sigmoid(x)
def _layernorm(x, axis=-1, eps=1e-5):
    m = x.mean(dim=axis, keepdim=True)
    v = x.var(dim=axis, keepdim=True, unbiased=False)
    return (x - m) / torch.sqrt(v + eps)
def _scatter_add(tgt, idx, src, axis=0):
    return tgt.clone().index_add_(axis, idx, src)
def _segment_sum(src, seg, n):
    out = torch.zeros((n,) + src.shape[1:], dtype=src.dtype)
    return out.index_add_(0, seg, src)
def _pad(x, pw):
    flat = tuple(v for pair in reversed(pw) for v in pair)
    return torch.nn.functional.pad(x, flat)

kernel_name = 'standalone__gqa_block_cached__near_ridge'

def setup_inputs():

    _w = (lambda a: (lambda t: t.to(torch.bfloat16) if t.is_floating_point() else t)(torch.as_tensor(a)))
    B, Sq, D = 8, 192, 640
    Hq, Hkv, Sk, Dh = 32, 32, 2048, 128
    assert Hq % Hkv == 0, (Hq, Hkv)
    return {
        "x": _w((lambda *s: torch.randn(*s, dtype=torch.bfloat16) * 0.8)(B, Sq, D)),
        "k_cache": _w((lambda *s: torch.randn(*s, dtype=torch.bfloat16) * 0.8)(B, Hkv, Sk, Dh)),
        "v_cache": _w((lambda *s: torch.randn(*s, dtype=torch.bfloat16) * 0.05)(B, Hkv, Sk, Dh)),
        "wq": _w((lambda *s: torch.randn(*s, dtype=torch.bfloat16) * 0.8)(D, Hq * Dh) * (D ** -0.5)),
        "wo": _w((lambda *s: torch.randn(*s, dtype=torch.bfloat16) * 0.05)(Hq * Dh, D) * ((Hq * Dh) ** -0.5)),
    }

def reference(x, k_cache, v_cache, wq, wo):
    B, Sq, D = x.shape
    Hq, Dh = 32, 128
    Hkv = k_cache.shape[1]
    rep = Hq // Hkv
    q = (lambda x, *d: x.permute(*d))((x @ wq).reshape(B, Sq, Hq, Dh), 0, 2, 1, 3)
    k = (lambda x, n, axis: torch.repeat_interleave(x, n, dim=axis))(k_cache, rep, axis=1)
    v = (lambda x, n, axis: torch.repeat_interleave(x, n, dim=axis))(v_cache, rep, axis=1)
    scores = torch.einsum("bhqd,bhkd->bhqk", q, k) * (Dh ** -0.5)
    p = _softmax(scores, axis=-1)
    o = torch.einsum("bhqk,bhkd->bhqd", p, v)
    return (lambda x, *d: x.permute(*d))(o, 0, 2, 1, 3).reshape(B, Sq, Hq * Dh) @ wo
```

```cpp
#include <hip/hip_runtime.h>
#include <hip/hip_bf16.h>
typedef __hip_bfloat16 bf16;
constexpr int B=8,SQ=192,D=640,H=32,SK=2048,DH=128;

__global__ void gemm_naive(const bf16* A, const bf16* Bm, bf16* C, int M, int N, int K){
  __shared__ float As[16][17], Bs[16][17];
  int tx=threadIdx.x, ty=threadIdx.y;
  int row=blockIdx.y*16+ty, col=blockIdx.x*16+tx;
  float acc=0;
  for(int k0=0;k0<K;k0+=16){
    As[ty][tx]=__bfloat162float(A[(size_t)row*K+k0+tx]);
    Bs[ty][tx]=__bfloat162float(Bm[(size_t)(k0+ty)*N+col]);
    __syncthreads();
    for(int k=0;k<16;k++) acc+=As[ty][k]*Bs[k][tx];
    __syncthreads();
  }
  C[(size_t)row*N+col]=__float2bfloat16(acc);
}
__global__ void attn_naive(const bf16* Q, const bf16* Kc, const bf16* Vc, bf16* O){
  __shared__ float q[DH]; __shared__ float s[SK]; __shared__ float red[256];
  int qi=blockIdx.x, h=blockIdx.y, b=blockIdx.z, t=threadIdx.x;
  if(t<DH) q[t]=__bfloat162float(Q[((size_t)(b*SQ+qi))*(H*DH)+h*DH+t]);
  __syncthreads();
  const bf16* Kp=Kc+((size_t)(b*H+h))*SK*DH; const bf16* Vp=Vc+((size_t)(b*H+h))*SK*DH;
  float mx=-1e30f;
  for(int k=t;k<SK;k+=256){ float a=0; for(int d=0;d<DH;d++) a+=q[d]*__bfloat162float(Kp[(size_t)k*DH+d]); a*=0.08838834764f; s[k]=a; mx=fmaxf(mx,a);}
  red[t]=mx; __syncthreads();
  for(int o=128;o>0;o>>=1){ if(t<o) red[t]=fmaxf(red[t],red[t+o]); __syncthreads(); }
  mx=red[0]; __syncthreads();
  float sum=0; for(int k=t;k<SK;k+=256){ float e=__expf(s[k]-mx); s[k]=e; sum+=e; }
  red[t]=sum; __syncthreads();
  for(int o=128;o>0;o>>=1){ if(t<o) red[t]+=red[t+o]; __syncthreads(); }
  sum=red[0];
  if(t<DH){ float a=0; for(int k=0;k<SK;k++) a+=s[k]*__bfloat162float(Vp[(size_t)k*DH+t]); O[((size_t)(b*SQ+qi))*(H*DH)+h*DH+t]=__float2bfloat16(a/sum);}
}
extern "C" void kernel_launch(void* const* d_in, const int* in_sizes, int n_in, void* d_out, int out_size, void* d_ws, size_t ws_size, hipStream_t stream){
  const bf16* x=(const bf16*)d_in[0]; const bf16* k=(const bf16*)d_in[1]; const bf16* v=(const bf16*)d_in[2];
  const bf16* wq=(const bf16*)d_in[3]; const bf16* wo=(const bf16*)d_in[4];
  bf16* q=(bf16*)d_ws; bf16* o=q+(size_t)B*SQ*H*DH;
  gemm_naive<<<dim3(H*DH/16,B*SQ/16),dim3(16,16),0,stream>>>(x,wq,q,B*SQ,H*DH,D);
  attn_naive<<<dim3(SQ,H,B),256,0,stream>>>(q,k,v,o);
  gemm_naive<<<dim3(D/16,B*SQ/16),dim3(16,16),0,stream>>>(o,wo,(bf16*)d_out,B*SQ,D,H*DH);
}
```

```cpp
#include <hip/hip_runtime.h>

__global__ __launch_bounds__(256,1) void attn_k(const void* x, const void* k, const void* v, const void* wq, const void* wo, void* out, void* ws){
  auto ka = __builtin_amdgcn_kernarg_segment_ptr();
  unsigned wid = __builtin_amdgcn_readfirstlane(threadIdx.x>>6);
  unsigned bid = blockIdx.x;
  asm volatile(
"s_load_dwordx8 s[60:67], %0, 0\n"
"s_load_dwordx4 s[68:71], %0, 32\n"
"s_load_dwordx2 s[72:73], %0, 48\n"
"s_mov_b32 s40, %1\n"
"s_getpc_b64 s[48:49]\n"
"s_lshr_b32 s53, %2, 3\n"
"s_lshl2_add_u32 s54, s53, s40\n"
"s_lshl_b32 s54, s54, 6\n"
"s_add_u32 s48, s48, s54\n"
"s_addc_u32 s49, s49, 0\n"
"v_mbcnt_lo_u32_b32 v14, -1, 0\n"
"v_lshlrev_b32 v14, 13, v14\n"
"s_mov_b64 exec, 7\n"
"global_load_dword v200, v14, s[48:49]\n"
"s_mov_b64 exec, -1\n"
"s_lshr_b32 s52, %2, 6\n"
"s_bfe_u32 s53, %2, 0x30003\n"
"s_and_b32 s54, %2, 1\n"
"s_lshl2_add_u32 s47, s54, s52\n"
"s_bfe_u32 s54, %2, 0x20001\n"
"s_lshl3_add_u32 s50, s54, s53\n"
"s_lshl_b32 s41, s47, 5\n"
"s_or_b32 s41, s41, s50\n"
"s_lshr_b32 s53, s53, 2\n"
"s_lshr_b32 s52, s52, 1\n"
"s_mul_i32 s52, s52, 2\n"
"s_add_u32 s52, s52, s53\n"
"s_mul_i32 s54, s54, 3\n"
"s_add_u32 s52, s52, s54\n"
"s_and_b32 s54, %2, 1\n"
"s_mul_i32 s54, s54, 5\n"
"s_add_u32 s52, s52, s54\n"
"s_mul_i32 s53, s52, 26\n"
"s_lshr_b32 s53, s53, 8\n"
"s_mul_i32 s53, s53, 10\n"
"s_sub_u32 s52, s52, s53\n"
"s_lshl_b32 s56, s52, 7\n"
"s_lshl_b32 s57, s52, 19\n"
"s_mul_i32 s58, s40, 12288\n"
"s_add_u32 s58, s58, 81920\n"
"s_lshl_b32 s59, s40, 12\n"
"s_add_u32 s59, s59, 32768\n"
"v_mbcnt_lo_u32_b32 v15, -1, 0\n"
"v_mbcnt_hi_u32_b32 v15, -1, v15\n"
"v_lshrrev_b32 v0, 3, v15\n"
"v_and_b32 v2, 7, v15\n"
"v_and_b32 v1, 6, v0\n"
"v_xor_b32 v1, v1, v2\n"
"v_mul_u32_u24 v0, 1280, v0\n"
"v_lshl_add_u32 v224, v1, 4, v0\n"
"v_add_u32 v225, 9216, v224\n"
"v_add_u32 v226, 18432, v224\n"
"v_lshrrev_b32 v0, 5, v15\n"
"v_bfe_u32 v1, v15, 3, 2\n"
"v_lshlrev_b32 v3, 13, v1\n"
"v_lshl_add_u32 v3, v0, 7, v3\n"
"v_lshrrev_b32 v4, 1, v1\n"
"v_lshlrev_b32 v6, 2, v4\n"
"v_xor_b32 v5, v6, v2\n"
"v_lshl_add_u32 v227, v5, 4, v3\n"
"v_add_u32 v228, 31744, v227\n"
"v_xor_b32 v229, 32, v227\n"
"v_add_u32 v229, 63488, v229\n"
"v_add_u32 v230, 31744, v229\n"
"s_waitcnt lgkmcnt(0)\n"
"s_add_u32 s54, s72, 33554432\n"
"s_addc_u32 s55, s73, 0\n"
"s_load_dwordx2 s[74:75], s[54:55], 32768\n"
"s_lshl2_add_u32 s52, s47, s40\n"
"s_mul_i32 s52, s52, 61440\n"
"s_add_u32 s28, s60, s52\n"
"s_addc_u32 s29, s61, 0\n"
"s_mov_b32 s30, 61440\n"
"s_mov_b32 s31, 0x20000\n"
"s_lshl_b32 s52, s50, 8\n"
"s_lshl_b32 s53, s40, 17\n"
"s_add_u32 s52, s52, s53\n"
"s_add_u32 s36, s66, s52\n"
"s_addc_u32 s37, s67, 0\n"
"s_sub_u32 s38, 5242880, s52\n"
"s_mov_b32 s39, 0x20000\n"
"s_add_u32 m0, s59, 32768\n"
"s_nop 0\n"
"buffer_load_dwordx4 v227, s[36:39], s57 offen offset:0  lds\n"
"buffer_load_dwordx4 v228, s[36:39], s57 offen offset:1024  lds\n"
"buffer_load_dwordx4 v229, s[36:39], s57 offen offset:2048  lds\n"
"buffer_load_dwordx4 v230, s[36:39], s57 offen offset:3072  lds\n"
"s_add_u32 s57, s57, 524288\n"
"s_cmp_eq_u32 s57, 5242880\n"
"s_cselect_b32 s57, 0, s57\n"
"s_add_u32 m0, s58, 0\n"
"s_nop 0\n"
"buffer_load_dwordx4 v224, s[28:31], s56 offen offset:0  lds\n"
"buffer_load_dwordx4 v225, s[28:31], s56 offen offset:1024  lds\n"
"buffer_load_dwordx4 v226, s[28:31], s56 offen offset:2048  lds\n"
"s_add_u32 s56, s56, 30720\n"
"s_add_u32 m0, s58, 3072\n"
"s_nop 0\n"
"buffer_load_dwordx4 v224, s[28:31], s56 offen offset:0  lds\n"
"buffer_load_dwordx4 v225, s[28:31], s56 offen offset:1024  lds\n"
"buffer_load_dwordx4 v226, s[28:31], s56 offen offset:2048  lds\n"
"s_sub_u32 s56, s56, 30592\n"
"s_cmp_eq_u32 s56, 1280\n"
"s_cselect_b32 s56, 0, s56\n"
"s_lshr_b32 s53, s41, 13\n"
"s_lshl_b32 s52, s41, 19\n"
"s_add_u32 s20, s62, s52\n"
"s_addc_u32 s21, s63, s53\n"
"s_mov_b32 s22, 524288\n"
"s_mov_b32 s23, 0x20000\n"
"s_add_u32 s24, s64, s52\n"
"s_addc_u32 s25, s65, s53\n"
"s_mov_b32 s26, 524288\n"
"s_mov_b32 s27, 0x20000\n"
"v_lshlrev_b32 v3, 8, v1\n"
"v_lshl_add_u32 v3, v0, 7, v3\n"
"s_sub_u32 s52, 0, s40\n"
"s_and_b32 s52, s52, 3\n"
"v_xor_b32 v6, s52, v5\n"
"v_lshl_add_u32 v252, v6, 4, v3\n"
"s_and_b32 s52, s40, 1\n"
"s_lshl_b32 s52, s52, 1\n"
"v_xor_b32 v6, s52, v5\n"
"v_lshl_add_u32 v253, v6, 4, v3\n"
"s_lshl_b32 s46, s40, 10\n"
"s_mov_b32 s44, s46\n"
"s_mov_b32 s51, s46\n"
"s_lshr_b32 s52, s40, 1\n"
"s_lshl_b32 s53, s52, 14\n"
"v_bfe_u32 v0, v15, 2, 2\n"
"v_and_b32 v1, 3, v15\n"
"v_lshrrev_b32 v2, 4, v15\n"
"v_lshl_add_u32 v3, v0, 10, s53\n"
"v_lshl_add_u32 v3, v1, 7, v3\n"
"v_sub_u32 v4, 0, v0\n"
"v_and_b32 v4, 3, v4\n"
"v_lshrrev_b32 v5, 1, v1\n"
"v_lshl_or_b32 v4, v5, 2, v4\n"
"v_xor_b32 v4, v4, v2\n"
"v_lshl_add_u32 v246, v4, 4, v3\n"
"v_xor_b32 v247, 64, v246\n"
"s_add_u32 s53, s53, 98304\n"
"v_lshlrev_b32 v3, 7, v0\n"
"v_lshl_add_u32 v3, v1, 3, v3\n"
"v_and_b32 v4, 2, v0\n"
"v_and_or_b32 v4, v2, 1, v4\n"
"v_lshl_add_u32 v3, v4, 5, v3\n"
"v_lshl_add_u32 v5, v2, 10, v3\n"
"v_add_u32 v248, s53, v5\n"
"v_lshl_add_u32 v5, v2, 11, v3\n"
"v_add_u32 v233, 32768, v5\n"
"v_xor_b32 v249, 32, v248\n"
"v_xor_b32 v234, 32, v233\n"
"v_xor_b32 v250, 64, v248\n"
"v_xor_b32 v235, 64, v233\n"
"v_xor_b32 v251, 96, v248\n"
"v_xor_b32 v236, 96, v233\n"
"v_and_b32 v3, 15, v15\n"
"v_and_b32 v4, 6, v15\n"
"v_xor_b32 v4, v4, v2\n"
"v_lshl_add_u32 v5, v3, 7, s58\n"
"v_lshl_add_u32 v231, v4, 4, v5\n"
"v_xor_b32 v232, 64, v231\n"
"s_sub_u32 s54, s58, 16384\n"
"v_lshl_add_u32 v5, v3, 4, s54\n"
"v_lshrrev_b32 v4, 1, v2\n"
"v_lshl_add_u32 v5, v4, 8, v5\n"
"v_and_b32 v4, 1, v2\n"
"v_lshl_add_u32 v237, v4, 3, v5\n"
"s_and_b32 s54, s40, 1\n"
"s_lshl1_add_u32 s54, s54, s52\n"
"s_xor_b32 s55, s54, 1\n"
"s_mul_i32 s54, s54, 12288\n"
"s_add_u32 s54, s54, 65536\n"
"s_mul_i32 s55, s55, 12288\n"
"s_add_u32 s55, s55, 65536\n"
"v_lshl_add_u32 v238, v15, 4, s55\n"
"v_lshl_add_u32 v239, v15, 4, s54\n"
"s_mov_b32 s42, 1199570944\n"
"s_mov_b32 s43, 0xff800000\n"
"s_mov_b32 s45, 7\n"
"s_mov_b32 s96, 0x3e0293ee\n"
"v_mov_b32 v254, s42\n"
"v_mov_b32 v4, 3078619136\n"
"v_cmp_gt_u32 vcc, 32, v15\n"
"s_nop 1\n"
"v_cndmask_b32 v254, v4, v254, vcc\n"
"v_mov_b32 v4, 0x3f803f80\n"
"v_mov_b32 v5, 0xbf80bf80\n"
"v_cmp_gt_u32 vcc, 8, v3\n"
"s_nop 1\n"
"v_cndmask_b32 v4, v5, v4, vcc\n"
"v_cmp_eq_u32 vcc, 0, v1\n"
"s_nop 1\n"
"v_cndmask_b32 v240, 0, v4, vcc\n"
"v_mov_b64 v[0:1], 0\n"
"v_mov_b64 v[2:3], 0\n"
"v_mov_b64 v[4:5], 0\n"
"v_mov_b64 v[6:7], 0\n"
"v_mov_b64 v[8:9], 0\n"
"v_mov_b64 v[10:11], 0\n"
"v_mov_b64 v[12:13], 0\n"
"v_mov_b64 v[14:15], 0\n"
"v_mov_b64 v[16:17], 0\n"
"v_mov_b64 v[18:19], 0\n"
"v_mov_b64 v[20:21], 0\n"
"v_mov_b64 v[22:23], 0\n"
"v_mov_b64 v[24:25], 0\n"
"v_mov_b64 v[26:27], 0\n"
"v_mov_b64 v[28:29], 0\n"
"v_mov_b64 v[30:31], 0\n"
"v_mov_b64 v[32:33], 0\n"
"v_mov_b64 v[34:35], 0\n"
"v_mov_b64 v[36:37], 0\n"
"v_mov_b64 v[38:39], 0\n"
"v_mov_b64 v[40:41], 0\n"
"v_mov_b64 v[42:43], 0\n"
"v_mov_b64 v[44:45], 0\n"
"v_mov_b64 v[46:47], 0\n"
"v_mov_b64 v[48:49], 0\n"
"v_mov_b64 v[50:51], 0\n"
"v_mov_b64 v[52:53], 0\n"
"v_mov_b64 v[54:55], 0\n"
"v_mov_b64 v[56:57], 0\n"
"v_mov_b64 v[58:59], 0\n"
"v_mov_b64 v[60:61], 0\n"
"v_mov_b64 v[62:63], 0\n"
"v_mov_b64 v[64:65], 0\n"
"v_mov_b64 v[66:67], 0\n"
"v_mov_b64 v[68:69], 0\n"
"v_mov_b64 v[70:71], 0\n"
"v_mov_b64 v[72:73], 0\n"
"v_mov_b64 v[74:75], 0\n"
"v_mov_b64 v[76:77], 0\n"
"v_mov_b64 v[78:79], 0\n"
"v_mov_b64 v[80:81], 0\n"
"v_mov_b64 v[82:83], 0\n"
"v_mov_b64 v[84:85], 0\n"
"v_mov_b64 v[86:87], 0\n"
"v_mov_b64 v[88:89], 0\n"
"v_mov_b64 v[90:91], 0\n"
"v_mov_b64 v[92:93], 0\n"
"v_mov_b64 v[94:95], 0\n"
"v_mov_b64 v[96:97], 0\n"
"v_mov_b64 v[98:99], 0\n"
"v_mov_b64 v[100:101], 0\n"
"v_mov_b64 v[102:103], 0\n"
"v_mov_b64 v[104:105], 0\n"
"v_mov_b64 v[106:107], 0\n"
"v_mov_b64 v[108:109], 0\n"
"v_mov_b64 v[110:111], 0\n"
"v_mov_b64 v[112:113], 0\n"
"v_mov_b64 v[114:115], 0\n"
"v_mov_b64 v[116:117], 0\n"
"v_mov_b64 v[118:119], 0\n"
"v_mov_b64 v[120:121], 0\n"
"v_mov_b64 v[122:123], 0\n"
"v_mov_b64 v[124:125], 0\n"
"v_mov_b64 v[126:127], 0\n"
"v_accvgpr_write_b32 a0, 0\n"
"v_accvgpr_write_b32 a1, 0\n"
"v_accvgpr_write_b32 a2, 0\n"
"v_accvgpr_write_b32 a3, 0\n"
"v_accvgpr_write_b32 a4, 0\n"
"v_accvgpr_write_b32 a5, 0\n"
"v_accvgpr_write_b32 a6, 0\n"
"v_accvgpr_write_b32 a7, 0\n"
"v_accvgpr_write_b32 a8, 0\n"
"v_accvgpr_write_b32 a9, 0\n"
"v_accvgpr_write_b32 a10, 0\n"
"v_accvgpr_write_b32 a11, 0\n"
"v_accvgpr_write_b32 a12, 0\n"
"v_accvgpr_write_b32 a13, 0\n"
"v_accvgpr_write_b32 a14, 0\n"
"v_accvgpr_write_b32 a15, 0\n"
"v_accvgpr_write_b32 a16, 0\n"
"v_accvgpr_write_b32 a17, 0\n"
"v_accvgpr_write_b32 a18, 0\n"
"v_accvgpr_write_b32 a19, 0\n"
"v_accvgpr_write_b32 a20, 0\n"
"v_accvgpr_write_b32 a21, 0\n"
"v_accvgpr_write_b32 a22, 0\n"
"v_accvgpr_write_b32 a23, 0\n"
"v_accvgpr_write_b32 a24, 0\n"
"v_accvgpr_write_b32 a25, 0\n"
"v_accvgpr_write_b32 a26, 0\n"
"v_accvgpr_write_b32 a27, 0\n"
"v_accvgpr_write_b32 a28, 0\n"
"v_accvgpr_write_b32 a29, 0\n"
"v_accvgpr_write_b32 a30, 0\n"
"v_accvgpr_write_b32 a31, 0\n"
"s_add_u32 m0, s59, 0\n"
"s_nop 0\n"
"buffer_load_dwordx4 v227, s[36:39], s57 offen offset:0  lds\n"
"buffer_load_dwordx4 v228, s[36:39], s57 offen offset:1024  lds\n"
"buffer_load_dwordx4 v229, s[36:39], s57 offen offset:2048  lds\n"
"buffer_load_dwordx4 v230, s[36:39], s57 offen offset:3072  lds\n"
"s_add_u32 s57, s57, 524288\n"
"s_cmp_eq_u32 s57, 5242880\n"
"s_cselect_b32 s57, 0, s57\n"
"s_add_u32 m0, s58, 6144\n"
"s_nop 0\n"
"buffer_load_dwordx4 v224, s[28:31], s56 offen offset:0  lds\n"
"buffer_load_dwordx4 v225, s[28:31], s56 offen offset:1024  lds\n"
"buffer_load_dwordx4 v226, s[28:31], s56 offen offset:2048  lds\n"
"s_add_u32 s56, s56, 30720\n"
"s_add_u32 m0, s58, 9216\n"
"s_nop 0\n"
"buffer_load_dwordx4 v224, s[28:31], s56 offen offset:0  lds\n"
"buffer_load_dwordx4 v225, s[28:31], s56 offen offset:1024  lds\n"
"buffer_load_dwordx4 v226, s[28:31], s56 offen offset:2048  lds\n"
"s_sub_u32 s56, s56, 30592\n"
"s_cmp_eq_u32 s56, 1280\n"
"s_cselect_b32 s56, 0, s56\n"
"s_waitcnt vmcnt(10) lgkmcnt(0)\n"
"s_barrier\n"
"s_add_u32 s74, s74, 2135587861\n"
"s_addc_u32 s75, s75, 2654435769\n"
"ds_read_b128 a[184:187], v231 offset:0\n"
"ds_read_b128 a[188:191], v232 offset:0\n"
"ds_read_b128 a[192:195], v231 offset:2048\n"
"ds_read_b128 a[196:199], v232 offset:2048\n"
"ds_read_b128 a[200:203], v231 offset:4096\n"
"ds_read_b128 a[204:207], v232 offset:4096\n"
"ds_read_b64_tr_b16 a[32:33], v233 offset:32768\n"
"ds_read_b64_tr_b16 a[34:35], v233 offset:33792\n"
"ds_read_b64_tr_b16 a[36:37], v233 offset:40960\n"
"ds_read_b64_tr_b16 a[38:39], v233 offset:41984\n"
"ds_read_b64_tr_b16 a[40:41], v234 offset:32768\n"
"ds_read_b64_tr_b16 a[42:43], v234 offset:33792\n"
"ds_read_b64_tr_b16 a[44:45], v234 offset:40960\n"
"ds_read_b64_tr_b16 a[46:47], v234 offset:41984\n"
"ds_read_b64_tr_b16 a[48:49], v235 offset:32768\n"
"ds_read_b64_tr_b16 a[50:51], v235 offset:33792\n"
"ds_read_b64_tr_b16 a[52:53], v235 offset:40960\n"
"ds_read_b64_tr_b16 a[54:55], v235 offset:41984\n"
"ds_read_b64_tr_b16 a[56:57], v236 offset:32768\n"
"ds_read_b64_tr_b16 a[58:59], v236 offset:33792\n"
"ds_read_b64_tr_b16 a[60:61], v236 offset:40960\n"
"ds_read_b64_tr_b16 a[62:63], v236 offset:41984\n"
"ds_read_b64_tr_b16 a[64:65], v233 offset:33280\n"
"ds_read_b64_tr_b16 a[66:67], v233 offset:34304\n"
"ds_read_b64_tr_b16 a[68:69], v233 offset:41472\n"
"ds_read_b64_tr_b16 a[70:71], v233 offset:42496\n"
"ds_read_b64_tr_b16 a[72:73], v234 offset:33280\n"
"ds_read_b64_tr_b16 a[74:75], v234 offset:34304\n"
"ds_read_b64_tr_b16 a[76:77], v234 offset:41472\n"
"ds_read_b64_tr_b16 a[78:79], v234 offset:42496\n"
"ds_read_b64_tr_b16 a[80:81], v235 offset:33280\n"
"ds_read_b64_tr_b16 a[82:83], v235 offset:34304\n"
"ds_read_b64_tr_b16 a[84:85], v235 offset:41472\n"
"ds_read_b64_tr_b16 a[86:87], v235 offset:42496\n"
"ds_read_b64_tr_b16 a[88:89], v236 offset:33280\n"
"ds_read_b64_tr_b16 a[90:91], v236 offset:34304\n"
"ds_read_b64_tr_b16 a[92:93], v236 offset:41472\n"
"ds_read_b64_tr_b16 a[94:95], v236 offset:42496\n"
"s_waitcnt lgkmcnt(15)\n"
"s_add_u32 m0, s59, 16384\n"
"s_nop 0\n"
"buffer_load_dwordx4 v227, s[36:39], s57 offen offset:0  lds\n"
"buffer_load_dwordx4 v228, s[36:39], s57 offen offset:1024  lds\n"
"buffer_load_dwordx4 v229, s[36:39], s57 offen offset:2048  lds\n"
"buffer_load_dwordx4 v230, s[36:39], s57 offen offset:3072  lds\n"
"s_add_u32 s57, s57, 524288\n"
"s_cmp_eq_u32 s57, 5242880\n"
"s_cselect_b32 s57, 0, s57\n"
"s_add_u32 m0, s58, 0\n"
"s_nop 0\n"
"buffer_load_dwordx4 v224, s[28:31], s56 offen offset:0  lds\n"
"buffer_load_dwordx4 v225, s[28:31], s56 offen offset:1024  lds\n"
"buffer_load_dwordx4 v226, s[28:31], s56 offen offset:2048  lds\n"
"s_add_u32 s56, s56, 30720\n"
"s_add_u32 m0, s58, 3072\n"
"s_nop 0\n"
"buffer_load_dwordx4 v224, s[28:31], s56 offen offset:0  lds\n"
"buffer_load_dwordx4 v225, s[28:31], s56 offen offset:1024  lds\n"
"buffer_load_dwordx4 v226, s[28:31], s56 offen offset:2048  lds\n"
"s_sub_u32 s56, s56, 30592\n"
"s_cmp_eq_u32 s56, 1280\n"
"s_cselect_b32 s56, 0, s56\n"
"s_add_u32 m0, s46, 0\n"
"s_nop 0\n"
"buffer_load_dwordx4 v252, s[20:23], s44 offen nt lds\n"
"s_add_u32 s44, s44, 4096\n"
"s_waitcnt vmcnt(11) lgkmcnt(0)\n"
"s_barrier\n"
"v_mfma_f32_16x16x32_bf16 v[128:131], a[32:35], a[184:187], 0\n"
"s_add_u32 m0, s59, 32768\n"
"ds_read_b128 a[160:163], v231 offset:6144\n"
"ds_read_b128 a[164:167], v232 offset:6144\n"
"v_mfma_f32_16x16x32_bf16 v[128:131], a[36:39], a[188:191], v[128:131]\n"
"buffer_load_dwordx4 v227, s[36:39], s57 offen offset:0  lds\n"
"v_mfma_f32_16x16x32_bf16 v[132:135], a[32:35], a[192:195], 0\n"
"ds_read_b128 a[168:171], v231 offset:8192\n"
"ds_read_b128 a[172:175], v232 offset:8192\n"
"v_mfma_f32_16x16x32_bf16 v[132:135], a[36:39], a[196:199], v[132:135]\n"
"ds_read_b128 a[176:179], v231 offset:10240\n"
"v_mfma_f32_16x16x32_bf16 v[136:139], a[32:35], a[200:203], 0\n"
"buffer_load_dwordx4 v228, s[36:39], s57 offen offset:1024  lds\n"
"s_sleep 1\n"
"v_mfma_f32_16x16x32_bf16 v[136:139], a[36:39], a[204:207], v[136:139]\n"
"ds_read_b128 a[180:183], v232 offset:10240\n"
"ds_read_b64_tr_b16 a[96:97], v233 offset:0\n"
"v_mfma_f32_16x16x32_bf16 v[140:143], a[40:43], a[184:187], 0\n"
"ds_read_b64_tr_b16 a[98:99], v233 offset:1024\n"
"v_mfma_f32_16x16x32_bf16 v[140:143], a[44:47], a[188:191], v[140:143]\n"
"buffer_load_dwordx4 v229, s[36:39], s57 offen offset:2048  lds\n"
"v_mfma_f32_16x16x32_bf16 v[144:147], a[40:43], a[192:195], 0\n"
"ds_read_b64_tr_b16 a[100:101], v233 offset:8192\n"
"ds_read_b64_tr_b16 a[102:103], v233 offset:9216\n"
"v_mfma_f32_16x16x32_bf16 v[144:147], a[44:47], a[196:199], v[144:147]\n"
"ds_read_b64_tr_b16 a[104:105], v234 offset:0\n"
"v_mfma_f32_16x16x32_bf16 v[148:151], a[40:43], a[200:203], 0\n"
"buffer_load_dwordx4 v230, s[36:39], s57 offen offset:3072  lds\n"
"v_mfma_f32_16x16x32_bf16 v[148:151], a[44:47], a[204:207], v[148:151]\n"
"s_add_u32 s57, s57, 524288\n"
"ds_read_b64_tr_b16 a[106:107], v234 offset:1024\n"
"ds_read_b64_tr_b16 a[108:109], v234 offset:8192\n"
"v_mfma_f32_16x16x32_bf16 v[152:155], a[48:51], a[184:187], 0\n"
"s_cmp_eq_u32 s57, 5242880\n"
"s_cselect_b32 s57, 0, s57\n"
"ds_read_b64_tr_b16 a[110:111], v234 offset:9216\n"
"v_mfma_f32_16x16x32_bf16 v[152:155], a[52:55], a[188:191], v[152:155]\n"
"s_waitcnt lgkmcnt(8)\n"
"ds_read_b64_tr_b16 a[112:113], v235 offset:0\n"
"v_mfma_f32_16x16x32_bf16 v[156:159], a[48:51], a[192:195], 0\n"
"s_add_u32 m0, s58, 6144\n"
"ds_read_b64_tr_b16 a[114:115], v235 offset:1024\n"
"s_sleep 1\n"
"v_mfma_f32_16x16x32_bf16 v[156:159], a[52:55], a[196:199], v[156:159]\n"
"buffer_load_dwordx4 v224, s[28:31], s56 offen offset:0  lds\n"
"v_mfma_f32_16x16x32_bf16 v[160:163], a[48:51], a[200:203], 0\n"
"ds_read_b64_tr_b16 a[116:117], v235 offset:8192\n"
"v_mfma_f32_16x16x32_bf16 v[160:163], a[52:55], a[204:207], v[160:163]\n"
"ds_read_b64_tr_b16 a[118:119], v235 offset:9216\n"
"v_mfma_f32_16x16x32_bf16 v[164:167], a[56:59], a[184:187], 0\n"
"ds_read_b64_tr_b16 a[120:121], v236 offset:0\n"
"v_mfma_f32_16x16x32_bf16 v[164:167], a[60:63], a[188:191], v[164:167]\n"
"ds_read_b64_tr_b16 a[122:123], v236 offset:1024\n"
"v_mfma_f32_16x16x32_bf16 v[168:171], a[56:59], a[192:195], 0\n"
"buffer_load_dwordx4 v225, s[28:31], s56 offen offset:1024  lds\n"
"v_mfma_f32_16x16x32_bf16 v[168:171], a[60:63], a[196:199], v[168:171]\n"
"ds_read_b64_tr_b16 a[124:125], v236 offset:8192\n"
"v_mfma_f32_16x16x32_bf16 v[172:175], a[56:59], a[200:203], 0\n"
"ds_read_b64_tr_b16 a[126:127], v236 offset:9216\n"
"v_mfma_f32_16x16x32_bf16 v[172:175], a[60:63], a[204:207], v[172:175]\n"
"ds_read_b64_tr_b16 a[128:129], v233 offset:512\n"
"v_mfma_f32_16x16x32_bf16 v[176:179], a[64:67], a[184:187], 0\n"
"ds_read_b64_tr_b16 a[130:131], v233 offset:1536\n"
"s_sleep 1\n"
"v_mfma_f32_16x16x32_bf16 v[176:179], a[68:71], a[188:191], v[176:179]\n"
"buffer_load_dwordx4 v226, s[28:31], s56 offen offset:2048  lds\n"
"v_mfma_f32_16x16x32_bf16 v[180:183], a[64:67], a[192:195], 0\n"
"s_add_u32 s56, s56, 30720\n"
"ds_read_b64_tr_b16 a[132:133], v233 offset:8704\n"
"v_mfma_f32_16x16x32_bf16 v[180:183], a[68:71], a[196:199], v[180:183]\n"
"ds_read_b64_tr_b16 a[134:135], v233 offset:9728\n"
"v_mfma_f32_16x16x32_bf16 v[184:187], a[64:67], a[200:203], 0\n"
"ds_read_b64_tr_b16 a[136:137], v234 offset:512\n"
"v_mfma_f32_16x16x32_bf16 v[184:187], a[68:71], a[204:207], v[184:187]\n"
"s_add_u32 m0, s58, 9216\n"
"ds_read_b64_tr_b16 a[138:139], v234 offset:1536\n"
"v_mfma_f32_16x16x32_bf16 v[188:191], a[72:75], a[184:187], 0\n"
"buffer_load_dwordx4 v224, s[28:31], s56 offen offset:0  lds\n"
"v_mfma_f32_16x16x32_bf16 v[188:191], a[76:79], a[188:191], v[188:191]\n"
"ds_read_b64_tr_b16 a[140:141], v234 offset:8704\n"
"v_mfma_f32_16x16x32_bf16 v[192:195], a[72:75], a[192:195], 0\n"
"ds_read_b64_tr_b16 a[142:143], v234 offset:9728\n"
"v_mfma_f32_16x16x32_bf16 v[192:195], a[76:79], a[196:199], v[192:195]\n"
"ds_read_b64_tr_b16 a[144:145], v235 offset:512\n"
"s_sleep 1\n"
"v_mfma_f32_16x16x32_bf16 v[196:199], a[72:75], a[200:203], 0\n"
"ds_read_b64_tr_b16 a[146:147], v235 offset:1536\n"
"v_mfma_f32_16x16x32_bf16 v[196:199], a[76:79], a[204:207], v[196:199]\n"
"buffer_load_dwordx4 v225, s[28:31], s56 offen offset:1024  lds\n"
"v_mfma_f32_16x16x32_bf16 v[200:203], a[80:83], a[184:187], 0\n"
"ds_read_b64_tr_b16 a[148:149], v235 offset:8704\n"
"v_mfma_f32_16x16x32_bf16 v[200:203], a[84:87], a[188:191], v[200:203]\n"
"ds_read_b64_tr_b16 a[150:151], v235 offset:9728\n"
"v_mfma_f32_16x16x32_bf16 v[204:207], a[80:83], a[192:195], 0\n"
"ds_read_b64_tr_b16 a[152:153], v236 offset:512\n"
"v_mfma_f32_16x16x32_bf16 v[204:207], a[84:87], a[196:199], v[204:207]\n"
"ds_read_b64_tr_b16 a[154:155], v236 offset:1536\n"
"v_mfma_f32_16x16x32_bf16 v[208:211], a[80:83], a[200:203], 0\n"
"buffer_load_dwordx4 v226, s[28:31], s56 offen offset:2048  lds\n"
"v_mfma_f32_16x16x32_bf16 v[208:211], a[84:87], a[204:207], v[208:211]\n"
"s_sub_u32 s56, s56, 30592\n"
"ds_read_b64_tr_b16 a[156:157], v236 offset:8704\n"
"v_mfma_f32_16x16x32_bf16 v[212:215], a[88:91], a[184:187], 0\n"
"s_cmp_eq_u32 s56, 1280\n"
"s_cselect_b32 s56, 0, s56\n"
"ds_read_b64_tr_b16 a[158:159], v236 offset:9728\n"
"v_mfma_f32_16x16x32_bf16 v[212:215], a[92:95], a[188:191], v[212:215]\n"
"s_sleep 1\n"
"v_mfma_f32_16x16x32_bf16 v[216:219], a[88:91], a[192:195], 0\n"
"s_add_u32 m0, s46, 4096\n"
"v_mfma_f32_16x16x32_bf16 v[216:219], a[92:95], a[196:199], v[216:219]\n"
"buffer_load_dwordx4 v252, s[20:23], s44 offen nt lds\n"
"v_mfma_f32_16x16x32_bf16 v[220:223], a[88:91], a[200:203], 0\n"
"s_add_u32 s44, s44, 4096\n"
"v_mfma_f32_16x16x32_bf16 v[220:223], a[92:95], a[204:207], v[220:223]\n"
"s_waitcnt vmcnt(12) lgkmcnt(0)\n"
"s_barrier\n"
"v_mfma_f32_16x16x32_bf16 v[128:131], a[96:99], a[160:163], v[128:131]\n"
"s_add_u32 m0, s59, 0\n"
"ds_read_b128 a[184:187], v231 offset:0\n"
"ds_read_b128 a[188:191], v232 offset:0\n"
"v_mfma_f32_16x16x32_bf16 v[128:131], a[100:103], a[164:167], v[128:131]\n"
"buffer_load_dwordx4 v227, s[36:39], s57 offen offset:0  lds\n"
"v_mfma_f32_16x16x32_bf16 v[132:135], a[96:99], a[168:171], v[132:135]\n"
"ds_read_b128 a[192:195], v231 offset:2048\n"
"ds_read_b128 a[196:199], v232 offset:2048\n"
"v_mfma_f32_16x16x32_bf16 v[132:135], a[100:103], a[172:175], v[132:135]\n"
"ds_read_b128 a[200:203], v231 offset:4096\n"
"v_mfma_f32_16x16x32_bf16 v[136:139], a[96:99], a[176:179], v[136:139]\n"
"buffer_load_dwordx4 v228, s[36:39], s57 offen offset:1024  lds\n"
"s_sleep 1\n"
"v_mfma_f32_16x16x32_bf16 v[136:139], a[100:103], a[180:183], v[136:139]\n"
"ds_read_b128 a[204:207], v232 offset:4096\n"
"ds_read_b64_tr_b16 a[32:33], v233 offset:16384\n"
"v_mfma_f32_16x16x32_bf16 v[140:143], a[104:107], a[160:163], v[140:143]\n"
"ds_read_b64_tr_b16 a[34:35], v233 offset:17408\n"
"v_mfma_f32_16x16x32_bf16 v[140:143], a[108:111], a[164:167], v[140:143]\n"
"buffer_load_dwordx4 v229, s[36:39], s57 offen offset:2048  lds\n"
"v_mfma_f32_16x16x32_bf16 v[144:147], a[104:107], a[168:171], v[144:147]\n"
"ds_read_b64_tr_b16 a[36:37], v233 offset:24576\n"
"ds_read_b64_tr_b16 a[38:39], v233 offset:25600\n"
"v_mfma_f32_16x16x32_bf16 v[144:147], a[108:111], a[172:175], v[144:147]\n"
"ds_read_b64_tr_b16 a[40:41], v234 offset:16384\n"
"v_mfma_f32_16x16x32_bf16 v[148:151], a[104:107], a[176:179], v[148:151]\n"
"buffer_load_dwordx4 v230, s[36:39], s57 offen offset:3072  lds\n"
"v_mfma_f32_16x16x32_bf16 v[148:151], a[108:111], a[180:183], v[148:151]\n"
"s_add_u32 s57, s57, 524288\n"
"ds_read_b64_tr_b16 a[42:43], v234 offset:17408\n"
"ds_read_b64_tr_b16 a[44:45], v234 offset:24576\n"
"v_mfma_f32_16x16x32_bf16 v[152:155], a[112:115], a[160:163], v[152:155]\n"
"s_cmp_eq_u32 s57, 5242880\n"
"s_cselect_b32 s57, 0, s57\n"
"ds_read_b64_tr_b16 a[46:47], v234 offset:25600\n"
"v_mfma_f32_16x16x32_bf16 v[152:155], a[116:119], a[164:167], v[152:155]\n"
"s_waitcnt lgkmcnt(8)\n"
"ds_read_b64_tr_b16 a[48:49], v235 offset:16384\n"
"v_mfma_f32_16x16x32_bf16 v[156:159], a[112:115], a[168:171], v[156:159]\n"
"s_add_u32 m0, s58, 0\n"
"ds_read_b64_tr_b16 a[50:51], v235 offset:17408\n"
"s_sleep 1\n"
"v_mfma_f32_16x16x32_bf16 v[156:159], a[116:119], a[172:175], v[156:159]\n"
"buffer_load_dwordx4 v224, s[28:31], s56 offen offset:0  lds\n"
"v_mfma_f32_16x16x32_bf16 v[160:163], a[112:115], a[176:179], v[160:163]\n"
"ds_read_b64_tr_b16 a[52:53], v235 offset:24576\n"
"v_mfma_f32_16x16x32_bf16 v[160:163], a[116:119], a[180:183], v[160:163]\n"
"ds_read_b64_tr_b16 a[54:55], v235 offset:25600\n"
"v_mfma_f32_16x16x32_bf16 v[164:167], a[120:123], a[160:163], v[164:167]\n"
"ds_read_b64_tr_b16 a[56:57], v236 offset:16384\n"
"v_mfma_f32_16x16x32_bf16 v[164:167], a[124:127], a[164:167], v[164:167]\n"
"ds_read_b64_tr_b16 a[58:59], v236 offset:17408\n"
"v_mfma_f32_16x16x32_bf16 v[168:171], a[120:123], a[168:171], v[168:171]\n"
"buffer_load_dwordx4 v225, s[28:31], s56 offen offset:1024  lds\n"
"v_mfma_f32_16x16x32_bf16 v[168:171], a[124:127], a[172:175], v[168:171]\n"
"ds_read_b64_tr_b16 a[60:61], v236 offset:24576\n"
"v_mfma_f32_16x16x32_bf16 v[172:175], a[120:123], a[176:179], v[172:175]\n"
"ds_read_b64_tr_b16 a[62:63], v236 offset:25600\n"
"v_mfma_f32_16x16x32_bf16 v[172:175], a[124:127], a[180:183], v[172:175]\n"
"ds_read_b64_tr_b16 a[64:65], v233 offset:16896\n"
"v_mfma_f32_16x16x32_bf16 v[176:179], a[128:131], a[160:163], v[176:179]\n"
"ds_read_b64_tr_b16 a[66:67], v233 offset:17920\n"
"s_sleep 1\n"
"v_mfma_f32_16x16x32_bf16 v[176:179], a[132:135], a[164:167], v[176:179]\n"
"buffer_load_dwordx4 v226, s[28:31], s56 offen offset:2048  lds\n"
"v_mfma_f32_16x16x32_bf16 v[180:183], a[128:131], a[168:171], v[180:183]\n"
"s_add_u32 s56, s56, 30720\n"
"ds_read_b64_tr_b16 a[68:69], v233 offset:25088\n"
"v_mfma_f32_16x16x32_bf16 v[180:183], a[132:135], a[172:175], v[180:183]\n"
"ds_read_b64_tr_b16 a[70:71], v233 offset:26112\n"
"v_mfma_f32_16x16x32_bf16 v[184:187], a[128:131], a[176:179], v[184:187]\n"
"ds_read_b64_tr_b16 a[72:73], v234 offset:16896\n"
"v_mfma_f32_16x16x32_bf16 v[184:187], a[132:135], a[180:183], v[184:187]\n"
"s_add_u32 m0, s58, 3072\n"
"ds_read_b64_tr_b16 a[74:75], v234 offset:17920\n"
"v_mfma_f32_16x16x32_bf16 v[188:191], a[136:139], a[160:163], v[188:191]\n"
"buffer_load_dwordx4 v224, s[28:31], s56 offen offset:0  lds\n"
"v_mfma_f32_16x16x32_bf16 v[188:191], a[140:143], a[164:167], v[188:191]\n"
"ds_read_b64_tr_b16 a[76:77], v234 offset:25088\n"
"v_mfma_f32_16x16x32_bf16 v[192:195], a[136:139], a[168:171], v[192:195]\n"
"ds_read_b64_tr_b16 a[78:79], v234 offset:26112\n"
"v_mfma_f32_16x16x32_bf16 v[192:195], a[140:143], a[172:175], v[192:195]\n"
"ds_read_b64_tr_b16 a[80:81], v235 offset:16896\n"
"s_sleep 1\n"
"v_mfma_f32_16x16x32_bf16 v[196:199], a[136:139], a[176:179], v[196:199]\n"
"ds_read_b64_tr_b16 a[82:83], v235 offset:17920\n"
"v_mfma_f32_16x16x32_bf16 v[196:199], a[140:143], a[180:183], v[196:199]\n"
"buffer_load_dwordx4 v225, s[28:31], s56 offen offset:1024  lds\n"
"v_mfma_f32_16x16x32_bf16 v[200:203], a[144:147], a[160:163], v[200:203]\n"
"ds_read_b64_tr_b16 a[84:85], v235 offset:25088\n"
"v_mfma_f32_16x16x32_bf16 v[200:203], a[148:151], a[164:167], v[200:203]\n"
"ds_read_b64_tr_b16 a[86:87], v235 offset:26112\n"
"v_mfma_f32_16x16x32_bf16 v[204:207], a[144:147], a[168:171], v[204:207]\n"
"ds_read_b64_tr_b16 a[88:89], v236 offset:16896\n"
"v_mfma_f32_16x16x32_bf16 v[204:207], a[148:151], a[172:175], v[204:207]\n"
"ds_read_b64_tr_b16 a[90:91], v236 offset:17920\n"
"v_mfma_f32_16x16x32_bf16 v[208:211], a[144:147], a[176:179], v[208:211]\n"
"buffer_load_dwordx4 v226, s[28:31], s56 offen offset:2048  lds\n"
"v_mfma_f32_16x16x32_bf16 v[208:211], a[148:151], a[180:183], v[208:211]\n"
"s_sub_u32 s56, s56, 30592\n"
"ds_read_b64_tr_b16 a[92:93], v236 offset:25088\n"
"v_mfma_f32_16x16x32_bf16 v[212:215], a[152:155], a[160:163], v[212:215]\n"
"s_cmp_eq_u32 s56, 1280\n"
"s_cselect_b32 s56, 0, s56\n"
"ds_read_b64_tr_b16 a[94:95], v236 offset:26112\n"
"v_mfma_f32_16x16x32_bf16 v[212:215], a[156:159], a[164:167], v[212:215]\n"
"s_sleep 1\n"
"v_mfma_f32_16x16x32_bf16 v[216:219], a[152:155], a[168:171], v[216:219]\n"
"s_add_u32 m0, s46, 8192\n"
"v_mfma_f32_16x16x32_bf16 v[216:219], a[156:159], a[172:175], v[216:219]\n"
"buffer_load_dwordx4 v252, s[20:23], s44 offen nt lds\n"
"v_mfma_f32_16x16x32_bf16 v[220:223], a[152:155], a[176:179], v[220:223]\n"
"s_add_u32 s44, s44, 4096\n"
"v_mfma_f32_16x16x32_bf16 v[220:223], a[156:159], a[180:183], v[220:223]\n"
"s_waitcnt vmcnt(12) lgkmcnt(0)\n"
"s_barrier\n"
"v_mfma_f32_16x16x32_bf16 v[128:131], a[32:35], a[184:187], v[128:131]\n"
"s_add_u32 m0, s59, 16384\n"
"ds_read_b128 a[160:163], v231 offset:6144\n"
"ds_read_b128 a[164:167], v232 offset:6144\n"
"v_mfma_f32_16x16x32_bf16 v[128:131], a[36:39], a[188:191], v[128:131]\n"
"buffer_load_dwordx4 v227, s[36:39], s57 offen offset:0  lds\n"
"v_mfma_f32_16x16x32_bf16 v[132:135], a[32:35], a[192:195], v[132:135]\n"
"ds_read_b128 a[168:171], v231 offset:8192\n"
"ds_read_b128 a[172:175], v232 offset:8192\n"
"v_mfma_f32_16x16x32_bf16 v[132:135], a[36:39], a[196:199], v[132:135]\n"
"ds_read_b128 a[176:179], v231 offset:10240\n"
"v_mfma_f32_16x16x32_bf16 v[136:139], a[32:35], a[200:203], v[136:139]\n"
"buffer_load_dwordx4 v228, s[36:39], s57 offen offset:1024  lds\n"
"v_mfma_f32_16x16x32_bf16 v[136:139], a[36:39], a[204:207], v[136:139]\n"
"ds_read_b128 a[180:183], v232 offset:10240\n"
"ds_read_b64_tr_b16 a[96:97], v233 offset:32768\n"
"v_mfma_f32_16x16x32_bf16 v[140:143], a[40:43], a[184:187], v[140:143]\n"
"ds_read_b64_tr_b16 a[98:99], v233 offset:33792\n"
"s_sleep 1\n"
"v_mfma_f32_16x16x32_bf16 v[140:143], a[44:47], a[188:191], v[140:143]\n"
"buffer_load_dwordx4 v229, s[36:39], s57 offen offset:2048  lds\n"
"v_mfma_f32_16x16x32_bf16 v[144:147], a[40:43], a[192:195], v[144:147]\n"
"ds_read_b64_tr_b16 a[100:101], v233 offset:40960\n"
"ds_read_b64_tr_b16 a[102:103], v233 offset:41984\n"
"v_mfma_f32_16x16x32_bf16 v[144:147], a[44:47], a[196:199], v[144:147]\n"
"ds_read_b64_tr_b16 a[104:105], v234 offset:32768\n"
"v_mfma_f32_16x16x32_bf16 v[148:151], a[40:43], a[200:203], v[148:151]\n"
"buffer_load_dwordx4 v230, s[36:39], s57 offen offset:3072  lds\n"
"v_mfma_f32_16x16x32_bf16 v[148:151], a[44:47], a[204:207], v[148:151]\n"
"s_add_u32 s57, s57, 524288\n"
"ds_read_b64_tr_b16 a[106:107], v234 offset:33792\n"
"ds_read_b64_tr_b16 a[108:109], v234 offset:40960\n"
"v_mfma_f32_16x16x32_bf16 v[152:155], a[48:51], a[184:187], v[152:155]\n"
"s_cmp_eq_u32 s57, 5242880\n"
"s_cselect_b32 s57, 0, s57\n"
"ds_read_b64_tr_b16 a[110:111], v234 offset:41984\n"
"v_mfma_f32_16x16x32_bf16 v[152:155], a[52:55], a[188:191], v[152:155]\n"
"s_waitcnt lgkmcnt(8)\n"
"ds_read_b64_tr_b16 a[112:113], v235 offset:32768\n"
"v_mfma_f32_16x16x32_bf16 v[156:159], a[48:51], a[192:195], v[156:159]\n"
"s_add_u32 m0, s58, 6144\n"
"ds_read_b64_tr_b16 a[114:115], v235 offset:33792\n"
"v_mfma_f32_16x16x32_bf16 v[156:159], a[52:55], a[196:199], v[156:159]\n"
"buffer_load_dwordx4 v224, s[28:31], s56 offen offset:0  lds\n"
"v_mfma_f32_16x16x32_bf16 v[160:163], a[48:51], a[200:203], v[160:163]\n"
"ds_read_b64_tr_b16 a[116:117], v235 offset:40960\n"
"v_mfma_f32_16x16x32_bf16 v[160:163], a[52:55], a[204:207], v[160:163]\n"
"ds_read_b64_tr_b16 a[118:119], v235 offset:41984\n"
"v_mfma_f32_16x16x32_bf16 v[164:167], a[56:59], a[184:187], v[164:167]\n"
"ds_read_b64_tr_b16 a[120:121], v236 offset:32768\n"
"s_sleep 1\n"
"v_mfma_f32_16x16x32_bf16 v[164:167], a[60:63], a[188:191], v[164:167]\n"
"ds_read_b64_tr_b16 a[122:123], v236 offset:33792\n"
"v_mfma_f32_16x16x32_bf16 v[168:171], a[56:59], a[192:195], v[168:171]\n"
"buffer_load_dwordx4 v225, s[28:31], s56 offen offset:1024  lds\n"
"v_mfma_f32_16x16x32_bf16 v[168:171], a[60:63], a[196:199], v[168:171]\n"
"ds_read_b64_tr_b16 a[124:125], v236 offset:40960\n"
"v_mfma_f32_16x16x32_bf16 v[172:175], a[56:59], a[200:203], v[172:175]\n"
"ds_read_b64_tr_b16 a[126:127], v236 offset:41984\n"
"v_mfma_f32_16x16x32_bf16 v[172:175], a[60:63], a[204:207], v[172:175]\n"
"ds_read_b64_tr_b16 a[128:129], v233 offset:33280\n"
"v_mfma_f32_16x16x32_bf16 v[176:179], a[64:67], a[184:187], v[176:179]\n"
"ds_read_b64_tr_b16 a[130:131], v233 offset:34304\n"
"v_mfma_f32_16x16x32_bf16 v[176:179], a[68:71], a[188:191], v[176:179]\n"
"buffer_load_dwordx4 v226, s[28:31], s56 offen offset:2048  lds\n"
"v_mfma_f32_16x16x32_bf16 v[180:183], a[64:67], a[192:195], v[180:183]\n"
"s_add_u32 s56, s56, 30720\n"
"ds_read_b64_tr_b16 a[132:133], v233 offset:41472\n"
"v_mfma_f32_16x16x32_bf16 v[180:183], a[68:71], a[196:199], v[180:183]\n"
"ds_read_b64_tr_b16 a[134:135], v233 offset:42496\n"
"v_mfma_f32_16x16x32_bf16 v[184:187], a[64:67], a[200:203], v[184:187]\n"
"ds_read_b64_tr_b16 a[136:137], v234 offset:33280\n"
"v_mfma_f32_16x16x32_bf16 v[184:187], a[68:71], a[204:207], v[184:187]\n"
"s_add_u32 m0, s58, 9216\n"
"ds_read_b64_tr_b16 a[138:139], v234 offset:34304\n"
"v_mfma_f32_16x16x32_bf16 v[188:191], a[72:75], a[184:187], v[188:191]\n"
"buffer_load_dwordx4 v224, s[28:31], s56 offen offset:0  lds\n"
"s_sleep 1\n"
"v_mfma_f32_16x16x32_bf16 v[188:191], a[76:79], a[188:191], v[188:191]\n"
"ds_read_b64_tr_b16 a[140:141], v234 offset:41472\n"
"v_mfma_f32_16x16x32_bf16 v[192:195], a[72:75], a[192:195], v[192:195]\n"
"ds_read_b64_tr_b16 a[142:143], v234 offset:42496\n"
"v_mfma_f32_16x16x32_bf16 v[192:195], a[76:79], a[196:199], v[192:195]\n"
"ds_read_b64_tr_b16 a[144:145], v235 offset:33280\n"
"v_mfma_f32_16x16x32_bf16 v[196:199], a[72:75], a[200:203], v[196:199]\n"
"ds_read_b64_tr_b16 a[146:147], v235 offset:34304\n"
"v_mfma_f32_16x16x32_bf16 v[196:199], a[76:79], a[204:207], v[196:199]\n"
"buffer_load_dwordx4 v225, s[28:31], s56 offen offset:1024  lds\n"
"v_mfma_f32_16x16x32_bf16 v[200:203], a[80:83], a[184:187], v[200:203]\n"
"ds_read_b64_tr_b16 a[148:149], v235 offset:41472\n"
"v_mfma_f32_16x16x32_bf16 v[200:203], a[84:87], a[188:191], v[200:203]\n"
"ds_read_b64_tr_b16 a[150:151], v235 offset:42496\n"
"v_mfma_f32_16x16x32_bf16 v[204:207], a[80:83], a[192:195], v[204:207]\n"
"ds_read_b64_tr_b16 a[152:153], v236 offset:33280\n"
"v_mfma_f32_16x16x32_bf16 v[204:207], a[84:87], a[196:199], v[204:207]\n"
"ds_read_b64_tr_b16 a[154:155], v236 offset:34304\n"
"v_mfma_f32_16x16x32_bf16 v[208:211], a[80:83], a[200:203], v[208:211]\n"
"buffer_load_dwordx4 v226, s[28:31], s56 offen offset:2048  lds\n"
"v_mfma_f32_16x16x32_bf16 v[208:211], a[84:87], a[204:207], v[208:211]\n"
"s_sub_u32 s56, s56, 30592\n"
"ds_read_b64_tr_b16 a[156:157], v236 offset:41472\n"
"v_mfma_f32_16x16x32_bf16 v[212:215], a[88:91], a[184:187], v[212:215]\n"
"s_cmp_eq_u32 s56, 1280\n"
"s_cselect_b32 s56, 0, s56\n"
"ds_read_b64_tr_b16 a[158:159], v236 offset:42496\n"
"s_sleep 1\n"
"v_mfma_f32_16x16x32_bf16 v[212:215], a[92:95], a[188:191], v[212:215]\n"
"v_mfma_f32_16x16x32_bf16 v[216:219], a[88:91], a[192:195], v[216:219]\n"
"s_add_u32 m0, s46, 12288\n"
"v_mfma_f32_16x16x32_bf16 v[216:219], a[92:95], a[196:199], v[216:219]\n"
"buffer_load_dwordx4 v252, s[20:23], s44 offen nt lds\n"
"v_mfma_f32_16x16x32_bf16 v[220:223], a[88:91], a[200:203], v[220:223]\n"
"s_add_u32 s44, s44, 4096\n"
"v_mfma_f32_16x16x32_bf16 v[220:223], a[92:95], a[204:207], v[220:223]\n"
"s_waitcnt vmcnt(12) lgkmcnt(0)\n"
"s_barrier\n"
"v_mfma_f32_16x16x32_bf16 v[128:131], a[96:99], a[160:163], v[128:131]\n"
"s_add_u32 m0, s59, 32768\n"
"ds_read_b128 a[184:187], v231 offset:0\n"
"ds_read_b128 a[188:191], v232 offset:0\n"
"v_mfma_f32_16x16x32_bf16 v[128:131], a[100:103], a[164:167], v[128:131]\n"
"buffer_load_dwordx4 v227, s[36:39], s57 offen offset:0  lds\n"
"v_mfma_f32_16x16x32_bf16 v[132:135], a[96:99], a[168:171], v[132:135]\n"
"ds_read_b128 a[192:195], v231 offset:2048\n"
"ds_read_b128 a[196:199], v232 offset:2048\n"
"v_mfma_f32_16x16x32_bf16 v[132:135], a[100:103], a[172:175], v[132:135]\n"
"ds_read_b128 a[200:203], v231 offset:4096\n"
"v_mfma_f32_16x16x32_bf16 v[136:139], a[96:99], a[176:179], v[136:139]\n"
"buffer_load_dwordx4 v228, s[36:39], s57 offen offset:1024  lds\n"
"v_mfma_f32_16x16x32_bf16 v[136:139], a[100:103], a[180:183], v[136:139]\n"
"ds_read_b128 a[204:207], v232 offset:4096\n"
"ds_read_b64_tr_b16 a[32:33], v233 offset:0\n"
"v_mfma_f32_16x16x32_bf16 v[140:143], a[104:107], a[160:163], v[140:143]\n"
"ds_read_b64_tr_b16 a[34:35], v233 offset:1024\n"
"v_mfma_f32_16x16x32_bf16 v[140:143], a[108:111], a[164:167], v[140:143]\n"
"buffer_load_dwordx4 v229, s[36:39], s57 offen offset:2048  lds\n"
"v_mfma_f32_16x16x32_bf16 v[144:147], a[104:107], a[168:171], v[144:147]\n"
"ds_read_b64_tr_b16 a[36:37], v233 offset:8192\n"
"ds_read_b64_tr_b16 a[38:39], v233 offset:9216\n"
"s_sleep 1\n"
"v_mfma_f32_16x16x32_bf16 v[144:147], a[108:111], a[172:175], v[144:147]\n"
"ds_read_b64_tr_b16 a[40:41], v234 offset:0\n"
"v_mfma_f32_16x16x32_bf16 v[148:151], a[104:107], a[176:179], v[148:151]\n"
"buffer_load_dwordx4 v230, s[36:39], s57 offen offset:3072  lds\n"
"v_mfma_f32_16x16x32_bf16 v[148:151], a[108:111], a[180:183], v[148:151]\n"
"s_add_u32 s57, s57, 524288\n"
"ds_read_b64_tr_b16 a[42:43], v234 offset:1024\n"
"ds_read_b64_tr_b16 a[44:45], v234 offset:8192\n"
"v_mfma_f32_16x16x32_bf16 v[152:155], a[112:115], a[160:163], v[152:155]\n"
"s_cmp_eq_u32 s57, 5242880\n"
"s_cselect_b32 s57, 0, s57\n"
"ds_read_b64_tr_b16 a[46:47], v234 offset:9216\n"
"v_mfma_f32_16x16x32_bf16 v[152:155], a[116:119], a[164:167], v[152:155]\n"
"s_waitcnt lgkmcnt(8)\n"
"ds_read_b64_tr_b16 a[48:49], v235 offset:0\n"
"v_mfma_f32_16x16x32_bf16 v[156:159], a[112:115], a[168:171], v[156:159]\n"
"s_add_u32 m0, s58, 0\n"
"ds_read_b64_tr_b16 a[50:51], v235 offset:1024\n"
"v_mfma_f32_16x16x32_bf16 v[156:159], a[116:119], a[172:175], v[156:159]\n"
"buffer_load_dwordx4 v224, s[28:31], s56 offen offset:0  lds\n"
"v_mfma_f32_16x16x32_bf16 v[160:163], a[112:115], a[176:179], v[160:163]\n"
"ds_read_b64_tr_b16 a[52:53], v235 offset:8192\n"
"v_mfma_f32_16x16x32_bf16 v[160:163], a[116:119], a[180:183], v[160:163]\n"
"ds_read_b64_tr_b16 a[54:55], v235 offset:9216\n"
"v_mfma_f32_16x16x32_bf16 v[164:167], a[120:123], a[160:163], v[164:167]\n"
"ds_read_b64_tr_b16 a[56:57], v236 offset:0\n"
"v_mfma_f32_16x16x32_bf16 v[164:167], a[124:127], a[164:167], v[164:167]\n"
"ds_read_b64_tr_b16 a[58:59], v236 offset:1024\n"
"v_mfma_f32_16x16x32_bf16 v[168:171], a[120:123], a[168:171], v[168:171]\n"
"buffer_load_dwordx4 v225, s[28:31], s56 offen offset:1024  lds\n"
"v_mfma_f32_16x16x32_bf16 v[168:171], a[124:127], a[172:175], v[168:171]\n"
"ds_read_b64_tr_b16 a[60:61], v236 offset:8192\n"
"v_mfma_f32_16x16x32_bf16 v[172:175], a[120:123], a[176:179], v[172:175]\n"
"ds_read_b64_tr_b16 a[62:63], v236 offset:9216\n"
"v_mfma_f32_16x16x32_bf16 v[172:175], a[124:127], a[180:183], v[172:175]\n"
"ds_read_b64_tr_b16 a[64:65], v233 offset:512\n"
"v_mfma_f32_16x16x32_bf16 v[176:179], a[128:131], a[160:163], v[176:179]\n"
"ds_read_b64_tr_b16 a[66:67], v233 offset:1536\n"
"s_sleep 1\n"
"v_mfma_f32_16x16x32_bf16 v[176:179], a[132:135], a[164:167], v[176:179]\n"
"buffer_load_dwordx4 v226, s[28:31], s56 offen offset:2048  lds\n"
"v_mfma_f32_16x16x32_bf16 v[180:183], a[128:131], a[168:171], v[180:183]\n"
"s_add_u32 s56, s56, 30720\n"
"ds_read_b64_tr_b16 a[68:69], v233 offset:8704\n"
"v_mfma_f32_16x16x32_bf16 v[180:183], a[132:135], a[172:175], v[180:183]\n"
"ds_read_b64_tr_b16 a[70:71], v233 offset:9728\n"
"v_mfma_f32_16x16x32_bf16 v[184:187], a[128:131], a[176:179], v[184:187]\n"
"ds_read_b64_tr_b16 a[72:73], v234 offset:512\n"
"v_mfma_f32_16x16x32_bf16 v[184:187], a[132:135], a[180:183], v[184:187]\n"
"s_add_u32 m0, s58, 3072\n"
"ds_read_b64_tr_b16 a[74:75], v234 offset:1536\n"
"v_mfma_f32_16x16x32_bf16 v[188:191], a[136:139], a[160:163], v[188:191]\n"
"buffer_load_dwordx4 v224, s[28:31], s56 offen offset:0  lds\n"
"v_mfma_f32_16x16x32_bf16 v[188:191], a[140:143], a[164:167], v[188:191]\n"
"ds_read_b64_tr_b16 a[76:77], v234 offset:8704\n"
"v_mfma_f32_16x16x32_bf16 v[192:195], a[136:139], a[168:171], v[192:195]\n"
"ds_read_b64_tr_b16 a[78:79], v234 offset:9728\n"
"v_mfma_f32_16x16x32_bf16 v[192:195], a[140:143], a[172:175], v[192:195]\n"
"ds_read_b64_tr_b16 a[80:81], v235 offset:512\n"
"v_mfma_f32_16x16x32_bf16 v[196:199], a[136:139], a[176:179], v[196:199]\n"
"ds_read_b64_tr_b16 a[82:83], v235 offset:1536\n"
"v_mfma_f32_16x16x32_bf16 v[196:199], a[140:143], a[180:183], v[196:199]\n"
"buffer_load_dwordx4 v225, s[28:31], s56 offen offset:1024  lds\n"
"v_mfma_f32_16x16x32_bf16 v[200:203], a[144:147], a[160:163], v[200:203]\n"
"ds_read_b64_tr_b16 a[84:85], v235 offset:8704\n"
"v_mfma_f32_16x16x32_bf16 v[200:203], a[148:151], a[164:167], v[200:203]\n"
"ds_read_b64_tr_b16 a[86:87], v235 offset:9728\n"
"v_mfma_f32_16x16x32_bf16 v[204:207], a[144:147], a[168:171], v[204:207]\n"
"ds_read_b64_tr_b16 a[88:89], v236 offset:512\n"
"v_mfma_f32_16x16x32_bf16 v[204:207], a[148:151], a[172:175], v[204:207]\n"
"ds_read_b64_tr_b16 a[90:91], v236 offset:1536\n"
"v_mfma_f32_16x16x32_bf16 v[208:211], a[144:147], a[176:179], v[208:211]\n"
"buffer_load_dwordx4 v226, s[28:31], s56 offen offset:2048  lds\n"
"s_sleep 1\n"
"v_mfma_f32_16x16x32_bf16 v[208:211], a[148:151], a[180:183], v[208:211]\n"
"s_sub_u32 s56, s56, 30592\n"
"ds_read_b64_tr_b16 a[92:93], v236 offset:8704\n"
"v_mfma_f32_16x16x32_bf16 v[212:215], a[152:155], a[160:163], v[212:215]\n"
"s_cmp_eq_u32 s56, 1280\n"
"s_cselect_b32 s56, 0, s56\n"
"ds_read_b64_tr_b16 a[94:95], v236 offset:9728\n"
"v_mfma_f32_16x16x32_bf16 v[212:215], a[156:159], a[164:167], v[212:215]\n"
"v_mfma_f32_16x16x32_bf16 v[216:219], a[152:155], a[168:171], v[216:219]\n"
"s_add_u32 m0, s46, 16384\n"
"v_mfma_f32_16x16x32_bf16 v[216:219], a[156:159], a[172:175], v[216:219]\n"
"buffer_load_dwordx4 v252, s[20:23], s44 offen nt lds\n"
"v_mfma_f32_16x16x32_bf16 v[220:223], a[152:155], a[176:179], v[220:223]\n"
"s_add_u32 s44, s44, 4096\n"
"v_mfma_f32_16x16x32_bf16 v[220:223], a[156:159], a[180:183], v[220:223]\n"
"s_waitcnt vmcnt(12) lgkmcnt(0)\n"
"s_barrier\n"
"v_mfma_f32_16x16x32_bf16 v[128:131], a[32:35], a[184:187], v[128:131]\n"
"s_add_u32 m0, s59, 0\n"
"ds_read_b128 a[160:163], v231 offset:6144\n"
"ds_read_b128 a[164:167], v232 offset:6144\n"
"v_mfma_f32_16x16x32_bf16 v[128:131], a[36:39], a[188:191], v[128:131]\n"
"buffer_load_dwordx4 v227, s[36:39], s57 offen offset:0  lds\n"
"v_mfma_f32_16x16x32_bf16 v[132:135], a[32:35], a[192:195], v[132:135]\n"
"ds_read_b128 a[168:171], v231 offset:8192\n"
"ds_read_b128 a[172:175], v232 offset:8192\n"
"v_mfma_f32_16x16x32_bf16 v[132:135], a[36:39], a[196:199], v[132:135]\n"
"ds_read_b128 a[176:179], v231 offset:10240\n"
"v_mfma_f32_16x16x32_bf16 v[136:139], a[32:35], a[200:203], v[136:139]\n"
"buffer_load_dwordx4 v228, s[36:39], s57 offen offset:1024  lds\n"
"v_mfma_f32_16x16x32_bf16 v[136:139], a[36:39], a[204:207], v[136:139]\n"
"ds_read_b128 a[180:183], v232 offset:10240\n"
"ds_read_b64_tr_b16 a[96:97], v233 offset:16384\n"
"v_mfma_f32_16x16x32_bf16 v[140:143], a[40:43], a[184:187], v[140:143]\n"
"ds_read_b64_tr_b16 a[98:99], v233 offset:17408\n"
"v_mfma_f32_16x16x32_bf16 v[140:143], a[44:47], a[188:191], v[140:143]\n"
"buffer_load_dwordx4 v229, s[36:39], s57 offen offset:2048  lds\n"
"v_mfma_f32_16x16x32_bf16 v[144:147], a[40:43], a[192:195], v[144:147]\n"
"ds_read_b64_tr_b16 a[100:101], v233 offset:24576\n"
"ds_read_b64_tr_b16 a[102:103], v233 offset:25600\n"
"v_mfma_f32_16x16x32_bf16 v[144:147], a[44:47], a[196:199], v[144:147]\n"
"ds_read_b64_tr_b16 a[104:105], v234 offset:16384\n"
"v_mfma_f32_16x16x32_bf16 v[148:151], a[40:43], a[200:203], v[148:151]\n"
"buffer_load_dwordx4 v230, s[36:39], s57 offen offset:3072  lds\n"
"v_mfma_f32_16x16x32_bf16 v[148:151], a[44:47], a[204:207], v[148:151]\n"
"s_add_u32 s57, s57, 524288\n"
"ds_read_b64_tr_b16 a[106:107], v234 offset:17408\n"
"ds_read_b64_tr_b16 a[108:109], v234 offset:24576\n"
"v_mfma_f32_16x16x32_bf16 v[152:155], a[48:51], a[184:187], v[152:155]\n"
"s_cmp_eq_u32 s57, 5242880\n"
"s_cselect_b32 s57, 0, s57\n"
"ds_read_b64_tr_b16 a[110:111], v234 offset:25600\n"
"s_sleep 1\n"
"v_mfma_f32_16x16x32_bf16 v[152:155], a[52:55], a[188:191], v[152:155]\n"
"s_waitcnt lgkmcnt(8)\n"
"ds_read_b64_tr_b16 a[112:113], v235 offset:16384\n"
"v_mfma_f32_16x16x32_bf16 v[156:159], a[48:51], a[192:195], v[156:159]\n"
"s_add_u32 m0, s58, 6144\n"
"ds_read_b64_tr_b16 a[114:115], v235 offset:17408\n"
"v_mfma_f32_16x16x32_bf16 v[156:159], a[52:55], a[196:199], v[156:159]\n"
"buffer_load_dwordx4 v224, s[28:31], s56 offen offset:0  lds\n"
"v_mfma_f32_16x16x32_bf16 v[160:163], a[48:51], a[200:203], v[160:163]\n"
"ds_read_b64_tr_b16 a[116:117], v235 offset:24576\n"
"v_mfma_f32_16x16x32_bf16 v[160:163], a[52:55], a[204:207], v[160:163]\n"
"ds_read_b64_tr_b16 a[118:119], v235 offset:25600\n"
"v_mfma_f32_16x16x32_bf16 v[164:167], a[56:59], a[184:187], v[164:167]\n"
"ds_read_b64_tr_b16 a[120:121], v236 offset:16384\n"
"v_mfma_f32_16x16x32_bf16 v[164:167], a[60:63], a[188:191], v[164:167]\n"
"ds_read_b64_tr_b16 a[122:123], v236 offset:17408\n"
"v_mfma_f32_16x16x32_bf16 v[168:171], a[56:59], a[192:195], v[168:171]\n"
"buffer_load_dwordx4 v225, s[28:31], s56 offen offset:1024  lds\n"
"v_mfma_f32_16x16x32_bf16 v[168:171], a[60:63], a[196:199], v[168:171]\n"
"ds_read_b64_tr_b16 a[124:125], v236 offset:24576\n"
"v_mfma_f32_16x16x32_bf16 v[172:175], a[56:59], a[200:203], v[172:175]\n"
"ds_read_b64_tr_b16 a[126:127], v236 offset:25600\n"
"v_mfma_f32_16x16x32_bf16 v[172:175], a[60:63], a[204:207], v[172:175]\n"
"ds_read_b64_tr_b16 a[128:129], v233 offset:16896\n"
"v_mfma_f32_16x16x32_bf16 v[176:179], a[64:67], a[184:187], v[176:179]\n"
"ds_read_b64_tr_b16 a[130:131], v233 offset:17920\n"
"v_mfma_f32_16x16x32_bf16 v[176:179], a[68:71], a[188:191], v[176:179]\n"
"buffer_load_dwordx4 v226, s[28:31], s56 offen offset:2048  lds\n"
"v_mfma_f32_16x16x32_bf16 v[180:183], a[64:67], a[192:195], v[180:183]\n"
"s_add_u32 s56, s56, 30720\n"
"ds_read_b64_tr_b16 a[132:133], v233 offset:25088\n"
"v_mfma_f32_16x16x32_bf16 v[180:183], a[68:71], a[196:199], v[180:183]\n"
"ds_read_b64_tr_b16 a[134:135], v233 offset:26112\n"
"v_mfma_f32_16x16x32_bf16 v[184:187], a[64:67], a[200:203], v[184:187]\n"
"ds_read_b64_tr_b16 a[136:137], v234 offset:16896\n"
"v_mfma_f32_16x16x32_bf16 v[184:187], a[68:71], a[204:207], v[184:187]\n"
"s_add_u32 m0, s58, 9216\n"
"ds_read_b64_tr_b16 a[138:139], v234 offset:17920\n"
"v_mfma_f32_16x16x32_bf16 v[188:191], a[72:75], a[184:187], v[188:191]\n"
"buffer_load_dwordx4 v224, s[28:31], s56 offen offset:0  lds\n"
"v_mfma_f32_16x16x32_bf16 v[188:191], a[76:79], a[188:191], v[188:191]\n"
"ds_read_b64_tr_b16 a[140:141], v234 offset:25088\n"
"v_mfma_f32_16x16x32_bf16 v[192:195], a[72:75], a[192:195], v[192:195]\n"
"ds_read_b64_tr_b16 a[142:143], v234 offset:26112\n"
"v_mfma_f32_16x16x32_bf16 v[192:195], a[76:79], a[196:199], v[192:195]\n"
"ds_read_b64_tr_b16 a[144:145], v235 offset:16896\n"
"v_mfma_f32_16x16x32_bf16 v[196:199], a[72:75], a[200:203], v[196:199]\n"
"ds_read_b64_tr_b16 a[146:147], v235 offset:17920\n"
"v_mfma_f32_16x16x32_bf16 v[196:199], a[76:79], a[204:207], v[196:199]\n"
"buffer_load_dwordx4 v225, s[28:31], s56 offen offset:1024  lds\n"
"v_mfma_f32_16x16x32_bf16 v[200:203], a[80:83], a[184:187], v[200:203]\n"
"ds_read_b64_tr_b16 a[148:149], v235 offset:25088\n"
"s_sleep 1\n"
"v_mfma_f32_16x16x32_bf16 v[200:203], a[84:87], a[188:191], v[200:203]\n"
"ds_read_b64_tr_b16 a[150:151], v235 offset:26112\n"
"v_mfma_f32_16x16x32_bf16 v[204:207], a[80:83], a[192:195], v[204:207]\n"
"ds_read_b64_tr_b16 a[152:153], v236 offset:16896\n"
"v_mfma_f32_16x16x32_bf16 v[204:207], a[84:87], a[196:199], v[204:207]\n"
"ds_read_b64_tr_b16 a[154:155], v236 offset:17920\n"
"v_mfma_f32_16x16x32_bf16 v[208:211], a[80:83], a[200:203], v[208:211]\n"
"buffer_load_dwordx4 v226, s[28:31], s56 offen offset:2048  lds\n"
"v_mfma_f32_16x16x32_bf16 v[208:211], a[84:87], a[204:207], v[208:211]\n"
"s_sub_u32 s56, s56, 30592\n"
"ds_read_b64_tr_b16 a[156:157], v236 offset:25088\n"
"v_mfma_f32_16x16x32_bf16 v[212:215], a[88:91], a[184:187], v[212:215]\n"
"s_cmp_eq_u32 s56, 1280\n"
"s_cselect_b32 s56, 0, s56\n"
"ds_read_b64_tr_b16 a[158:159], v236 offset:26112\n"
"v_mfma_f32_16x16x32_bf16 v[212:215], a[92:95], a[188:191], v[212:215]\n"
"v_mfma_f32_16x16x32_bf16 v[216:219], a[88:91], a[192:195], v[216:219]\n"
"s_add_u32 m0, s46, 20480\n"
"v_mfma_f32_16x16x32_bf16 v[216:219], a[92:95], a[196:199], v[216:219]\n"
"buffer_load_dwordx4 v252, s[20:23], s44 offen nt lds\n"
"v_mfma_f32_16x16x32_bf16 v[220:223], a[88:91], a[200:203], v[220:223]\n"
"s_add_u32 s44, s44, 4096\n"
"v_mfma_f32_16x16x32_bf16 v[220:223], a[92:95], a[204:207], v[220:223]\n"
"s_waitcnt vmcnt(12) lgkmcnt(0)\n"
"s_barrier\n"
"v_mfma_f32_16x16x32_bf16 v[128:131], a[96:99], a[160:163], v[128:131]\n"
"s_add_u32 m0, s59, 16384\n"
"ds_read_b128 a[184:187], v231 offset:0\n"
"ds_read_b128 a[188:191], v232 offset:0\n"
"v_mfma_f32_16x16x32_bf16 v[128:131], a[100:103], a[164:167], v[128:131]\n"
"buffer_load_dwordx4 v227, s[36:39], s57 offen offset:0  lds\n"
"v_mfma_f32_16x16x32_bf16 v[132:135], a[96:99], a[168:171], v[132:135]\n"
"ds_read_b128 a[192:195], v231 offset:2048\n"
"ds_read_b128 a[196:199], v232 offset:2048\n"
"v_mfma_f32_16x16x32_bf16 v[132:135], a[100:103], a[172:175], v[132:135]\n"
"ds_read_b128 a[200:203], v231 offset:4096\n"
"v_mfma_f32_16x16x32_bf16 v[136:139], a[96:99], a[176:179], v[136:139]\n"
"buffer_load_dwordx4 v228, s[36:39], s57 offen offset:1024  lds\n"
"v_mfma_f32_16x16x32_bf16 v[136:139], a[100:103], a[180:183], v[136:139]\n"
"ds_read_b128 a[204:207], v232 offset:4096\n"
"ds_read_b64_tr_b16 a[32:33], v233 offset:32768\n"
"v_mfma_f32_16x16x32_bf16 v[140:143], a[104:107], a[160:163], v[140:143]\n"
"ds_read_b64_tr_b16 a[34:35], v233 offset:33792\n"
"v_mfma_f32_16x16x32_bf16 v[140:143], a[108:111], a[164:167], v[140:143]\n"
"buffer_load_dwordx4 v229, s[36:39], s57 offen offset:2048  lds\n"
"v_mfma_f32_16x16x32_bf16 v[144:147], a[104:107], a[168:171], v[144:147]\n"
"ds_read_b64_tr_b16 a[36:37], v233 offset:40960\n"
"ds_read_b64_tr_b16 a[38:39], v233 offset:41984\n"
"v_mfma_f32_16x16x32_bf16 v[144:147], a[108:111], a[172:175], v[144:147]\n"
"ds_read_b64_tr_b16 a[40:41], v234 offset:32768\n"
"v_mfma_f32_16x16x32_bf16 v[148:151], a[104:107], a[176:179], v[148:151]\n"
"buffer_load_dwordx4 v230, s[36:39], s57 offen offset:3072  lds\n"
"v_mfma_f32_16x16x32_bf16 v[148:151], a[108:111], a[180:183], v[148:151]\n"
"s_add_u32 s57, s57, 524288\n"
"ds_read_b64_tr_b16 a[42:43], v234 offset:33792\n"
"ds_read_b64_tr_b16 a[44:45], v234 offset:40960\n"
"v_mfma_f32_16x16x32_bf16 v[152:155], a[112:115], a[160:163], v[152:155]\n"
"s_cmp_eq_u32 s57, 5242880\n"
"s_cselect_b32 s57, 0, s57\n"
"ds_read_b64_tr_b16 a[46:47], v234 offset:41984\n"
"v_mfma_f32_16x16x32_bf16 v[152:155], a[116:119], a[164:167], v[152:155]\n"
"s_waitcnt lgkmcnt(8)\n"
"ds_read_b64_tr_b16 a[48:49], v235 offset:32768\n"
"v_mfma_f32_16x16x32_bf16 v[156:159], a[112:115], a[168:171], v[156:159]\n"
"s_add_u32 m0, s58, 0\n"
"ds_read_b64_tr_b16 a[50:51], v235 offset:33792\n"
"v_mfma_f32_16x16x32_bf16 v[156:159], a[116:119], a[172:175], v[156:159]\n"
"buffer_load_dwordx4 v224, s[28:31], s56 offen offset:0  lds\n"
"v_mfma_f32_16x16x32_bf16 v[160:163], a[112:115], a[176:179], v[160:163]\n"
"ds_read_b64_tr_b16 a[52:53], v235 offset:40960\n"
"v_mfma_f32_16x16x32_bf16 v[160:163], a[116:119], a[180:183], v[160:163]\n"
"ds_read_b64_tr_b16 a[54:55], v235 offset:41984\n"
"v_mfma_f32_16x16x32_bf16 v[164:167], a[120:123], a[160:163], v[164:167]\n"
"ds_read_b64_tr_b16 a[56:57], v236 offset:32768\n"
"v_mfma_f32_16x16x32_bf16 v[164:167], a[124:127], a[164:167], v[164:167]\n"
"ds_read_b64_tr_b16 a[58:59], v236 offset:33792\n"
"v_mfma_f32_16x16x32_bf16 v[168:171], a[120:123], a[168:171], v[168:171]\n"
"buffer_load_dwordx4 v225, s[28:31], s56 offen offset:1024  lds\n"
"v_mfma_f32_16x16x32_bf16 v[168:171], a[124:127], a[172:175], v[168:171]\n"
"ds_read_b64_tr_b16 a[60:61], v236 offset:40960\n"
"v_mfma_f32_16x16x32_bf16 v[172:175], a[120:123], a[176:179], v[172:175]\n"
"ds_read_b64_tr_b16 a[62:63], v236 offset:41984\n"
"v_mfma_f32_16x16x32_bf16 v[172:175], a[124:127], a[180:183], v[172:175]\n"
"ds_read_b64_tr_b16 a[64:65], v233 offset:33280\n"
"v_mfma_f32_16x16x32_bf16 v[176:179], a[128:131], a[160:163], v[176:179]\n"
"ds_read_b64_tr_b16 a[66:67], v233 offset:34304\n"
"v_mfma_f32_16x16x32_bf16 v[176:179], a[132:135], a[164:167], v[176:179]\n"
"buffer_load_dwordx4 v226, s[28:31], s56 offen offset:2048  lds\n"
"v_mfma_f32_16x16x32_bf16 v[180:183], a[128:131], a[168:171], v[180:183]\n"
"s_add_u32 s56, s56, 30720\n"
"ds_read_b64_tr_b16 a[68:69], v233 offset:41472\n"
"v_mfma_f32_16x16x32_bf16 v[180:183], a[132:135], a[172:175], v[180:183]\n"
"ds_read_b64_tr_b16 a[70:71], v233 offset:42496\n"
"v_mfma_f32_16x16x32_bf16 v[184:187], a[128:131], a[176:179], v[184:187]\n"
"ds_read_b64_tr_b16 a[72:73], v234 offset:33280\n"
"v_mfma_f32_16x16x32_bf16 v[184:187], a[132:135], a[180:183], v[184:187]\n"
"s_add_u32 m0, s58, 3072\n"
"ds_read_b64_tr_b16 a[74:75], v234 offset:34304\n"
"v_mfma_f32_16x16x32_bf16 v[188:191], a[136:139], a[160:163], v[188:191]\n"
"buffer_load_dwordx4 v224, s[28:31], s56 offen offset:0  lds\n"
"v_mfma_f32_16x16x32_bf16 v[188:191], a[140:143], a[164:167], v[188:191]\n"
"ds_read_b64_tr_b16 a[76:77], v234 offset:41472\n"
"v_mfma_f32_16x16x32_bf16 v[192:195], a[136:139], a[168:171], v[192:195]\n"
"ds_read_b64_tr_b16 a[78:79], v234 offset:42496\n"
"v_mfma_f32_16x16x32_bf16 v[192:195], a[140:143], a[172:175], v[192:195]\n"
"ds_read_b64_tr_b16 a[80:81], v235 offset:33280\n"
"v_mfma_f32_16x16x32_bf16 v[196:199], a[136:139], a[176:179], v[196:199]\n"
"ds_read_b64_tr_b16 a[82:83], v235 offset:34304\n"
"v_mfma_f32_16x16x32_bf16 v[196:199], a[140:143], a[180:183], v[196:199]\n"
"buffer_load_dwordx4 v225, s[28:31], s56 offen offset:1024  lds\n"
"v_mfma_f32_16x16x32_bf16 v[200:203], a[144:147], a[160:163], v[200:203]\n"
"ds_read_b64_tr_b16 a[84:85], v235 offset:41472\n"
"v_mfma_f32_16x16x32_bf16 v[200:203], a[148:151], a[164:167], v[200:203]\n"
"ds_read_b64_tr_b16 a[86:87], v235 offset:42496\n"
"v_mfma_f32_16x16x32_bf16 v[204:207], a[144:147], a[168:171], v[204:207]\n"
"ds_read_b64_tr_b16 a[88:89], v236 offset:33280\n"
"v_mfma_f32_16x16x32_bf16 v[204:207], a[148:151], a[172:175], v[204:207]\n"
"ds_read_b64_tr_b16 a[90:91], v236 offset:34304\n"
"v_mfma_f32_16x16x32_bf16 v[208:211], a[144:147], a[176:179], v[208:211]\n"
"buffer_load_dwordx4 v226, s[28:31], s56 offen offset:2048  lds\n"
"v_mfma_f32_16x16x32_bf16 v[208:211], a[148:151], a[180:183], v[208:211]\n"
"s_sub_u32 s56, s56, 30592\n"
"ds_read_b64_tr_b16 a[92:93], v236 offset:41472\n"
"v_mfma_f32_16x16x32_bf16 v[212:215], a[152:155], a[160:163], v[212:215]\n"
"s_cmp_eq_u32 s56, 1280\n"
"s_cselect_b32 s56, 0, s56\n"
"ds_read_b64_tr_b16 a[94:95], v236 offset:42496\n"
"v_mfma_f32_16x16x32_bf16 v[212:215], a[156:159], a[164:167], v[212:215]\n"
"v_mfma_f32_16x16x32_bf16 v[216:219], a[152:155], a[168:171], v[216:219]\n"
"s_add_u32 m0, s46, 24576\n"
"v_mfma_f32_16x16x32_bf16 v[216:219], a[156:159], a[172:175], v[216:219]\n"
"buffer_load_dwordx4 v252, s[20:23], s44 offen nt lds\n"
"v_mfma_f32_16x16x32_bf16 v[220:223], a[152:155], a[176:179], v[220:223]\n"
"s_add_u32 s44, s44, 4096\n"
"v_mfma_f32_16x16x32_bf16 v[220:223], a[156:159], a[180:183], v[220:223]\n"
"s_waitcnt vmcnt(12) lgkmcnt(0)\n"
"s_barrier\n"
"v_mfma_f32_16x16x32_bf16 v[128:131], a[32:35], a[184:187], v[128:131]\n"
"s_add_u32 m0, s59, 32768\n"
"ds_read_b128 a[160:163], v231 offset:6144\n"
"ds_read_b128 a[164:167], v232 offset:6144\n"
"v_mfma_f32_16x16x32_bf16 v[128:131], a[36:39], a[188:191], v[128:131]\n"
"buffer_load_dwordx4 v227, s[36:39], s57 offen offset:0  lds\n"
"v_mfma_f32_16x16x32_bf16 v[132:135], a[32:35], a[192:195], v[132:135]\n"
"ds_read_b128 a[168:171], v231 offset:8192\n"
"ds_read_b128 a[172:175], v232 offset:8192\n"
"v_mfma_f32_16x16x32_bf16 v[132:135], a[36:39], a[196:199], v[132:135]\n"
"ds_read_b128 a[176:179], v231 offset:10240\n"
"v_mfma_f32_16x16x32_bf16 v[136:139], a[32:35], a[200:203], v[136:139]\n"
"buffer_load_dwordx4 v228, s[36:39], s57 offen offset:1024  lds\n"
"v_mfma_f32_16x16x32_bf16 v[136:139], a[36:39], a[204:207], v[136:139]\n"
"ds_read_b128 a[180:183], v232 offset:10240\n"
"ds_read_b64_tr_b16 a[96:97], v233 offset:0\n"
"v_mfma_f32_16x16x32_bf16 v[140:143], a[40:43], a[184:187], v[140:143]\n"
"ds_read_b64_tr_b16 a[98:99], v233 offset:1024\n"
"v_mfma_f32_16x16x32_bf16 v[140:143], a[44:47], a[188:191], v[140:143]\n"
"buffer_load_dwordx4 v229, s[36:39], s57 offen offset:2048  lds\n"
"v_mfma_f32_16x16x32_bf16 v[144:147], a[40:43], a[192:195], v[144:147]\n"
"ds_read_b64_tr_b16 a[100:101], v233 offset:8192\n"
"ds_read_b64_tr_b16 a[102:103], v233 offset:9216\n"
"v_mfma_f32_16x16x32_bf16 v[144:147], a[44:47], a[196:199], v[144:147]\n"
"ds_read_b64_tr_b16 a[104:105], v234 offset:0\n"
"v_mfma_f32_16x16x32_bf16 v[148:151], a[40:43], a[200:203], v[148:151]\n"
"buffer_load_dwordx4 v230, s[36:39], s57 offen offset:3072  lds\n"
"v_mfma_f32_16x16x32_bf16 v[148:151], a[44:47], a[204:207], v[148:151]\n"
"s_add_u32 s57, s57, 524288\n"
"ds_read_b64_tr_b16 a[106:107], v234 offset:1024\n"
"ds_read_b64_tr_b16 a[108:109], v234 offset:8192\n"
"v_mfma_f32_16x16x32_bf16 v[152:155], a[48:51], a[184:187], v[152:155]\n"
"s_cmp_eq_u32 s57, 5242880\n"
"s_cselect_b32 s57, 0, s57\n"
"ds_read_b64_tr_b16 a[110:111], v234 offset:9216\n"
"v_mfma_f32_16x16x32_bf16 v[152:155], a[52:55], a[188:191], v[152:155]\n"
"s_waitcnt lgkmcnt(8)\n"
"ds_read_b64_tr_b16 a[112:113], v235 offset:0\n"
"v_mfma_f32_16x16x32_bf16 v[156:159], a[48:51], a[192:195], v[156:159]\n"
"s_add_u32 m0, s58, 6144\n"
"ds_read_b64_tr_b16 a[114:115], v235 offset:1024\n"
"v_mfma_f32_16x16x32_bf16 v[156:159], a[52:55], a[196:199], v[156:159]\n"
"buffer_load_dwordx4 v224, s[28:31], s56 offen offset:0  lds\n"
"v_mfma_f32_16x16x32_bf16 v[160:163], a[48:51], a[200:203], v[160:163]\n"
"ds_read_b64_tr_b16 a[116:117], v235 offset:8192\n"
"v_mfma_f32_16x16x32_bf16 v[160:163], a[52:55], a[204:207], v[160:163]\n"
"ds_read_b64_tr_b16 a[118:119], v235 offset:9216\n"
"v_mfma_f32_16x16x32_bf16 v[164:167], a[56:59], a[184:187], v[164:167]\n"
"ds_read_b64_tr_b16 a[120:121], v236 offset:0\n"
"v_mfma_f32_16x16x32_bf16 v[164:167], a[60:63], a[188:191], v[164:167]\n"
"ds_read_b64_tr_b16 a[122:123], v236 offset:1024\n"
"v_mfma_f32_16x16x32_bf16 v[168:171], a[56:59], a[192:195], v[168:171]\n"
"buffer_load_dwordx4 v225, s[28:31], s56 offen offset:1024  lds\n"
"v_mfma_f32_16x16x32_bf16 v[168:171], a[60:63], a[196:199], v[168:171]\n"
"ds_read_b64_tr_b16 a[124:125], v236 offset:8192\n"
"v_mfma_f32_16x16x32_bf16 v[172:175], a[56:59], a[200:203], v[172:175]\n"
"ds_read_b64_tr_b16 a[126:127], v236 offset:9216\n"
"v_mfma_f32_16x16x32_bf16 v[172:175], a[60:63], a[204:207], v[172:175]\n"
"ds_read_b64_tr_b16 a[128:129], v233 offset:512\n"
"v_mfma_f32_16x16x32_bf16 v[176:179], a[64:67], a[184:187], v[176:179]\n"
"ds_read_b64_tr_b16 a[130:131], v233 offset:1536\n"
"v_mfma_f32_16x16x32_bf16 v[176:179], a[68:71], a[188:191], v[176:179]\n"
"buffer_load_dwordx4 v226, s[28:31], s56 offen offset:2048  lds\n"
"v_mfma_f32_16x16x32_bf16 v[180:183], a[64:67], a[192:195], v[180:183]\n"
"s_add_u32 s56, s56, 30720\n"
"ds_read_b64_tr_b16 a[132:133], v233 offset:8704\n"
"v_mfma_f32_16x16x32_bf16 v[180:183], a[68:71], a[196:199], v[180:183]\n"
"ds_read_b64_tr_b16 a[134:135], v233 offset:9728\n"
"v_mfma_f32_16x16x32_bf16 v[184:187], a[64:67], a[200:203], v[184:187]\n"
"ds_read_b64_tr_b16 a[136:137], v234 offset:512\n"
"v_mfma_f32_16x16x32_bf16 v[184:187], a[68:71], a[204:207], v[184:187]\n"
"s_add_u32 m0, s58, 9216\n"
"ds_read_b64_tr_b16 a[138:139], v234 offset:1536\n"
"v_mfma_f32_16x16x32_bf16 v[188:191], a[72:75], a[184:187], v[188:191]\n"
"buffer_load_dwordx4 v224, s[28:31], s56 offen offset:0  lds\n"
"v_mfma_f32_16x16x32_bf16 v[188:191], a[76:79], a[188:191], v[188:191]\n"
"ds_read_b64_tr_b16 a[140:141], v234 offset:8704\n"
"v_mfma_f32_16x16x32_bf16 v[192:195], a[72:75], a[192:195], v[192:195]\n"
"ds_read_b64_tr_b16 a[142:143], v234 offset:9728\n"
"v_mfma_f32_16x16x32_bf16 v[192:195], a[76:79], a[196:199], v[192:195]\n"
"ds_read_b64_tr_b16 a[144:145], v235 offset:512\n"
"v_mfma_f32_16x16x32_bf16 v[196:199], a[72:75], a[200:203], v[196:199]\n"
"ds_read_b64_tr_b16 a[146:147], v235 offset:1536\n"
"v_mfma_f32_16x16x32_bf16 v[196:199], a[76:79], a[204:207], v[196:199]\n"
"buffer_load_dwordx4 v225, s[28:31], s56 offen offset:1024  lds\n"
"v_mfma_f32_16x16x32_bf16 v[200:203], a[80:83], a[184:187], v[200:203]\n"
"ds_read_b64_tr_b16 a[148:149], v235 offset:8704\n"
"v_mfma_f32_16x16x32_bf16 v[200:203], a[84:87], a[188:191], v[200:203]\n"
"ds_read_b64_tr_b16 a[150:151], v235 offset:9728\n"
"v_mfma_f32_16x16x32_bf16 v[204:207], a[80:83], a[192:195], v[204:207]\n"
"ds_read_b64_tr_b16 a[152:153], v236 offset:512\n"
"v_mfma_f32_16x16x32_bf16 v[204:207], a[84:87], a[196:199], v[204:207]\n"
"ds_read_b64_tr_b16 a[154:155], v236 offset:1536\n"
"v_mfma_f32_16x16x32_bf16 v[208:211], a[80:83], a[200:203], v[208:211]\n"
"buffer_load_dwordx4 v226, s[28:31], s56 offen offset:2048  lds\n"
"v_mfma_f32_16x16x32_bf16 v[208:211], a[84:87], a[204:207], v[208:211]\n"
"s_sub_u32 s56, s56, 30592\n"
"ds_read_b64_tr_b16 a[156:157], v236 offset:8704\n"
"v_mfma_f32_16x16x32_bf16 v[212:215], a[88:91], a[184:187], v[212:215]\n"
"s_cmp_eq_u32 s56, 1280\n"
"s_cselect_b32 s56, 0, s56\n"
"ds_read_b64_tr_b16 a[158:159], v236 offset:9728\n"
"v_mfma_f32_16x16x32_bf16 v[212:215], a[92:95], a[188:191], v[212:215]\n"
"v_mfma_f32_16x16x32_bf16 v[216:219], a[88:91], a[192:195], v[216:219]\n"
"s_add_u32 m0, s46, 28672\n"
"v_mfma_f32_16x16x32_bf16 v[216:219], a[92:95], a[196:199], v[216:219]\n"
"buffer_load_dwordx4 v252, s[20:23], s44 offen nt lds\n"
"v_mfma_f32_16x16x32_bf16 v[220:223], a[88:91], a[200:203], v[220:223]\n"
"s_add_u32 s44, s44, 4096\n"
"v_mfma_f32_16x16x32_bf16 v[220:223], a[92:95], a[204:207], v[220:223]\n"
"s_waitcnt vmcnt(12) lgkmcnt(0)\n"
"s_barrier\n"
"v_mfma_f32_16x16x32_bf16 v[128:131], a[96:99], a[160:163], v[128:131]\n"
"s_add_u32 m0, s46, 131072\n"
"ds_read_b128 a[184:187], v231 offset:0\n"
"v_mfma_f32_16x16x32_bf16 v[128:131], a[100:103], a[164:167], v[128:131]\n"
"buffer_load_dwordx4 v253, s[24:27], s51 offen nt lds\n"
"v_mfma_f32_16x16x32_bf16 v[132:135], a[96:99], a[168:171], v[132:135]\n"
"s_add_u32 s51, s51, 4096\n"
"ds_read_b128 a[188:191], v232 offset:0\n"
"v_mfma_f32_16x16x32_bf16 v[132:135], a[100:103], a[172:175], v[132:135]\n"
"s_and_b32 s52, s40, 1\n"
"ds_read_b128 a[192:195], v231 offset:2048\n"
"v_mfma_f32_16x16x32_bf16 v[136:139], a[96:99], a[176:179], v[136:139]\n"
"s_lshr_b32 s53, s40, 1\n"
"ds_read_b128 a[196:199], v232 offset:2048\n"
"v_mfma_f32_16x16x32_bf16 v[136:139], a[100:103], a[180:183], v[136:139]\n"
"s_lshl1_add_u32 s52, s52, s53\n"
"ds_read_b128 a[200:203], v231 offset:4096\n"
"v_mfma_f32_16x16x32_bf16 v[140:143], a[104:107], a[160:163], v[140:143]\n"
"s_mul_i32 s52, s52, 12288\n"
"ds_read_b128 a[204:207], v232 offset:4096\n"
"v_mfma_f32_16x16x32_bf16 v[140:143], a[108:111], a[164:167], v[140:143]\n"
"s_mul_i32 s53, s41, 49280\n"
"ds_read_b64_tr_b16 a[32:33], v233 offset:16384\n"
"v_mfma_f32_16x16x32_bf16 v[144:147], a[104:107], a[168:171], v[144:147]\n"
"s_add_u32 s52, s52, s53\n"
"ds_read_b64_tr_b16 a[34:35], v233 offset:17408\n"
"v_mfma_f32_16x16x32_bf16 v[144:147], a[108:111], a[172:175], v[144:147]\n"
"s_add_u32 m0, s46, 135168\n"
"ds_read_b64_tr_b16 a[36:37], v233 offset:24576\n"
"v_mfma_f32_16x16x32_bf16 v[148:151], a[104:107], a[176:179], v[148:151]\n"
"buffer_load_dwordx4 v253, s[24:27], s51 offen nt lds\n"
"v_mfma_f32_16x16x32_bf16 v[148:151], a[108:111], a[180:183], v[148:151]\n"
"s_add_u32 s51, s51, 4096\n"
"ds_read_b64_tr_b16 a[38:39], v233 offset:25600\n"
"v_mfma_f32_16x16x32_bf16 v[152:155], a[112:115], a[160:163], v[152:155]\n"
"s_add_u32 s52, s52, 16777216\n"
"ds_read_b64_tr_b16 a[40:41], v234 offset:16384\n"
"v_mfma_f32_16x16x32_bf16 v[152:155], a[116:119], a[164:167], v[152:155]\n"
"s_add_u32 s28, s72, s52\n"
"s_addc_u32 s29, s73, 0\n"
"ds_read_b64_tr_b16 a[42:43], v234 offset:17408\n"
"v_mfma_f32_16x16x32_bf16 v[156:159], a[112:115], a[168:171], v[156:159]\n"
"s_mov_b32 s30, 12288\n"
"ds_read_b64_tr_b16 a[44:45], v234 offset:24576\n"
"v_mfma_f32_16x16x32_bf16 v[156:159], a[116:119], a[172:175], v[156:159]\n"
"s_lshl_b32 s58, s40, 13\n"
"ds_read_b64_tr_b16 a[46:47], v234 offset:25600\n"
"v_mfma_f32_16x16x32_bf16 v[160:163], a[112:115], a[176:179], v[160:163]\n"
"s_lshr_b32 s55, %2, 3\n"
"ds_read_b64_tr_b16 a[48:49], v235 offset:16384\n"
"v_mfma_f32_16x16x32_bf16 v[160:163], a[116:119], a[180:183], v[160:163]\n"
"s_min_u32 s52, s55, 29\n"
"ds_read_b64_tr_b16 a[50:51], v235 offset:17408\n"
"v_mfma_f32_16x16x32_bf16 v[164:167], a[120:123], a[160:163], v[164:167]\n"
"s_add_u32 m0, s46, 139264\n"
"ds_read_b64_tr_b16 a[52:53], v235 offset:24576\n"
"v_mfma_f32_16x16x32_bf16 v[164:167], a[124:127], a[164:167], v[164:167]\n"
"buffer_load_dwordx4 v253, s[24:27], s51 offen nt lds\n"
"v_mfma_f32_16x16x32_bf16 v[168:171], a[120:123], a[168:171], v[168:171]\n"
"s_add_u32 s51, s51, 4096\n"
"ds_read_b64_tr_b16 a[54:55], v235 offset:25600\n"
"v_mfma_f32_16x16x32_bf16 v[168:171], a[124:127], a[172:175], v[168:171]\n"
"s_mul_i32 s53, s52, 52\n"
"ds_read_b64_tr_b16 a[56:57], v236 offset:16384\n"
"v_mfma_f32_16x16x32_bf16 v[172:175], a[120:123], a[176:179], v[172:175]\n"
"s_lshr_b32 s53, s53, 8\n"
"ds_read_b64_tr_b16 a[58:59], v236 offset:17408\n"
"v_mfma_f32_16x16x32_bf16 v[172:175], a[124:127], a[180:183], v[172:175]\n"
"s_mul_i32 s54, s53, 5\n"
"ds_read_b64_tr_b16 a[60:61], v236 offset:24576\n"
"v_mfma_f32_16x16x32_bf16 v[176:179], a[128:131], a[160:163], v[176:179]\n"
"s_sub_u32 s54, s52, s54\n"
"ds_read_b64_tr_b16 a[62:63], v236 offset:25600\n"
"v_mfma_f32_16x16x32_bf16 v[176:179], a[132:135], a[164:167], v[176:179]\n"
"s_and_b32 s52, s40, 1\n"
"ds_read_b64_tr_b16 a[64:65], v233 offset:16896\n"
"v_mfma_f32_16x16x32_bf16 v[180:183], a[128:131], a[168:171], v[180:183]\n"
"s_lshl1_add_u32 s54, s54, s52\n"
"ds_read_b64_tr_b16 a[66:67], v233 offset:17920\n"
"v_mfma_f32_16x16x32_bf16 v[180:183], a[132:135], a[172:175], v[180:183]\n"
"s_add_u32 m0, s46, 143360\n"
"ds_read_b64_tr_b16 a[68:69], v233 offset:25088\n"
"v_mfma_f32_16x16x32_bf16 v[184:187], a[128:131], a[176:179], v[184:187]\n"
"buffer_load_dwordx4 v253, s[24:27], s51 offen nt lds\n"
"v_mfma_f32_16x16x32_bf16 v[184:187], a[132:135], a[180:183], v[184:187]\n"
"s_add_u32 s51, s51, 4096\n"
"ds_read_b64_tr_b16 a[70:71], v233 offset:26112\n"
"v_mfma_f32_16x16x32_bf16 v[188:191], a[136:139], a[160:163], v[188:191]\n"
"s_lshl_b32 s60, s54, 7\n"
"ds_read_b64_tr_b16 a[72:73], v234 offset:16896\n"
"v_mfma_f32_16x16x32_bf16 v[188:191], a[140:143], a[164:167], v[188:191]\n"
"s_bfe_u32 s52, %2, 0x10002\n"
"ds_read_b64_tr_b16 a[74:75], v234 offset:17920\n"
"v_mfma_f32_16x16x32_bf16 v[192:195], a[136:139], a[168:171], v[192:195]\n"
"s_mul_i32 s52, s52, 6\n"
"ds_read_b64_tr_b16 a[76:77], v234 offset:25088\n"
"v_mfma_f32_16x16x32_bf16 v[192:195], a[140:143], a[172:175], v[192:195]\n"
"s_add_u32 s47, s52, s53\n"
"ds_read_b64_tr_b16 a[78:79], v234 offset:26112\n"
"v_mfma_f32_16x16x32_bf16 v[196:199], a[136:139], a[176:179], v[196:199]\n"
"s_and_b32 s52, %2, 3\n"
"ds_read_b64_tr_b16 a[80:81], v235 offset:16896\n"
"v_mfma_f32_16x16x32_bf16 v[196:199], a[140:143], a[180:183], v[196:199]\n"
"s_lshl2_add_u32 s52, s52, s40\n"
"ds_read_b64_tr_b16 a[82:83], v235 offset:17920\n"
"v_mfma_f32_16x16x32_bf16 v[200:203], a[144:147], a[160:163], v[200:203]\n"
"s_add_u32 m0, s46, 147456\n"
"ds_read_b64_tr_b16 a[84:85], v235 offset:25088\n"
"v_mfma_f32_16x16x32_bf16 v[200:203], a[148:151], a[164:167], v[200:203]\n"
"buffer_load_dwordx4 v253, s[24:27], s51 offen nt lds\n"
"v_mfma_f32_16x16x32_bf16 v[204:207], a[144:147], a[168:171], v[204:207]\n"
"s_add_u32 s51, s51, 4096\n"
"ds_read_b64_tr_b16 a[86:87], v235 offset:26112\n"
"v_mfma_f32_16x16x32_bf16 v[204:207], a[148:151], a[172:175], v[204:207]\n"
"s_and_b32 s53, s53, 3\n"
"ds_read_b64_tr_b16 a[88:89], v236 offset:16896\n"
"v_mfma_f32_16x16x32_bf16 v[208:211], a[144:147], a[176:179], v[208:211]\n"
"s_lshl_b32 s52, s52, 1\n"
"ds_read_b64_tr_b16 a[90:91], v236 offset:17920\n"
"v_mfma_f32_16x16x32_bf16 v[208:211], a[148:151], a[180:183], v[208:211]\n"
"s_xor_b32 s50, s52, s53\n"
"ds_read_b64_tr_b16 a[92:93], v236 offset:25088\n"
"v_mfma_f32_16x16x32_bf16 v[212:215], a[152:155], a[160:163], v[212:215]\n"
"s_mov_b32 s36, s68\n"
"ds_read_b64_tr_b16 a[94:95], v236 offset:26112\n"
"v_mfma_f32_16x16x32_bf16 v[212:215], a[156:159], a[164:167], v[212:215]\n"
"s_mov_b32 s37, s69\n"
"v_mfma_f32_16x16x32_bf16 v[216:219], a[152:155], a[168:171], v[216:219]\n"
"s_mov_b32 s38, 5242880\n"
"v_mfma_f32_16x16x32_bf16 v[216:219], a[156:159], a[172:175], v[216:219]\n"
"s_xor_b32 s56, s50, 0\n"
"v_mfma_f32_16x16x32_bf16 v[220:223], a[152:155], a[176:179], v[220:223]\n"
"s_mul_i32 s56, s56, 163840\n"
"v_mfma_f32_16x16x32_bf16 v[220:223], a[156:159], a[180:183], v[220:223]\n"
"s_add_u32 s56, s56, s60\n"
"s_waitcnt vmcnt(6) lgkmcnt(0)\n"
"s_barrier\n"
"v_mfma_f32_16x16x32_bf16 v[128:131], a[32:35], a[184:187], v[128:131]\n"
"s_add_u32 m0, s46, 151552\n"
"ds_read_b128 a[160:163], v231 offset:6144\n"
"v_mfma_f32_16x16x32_bf16 v[128:131], a[36:39], a[188:191], v[128:131]\n"
"buffer_load_dwordx4 v253, s[24:27], s51 offen nt lds\n"
"v_mfma_f32_16x16x32_bf16 v[132:135], a[32:35], a[192:195], v[132:135]\n"
"s_add_u32 s51, s51, 4096\n"
"ds_read_b128 a[164:167], v232 offset:6144\n"
"v_mfma_f32_16x16x32_bf16 v[132:135], a[36:39], a[196:199], v[132:135]\n"
"s_xor_b32 s56, s56, 128\n"
"ds_read_b128 a[168:171], v231 offset:8192\n"
"v_mfma_f32_16x16x32_bf16 v[136:139], a[32:35], a[200:203], v[136:139]\n"
"s_xor_b32 s57, s50, 0\n"
"ds_read_b128 a[172:175], v232 offset:8192\n"
"v_mfma_f32_16x16x32_bf16 v[136:139], a[36:39], a[204:207], v[136:139]\n"
"s_mul_i32 s57, s57, 163840\n"
"ds_read_b128 a[176:179], v231 offset:10240\n"
"v_mfma_f32_16x16x32_bf16 v[140:143], a[40:43], a[184:187], v[140:143]\n"
"s_add_u32 s57, s57, s60\n"
"ds_read_b128 a[180:183], v232 offset:10240\n"
"v_mfma_f32_16x16x32_bf16 v[140:143], a[44:47], a[188:191], v[140:143]\n"
"s_xor_b32 s59, s50, 1\n"
"ds_read_b64_tr_b16 a[96:97], v233 offset:32768\n"
"v_mfma_f32_16x16x32_bf16 v[144:147], a[40:43], a[192:195], v[144:147]\n"
"s_mul_i32 s59, s59, 163840\n"
"ds_read_b64_tr_b16 a[98:99], v233 offset:33792\n"
"v_mfma_f32_16x16x32_bf16 v[144:147], a[44:47], a[196:199], v[144:147]\n"
"s_add_u32 m0, s46, 155648\n"
"ds_read_b64_tr_b16 a[100:101], v233 offset:40960\n"
"v_mfma_f32_16x16x32_bf16 v[148:151], a[40:43], a[200:203], v[148:151]\n"
"buffer_load_dwordx4 v253, s[24:27], s51 offen nt lds\n"
"v_mfma_f32_16x16x32_bf16 v[148:151], a[44:47], a[204:207], v[148:151]\n"
"s_add_u32 s51, s51, 4096\n"
"ds_read_b64_tr_b16 a[102:103], v233 offset:41984\n"
"v_mfma_f32_16x16x32_bf16 v[152:155], a[48:51], a[184:187], v[152:155]\n"
"s_add_u32 s59, s59, s60\n"
"ds_read_b64_tr_b16 a[104:105], v234 offset:32768\n"
"v_mfma_f32_16x16x32_bf16 v[152:155], a[52:55], a[188:191], v[152:155]\n"
"s_xor_b32 s59, s59, 128\n"
"ds_read_b64_tr_b16 a[106:107], v234 offset:33792\n"
"v_mfma_f32_16x16x32_bf16 v[156:159], a[48:51], a[192:195], v[156:159]\n"
"ds_read_b64_tr_b16 a[108:109], v234 offset:40960\n"
"v_mfma_f32_16x16x32_bf16 v[156:159], a[52:55], a[196:199], v[156:159]\n"
"ds_read_b64_tr_b16 a[110:111], v234 offset:41984\n"
"v_mfma_f32_16x16x32_bf16 v[160:163], a[48:51], a[200:203], v[160:163]\n"
"ds_read_b64_tr_b16 a[112:113], v235 offset:32768\n"
"v_mfma_f32_16x16x32_bf16 v[160:163], a[52:55], a[204:207], v[160:163]\n"
"ds_read_b64_tr_b16 a[114:115], v235 offset:33792\n"
"v_mfma_f32_16x16x32_bf16 v[164:167], a[56:59], a[184:187], v[164:167]\n"
"s_add_u32 m0, s46, 159744\n"
"ds_read_b64_tr_b16 a[116:117], v235 offset:40960\n"
"v_mfma_f32_16x16x32_bf16 v[164:167], a[60:63], a[188:191], v[164:167]\n"
"buffer_load_dwordx4 v253, s[24:27], s51 offen nt lds\n"
"v_mfma_f32_16x16x32_bf16 v[168:171], a[56:59], a[192:195], v[168:171]\n"
"s_add_u32 s51, s51, 4096\n"
"ds_read_b64_tr_b16 a[118:119], v235 offset:41984\n"
"v_mfma_f32_16x16x32_bf16 v[168:171], a[60:63], a[196:199], v[168:171]\n"
"ds_read_b64_tr_b16 a[120:121], v236 offset:32768\n"
"v_mfma_f32_16x16x32_bf16 v[172:175], a[56:59], a[200:203], v[172:175]\n"
"ds_read_b64_tr_b16 a[122:123], v236 offset:33792\n"
"v_mfma_f32_16x16x32_bf16 v[172:175], a[60:63], a[204:207], v[172:175]\n"
"ds_read_b64_tr_b16 a[124:125], v236 offset:40960\n"
"v_mfma_f32_16x16x32_bf16 v[176:179], a[64:67], a[184:187], v[176:179]\n"
"ds_read_b64_tr_b16 a[126:127], v236 offset:41984\n"
"v_mfma_f32_16x16x32_bf16 v[176:179], a[68:71], a[188:191], v[176:179]\n"
"ds_read_b64_tr_b16 a[128:129], v233 offset:33280\n"
"v_mfma_f32_16x16x32_bf16 v[180:183], a[64:67], a[192:195], v[180:183]\n"
"ds_read_b64_tr_b16 a[130:131], v233 offset:34304\n"
"v_mfma_f32_16x16x32_bf16 v[180:183], a[68:71], a[196:199], v[180:183]\n"
"s_add_u32 m0, s46, 32768\n"
"ds_read_b64_tr_b16 a[132:133], v233 offset:41472\n"
"v_mfma_f32_16x16x32_bf16 v[184:187], a[64:67], a[200:203], v[184:187]\n"
"buffer_load_dwordx4 v252, s[20:23], s44 offen nt lds\n"
"v_mfma_f32_16x16x32_bf16 v[184:187], a[68:71], a[204:207], v[184:187]\n"
"s_add_u32 s44, s44, 4096\n"
"ds_read_b64_tr_b16 a[134:135], v233 offset:42496\n"
"v_mfma_f32_16x16x32_bf16 v[188:191], a[72:75], a[184:187], v[188:191]\n"
"ds_read_b64_tr_b16 a[136:137], v234 offset:33280\n"
"v_mfma_f32_16x16x32_bf16 v[188:191], a[76:79], a[188:191], v[188:191]\n"
"ds_read_b64_tr_b16 a[138:139], v234 offset:34304\n"
"v_mfma_f32_16x16x32_bf16 v[192:195], a[72:75], a[192:195], v[192:195]\n"
"ds_read_b64_tr_b16 a[140:141], v234 offset:41472\n"
"v_mfma_f32_16x16x32_bf16 v[192:195], a[76:79], a[196:199], v[192:195]\n"
"ds_read_b64_tr_b16 a[142:143], v234 offset:42496\n"
"v_mfma_f32_16x16x32_bf16 v[196:199], a[72:75], a[200:203], v[196:199]\n"
"ds_read_b64_tr_b16 a[144:145], v235 offset:33280\n"
"v_mfma_f32_16x16x32_bf16 v[196:199], a[76:79], a[204:207], v[196:199]\n"
"ds_read_b64_tr_b16 a[146:147], v235 offset:34304\n"
"v_mfma_f32_16x16x32_bf16 v[200:203], a[80:83], a[184:187], v[200:203]\n"
"s_add_u32 m0, s46, 36864\n"
"ds_read_b64_tr_b16 a[148:149], v235 offset:41472\n"
"v_mfma_f32_16x16x32_bf16 v[200:203], a[84:87], a[188:191], v[200:203]\n"
"buffer_load_dwordx4 v252, s[20:23], s44 offen nt lds\n"
"v_mfma_f32_16x16x32_bf16 v[204:207], a[80:83], a[192:195], v[204:207]\n"
"s_add_u32 s44, s44, 4096\n"
"ds_read_b64_tr_b16 a[150:151], v235 offset:42496\n"
"v_mfma_f32_16x16x32_bf16 v[204:207], a[84:87], a[196:199], v[204:207]\n"
"ds_read_b64_tr_b16 a[152:153], v236 offset:33280\n"
"v_mfma_f32_16x16x32_bf16 v[208:211], a[80:83], a[200:203], v[208:211]\n"
"ds_read_b64_tr_b16 a[154:155], v236 offset:34304\n"
"v_mfma_f32_16x16x32_bf16 v[208:211], a[84:87], a[204:207], v[208:211]\n"
"ds_read_b64_tr_b16 a[156:157], v236 offset:41472\n"
"v_mfma_f32_16x16x32_bf16 v[212:215], a[88:91], a[184:187], v[212:215]\n"
"ds_read_b64_tr_b16 a[158:159], v236 offset:42496\n"
"v_mfma_f32_16x16x32_bf16 v[212:215], a[92:95], a[188:191], v[212:215]\n"
"v_mfma_f32_16x16x32_bf16 v[216:219], a[88:91], a[192:195], v[216:219]\n"
"v_mfma_f32_16x16x32_bf16 v[216:219], a[92:95], a[196:199], v[216:219]\n"
"v_mfma_f32_16x16x32_bf16 v[220:223], a[88:91], a[200:203], v[220:223]\n"
"v_mfma_f32_16x16x32_bf16 v[220:223], a[92:95], a[204:207], v[220:223]\n"
"s_waitcnt lgkmcnt(0)\n"
"s_barrier\n"
"v_mfma_f32_16x16x32_bf16 v[128:131], a[96:99], a[160:163], v[128:131]\n"
"s_add_u32 m0, s46, 40960\n"
"v_mfma_f32_16x16x32_bf16 v[128:131], a[100:103], a[164:167], v[128:131]\n"
"buffer_load_dwordx4 v252, s[20:23], s44 offen nt lds\n"
"v_mfma_f32_16x16x32_bf16 v[132:135], a[96:99], a[168:171], v[132:135]\n"
"s_add_u32 s44, s44, 4096\n"
"v_mfma_f32_16x16x32_bf16 v[132:135], a[100:103], a[172:175], v[132:135]\n"
"s_nop 2\n"
"v_mfma_f32_16x16x32_bf16 v[136:139], a[96:99], a[176:179], v[136:139]\n"
"s_nop 2\n"
"v_mfma_f32_16x16x32_bf16 v[136:139], a[100:103], a[180:183], v[136:139]\n"
"s_nop 2\n"
"v_mfma_f32_16x16x32_bf16 v[140:143], a[104:107], a[160:163], v[140:143]\n"
"v_mul_f32 v128, s96, v128\n"
"v_mul_f32 v129, s96, v129\n"
"v_mul_f32 v130, s96, v130\n"
"v_mul_f32 v131, s96, v131\n"
"v_mfma_f32_16x16x32_bf16 v[140:143], a[108:111], a[164:167], v[140:143]\n"
"s_add_u32 m0, s46, 45056\n"
"v_cvt_pk_bf16_f32 v128, v128, v129\n"
"v_cvt_pk_bf16_f32 v129, v130, v131\n"
"v_mfma_f32_16x16x32_bf16 v[144:147], a[104:107], a[168:171], v[144:147]\n"
"buffer_load_dwordx4 v252, s[20:23], s44 offen nt lds\n"
"v_mul_f32 v132, s96, v132\n"
"v_mul_f32 v133, s96, v133\n"
"v_mul_f32 v134, s96, v134\n"
"v_mul_f32 v135, s96, v135\n"
"v_mfma_f32_16x16x32_bf16 v[144:147], a[108:111], a[172:175], v[144:147]\n"
"s_add_u32 s44, s44, 4096\n"
"ds_write_b64 v237, v[128:129] offset:0\n"
"v_cvt_pk_bf16_f32 v132, v132, v133\n"
"v_cvt_pk_bf16_f32 v133, v134, v135\n"
"v_mfma_f32_16x16x32_bf16 v[148:151], a[104:107], a[176:179], v[148:151]\n"
"v_mul_f32 v136, s96, v136\n"
"v_mul_f32 v137, s96, v137\n"
"v_mul_f32 v138, s96, v138\n"
"v_mul_f32 v139, s96, v139\n"
"v_mfma_f32_16x16x32_bf16 v[148:151], a[108:111], a[180:183], v[148:151]\n"
"ds_write_b64 v237, v[132:133] offset:4096\n"
"v_cvt_pk_bf16_f32 v136, v136, v137\n"
"v_cvt_pk_bf16_f32 v137, v138, v139\n"
"v_mfma_f32_16x16x32_bf16 v[152:155], a[112:115], a[160:163], v[152:155]\n"
"v_mul_f32 v140, s96, v140\n"
"v_mul_f32 v141, s96, v141\n"
"v_mul_f32 v142, s96, v142\n"
"v_mul_f32 v143, s96, v143\n"
"v_mfma_f32_16x16x32_bf16 v[152:155], a[116:119], a[164:167], v[152:155]\n"
"ds_write_b64 v237, v[136:137] offset:8192\n"
"v_cvt_pk_bf16_f32 v140, v140, v141\n"
"v_cvt_pk_bf16_f32 v141, v142, v143\n"
"v_mfma_f32_16x16x32_bf16 v[156:159], a[112:115], a[168:171], v[156:159]\n"
"s_add_u32 m0, s46, 49152\n"
"v_mul_f32 v144, s96, v144\n"
"v_mul_f32 v145, s96, v145\n"
"v_mul_f32 v146, s96, v146\n"
"v_mul_f32 v147, s96, v147\n"
"v_mfma_f32_16x16x32_bf16 v[156:159], a[116:119], a[172:175], v[156:159]\n"
"buffer_load_dwordx4 v252, s[20:23], s44 offen nt lds\n"
"ds_write_b64 v237, v[140:141] offset:512\n"
"v_cvt_pk_bf16_f32 v144, v144, v145\n"
"v_cvt_pk_bf16_f32 v145, v146, v147\n"
"v_mfma_f32_16x16x32_bf16 v[160:163], a[112:115], a[176:179], v[160:163]\n"
"s_add_u32 s44, s44, 4096\n"
"v_mul_f32 v148, s96, v148\n"
"v_mul_f32 v149, s96, v149\n"
"v_mul_f32 v150, s96, v150\n"
"v_mul_f32 v151, s96, v151\n"
"v_mfma_f32_16x16x32_bf16 v[160:163], a[116:119], a[180:183], v[160:163]\n"
"ds_write_b64 v237, v[144:145] offset:4608\n"
"v_cvt_pk_bf16_f32 v148, v148, v149\n"
"v_cvt_pk_bf16_f32 v149, v150, v151\n"
"v_mfma_f32_16x16x32_bf16 v[164:167], a[120:123], a[160:163], v[164:167]\n"
"v_mul_f32 v152, s96, v152\n"
"v_mul_f32 v153, s96, v153\n"
"v_mul_f32 v154, s96, v154\n"
"v_mul_f32 v155, s96, v155\n"
"v_mfma_f32_16x16x32_bf16 v[164:167], a[124:127], a[164:167], v[164:167]\n"
"ds_write_b64 v237, v[148:149] offset:8704\n"
"v_cvt_pk_bf16_f32 v152, v152, v153\n"
"v_cvt_pk_bf16_f32 v153, v154, v155\n"
"v_mfma_f32_16x16x32_bf16 v[168:171], a[120:123], a[168:171], v[168:171]\n"
"v_mul_f32 v156, s96, v156\n"
"v_mul_f32 v157, s96, v157\n"
"v_mul_f32 v158, s96, v158\n"
"v_mul_f32 v159, s96, v159\n"
"v_mfma_f32_16x16x32_bf16 v[168:171], a[124:127], a[172:175], v[168:171]\n"
"s_add_u32 m0, s46, 53248\n"
"ds_write_b64 v237, v[152:153] offset:1024\n"
"v_cvt_pk_bf16_f32 v156, v156, v157\n"
"v_cvt_pk_bf16_f32 v157, v158, v159\n"
"v_mfma_f32_16x16x32_bf16 v[172:175], a[120:123], a[176:179], v[172:175]\n"
"buffer_load_dwordx4 v252, s[20:23], s44 offen nt lds\n"
"v_mul_f32 v160, s96, v160\n"
"v_mul_f32 v161, s96, v161\n"
"v_mul_f32 v162, s96, v162\n"
"v_mul_f32 v163, s96, v163\n"
"v_mfma_f32_16x16x32_bf16 v[172:175], a[124:127], a[180:183], v[172:175]\n"
"s_add_u32 s44, s44, 4096\n"
"ds_write_b64 v237, v[156:157] offset:5120\n"
"v_cvt_pk_bf16_f32 v160, v160, v161\n"
"v_cvt_pk_bf16_f32 v161, v162, v163\n"
"v_mfma_f32_16x16x32_bf16 v[176:179], a[128:131], a[160:163], v[176:179]\n"
"v_mul_f32 v164, s96, v164\n"
"v_mul_f32 v165, s96, v165\n"
"v_mul_f32 v166, s96, v166\n"
"v_mul_f32 v167, s96, v167\n"
"v_mfma_f32_16x16x32_bf16 v[176:179], a[132:135], a[164:167], v[176:179]\n"
"ds_write_b64 v237, v[160:161] offset:9216\n"
"v_cvt_pk_bf16_f32 v164, v164, v165\n"
"v_cvt_pk_bf16_f32 v165, v166, v167\n"
"v_mfma_f32_16x16x32_bf16 v[180:183], a[128:131], a[168:171], v[180:183]\n"
"v_mul_f32 v168, s96, v168\n"
"v_mul_f32 v169, s96, v169\n"
"v_mul_f32 v170, s96, v170\n"
"v_mul_f32 v171, s96, v171\n"
"v_mfma_f32_16x16x32_bf16 v[180:183], a[132:135], a[172:175], v[180:183]\n"
"ds_write_b64 v237, v[164:165] offset:1536\n"
"v_cvt_pk_bf16_f32 v168, v168, v169\n"
"v_cvt_pk_bf16_f32 v169, v170, v171\n"
"v_mfma_f32_16x16x32_bf16 v[184:187], a[128:131], a[176:179], v[184:187]\n"
"s_add_u32 m0, s46, 57344\n"
"v_mul_f32 v172, s96, v172\n"
"v_mul_f32 v173, s96, v173\n"
"v_mul_f32 v174, s96, v174\n"
"v_mul_f32 v175, s96, v175\n"
"v_mfma_f32_16x16x32_bf16 v[184:187], a[132:135], a[180:183], v[184:187]\n"
"buffer_load_dwordx4 v252, s[20:23], s44 offen nt lds\n"
"ds_write_b64 v237, v[168:169] offset:5632\n"
"v_cvt_pk_bf16_f32 v172, v172, v173\n"
"v_cvt_pk_bf16_f32 v173, v174, v175\n"
"v_mfma_f32_16x16x32_bf16 v[188:191], a[136:139], a[160:163], v[188:191]\n"
"s_add_u32 s44, s44, 4096\n"
"v_mul_f32 v176, s96, v176\n"
"v_mul_f32 v177, s96, v177\n"
"v_mul_f32 v178, s96, v178\n"
"v_mul_f32 v179, s96, v179\n"
"v_mfma_f32_16x16x32_bf16 v[188:191], a[140:143], a[164:167], v[188:191]\n"
"ds_write_b64 v237, v[172:173] offset:9728\n"
"v_cvt_pk_bf16_f32 v176, v176, v177\n"
"v_cvt_pk_bf16_f32 v177, v178, v179\n"
"v_mfma_f32_16x16x32_bf16 v[192:195], a[136:139], a[168:171], v[192:195]\n"
"v_mul_f32 v180, s96, v180\n"
"v_mul_f32 v181, s96, v181\n"
"v_mul_f32 v182, s96, v182\n"
"v_mul_f32 v183, s96, v183\n"
"v_mfma_f32_16x16x32_bf16 v[192:195], a[140:143], a[172:175], v[192:195]\n"
"ds_write_b64 v237, v[176:177] offset:2048\n"
"v_cvt_pk_bf16_f32 v180, v180, v181\n"
"v_cvt_pk_bf16_f32 v181, v182, v183\n"
"v_mfma_f32_16x16x32_bf16 v[196:199], a[136:139], a[176:179], v[196:199]\n"
"v_mul_f32 v184, s96, v184\n"
"v_mul_f32 v185, s96, v185\n"
"v_mul_f32 v186, s96, v186\n"
"v_mul_f32 v187, s96, v187\n"
"v_mfma_f32_16x16x32_bf16 v[196:199], a[140:143], a[180:183], v[196:199]\n"
"s_add_u32 m0, s46, 61440\n"
"ds_write_b64 v237, v[180:181] offset:6144\n"
"v_cvt_pk_bf16_f32 v184, v184, v185\n"
"v_cvt_pk_bf16_f32 v185, v186, v187\n"
"v_mfma_f32_16x16x32_bf16 v[200:203], a[144:147], a[160:163], v[200:203]\n"
"buffer_load_dwordx4 v252, s[20:23], s44 offen nt lds\n"
"v_mul_f32 v188, s96, v188\n"
"v_mul_f32 v189, s96, v189\n"
"v_mul_f32 v190, s96, v190\n"
"v_mul_f32 v191, s96, v191\n"
"v_mfma_f32_16x16x32_bf16 v[200:203], a[148:151], a[164:167], v[200:203]\n"
"s_add_u32 s44, s44, 4096\n"
"ds_write_b64 v237, v[184:185] offset:10240\n"
"v_cvt_pk_bf16_f32 v188, v188, v189\n"
"v_cvt_pk_bf16_f32 v189, v190, v191\n"
"v_mfma_f32_16x16x32_bf16 v[204:207], a[144:147], a[168:171], v[204:207]\n"
"v_mul_f32 v192, s96, v192\n"
"v_mul_f32 v193, s96, v193\n"
"v_mul_f32 v194, s96, v194\n"
"v_mul_f32 v195, s96, v195\n"
"v_mfma_f32_16x16x32_bf16 v[204:207], a[148:151], a[172:175], v[204:207]\n"
"ds_write_b64 v237, v[188:189] offset:2560\n"
"v_cvt_pk_bf16_f32 v192, v192, v193\n"
"v_cvt_pk_bf16_f32 v193, v194, v195\n"
"v_mfma_f32_16x16x32_bf16 v[208:211], a[144:147], a[176:179], v[208:211]\n"
"v_mul_f32 v196, s96, v196\n"
"v_mul_f32 v197, s96, v197\n"
"v_mul_f32 v198, s96, v198\n"
"v_mul_f32 v199, s96, v199\n"
"v_mfma_f32_16x16x32_bf16 v[208:211], a[148:151], a[180:183], v[208:211]\n"
"ds_write_b64 v237, v[192:193] offset:6656\n"
"v_cvt_pk_bf16_f32 v196, v196, v197\n"
"v_cvt_pk_bf16_f32 v197, v198, v199\n"
"v_mfma_f32_16x16x32_bf16 v[212:215], a[152:155], a[160:163], v[212:215]\n"
"v_mul_f32 v200, s96, v200\n"
"v_mul_f32 v201, s96, v201\n"
"v_mul_f32 v202, s96, v202\n"
"v_mul_f32 v203, s96, v203\n"
"v_mfma_f32_16x16x32_bf16 v[212:215], a[156:159], a[164:167], v[212:215]\n"
"ds_write_b64 v237, v[196:197] offset:10752\n"
"v_cvt_pk_bf16_f32 v200, v200, v201\n"
"v_cvt_pk_bf16_f32 v201, v202, v203\n"
"v_mfma_f32_16x16x32_bf16 v[216:219], a[152:155], a[168:171], v[216:219]\n"
"v_mul_f32 v204, s96, v204\n"
"v_mul_f32 v205, s96, v205\n"
"v_mul_f32 v206, s96, v206\n"
"v_mul_f32 v207, s96, v207\n"
"v_mfma_f32_16x16x32_bf16 v[216:219], a[156:159], a[172:175], v[216:219]\n"
"ds_write_b64 v237, v[200:201] offset:3072\n"
"v_cvt_pk_bf16_f32 v204, v204, v205\n"
"v_cvt_pk_bf16_f32 v205, v206, v207\n"
"v_mfma_f32_16x16x32_bf16 v[220:223], a[152:155], a[176:179], v[220:223]\n"
"v_mul_f32 v208, s96, v208\n"
"v_mul_f32 v209, s96, v209\n"
"v_mul_f32 v210, s96, v210\n"
"v_mul_f32 v211, s96, v211\n"
"v_mfma_f32_16x16x32_bf16 v[220:223], a[156:159], a[180:183], v[220:223]\n"
"ds_write_b64 v237, v[204:205] offset:7168\n"
"v_cvt_pk_bf16_f32 v208, v208, v209\n"
"v_cvt_pk_bf16_f32 v209, v210, v211\n"
"v_mul_f32 v212, s96, v212\n"
"v_mul_f32 v213, s96, v213\n"
"v_mul_f32 v214, s96, v214\n"
"v_mul_f32 v215, s96, v215\n"
"ds_write_b64 v237, v[208:209] offset:11264\n"
"v_cvt_pk_bf16_f32 v212, v212, v213\n"
"v_cvt_pk_bf16_f32 v213, v214, v215\n"
"v_mul_f32 v216, s96, v216\n"
"v_mul_f32 v217, s96, v217\n"
"v_mul_f32 v218, s96, v218\n"
"v_mul_f32 v219, s96, v219\n"
"ds_write_b64 v237, v[212:213] offset:3584\n"
"v_cvt_pk_bf16_f32 v216, v216, v217\n"
"v_cvt_pk_bf16_f32 v217, v218, v219\n"
"v_mul_f32 v220, s96, v220\n"
"v_mul_f32 v221, s96, v221\n"
"v_mul_f32 v222, s96, v222\n"
"v_mul_f32 v223, s96, v223\n"
"ds_write_b64 v237, v[216:217] offset:7680\n"
"v_cvt_pk_bf16_f32 v220, v220, v221\n"
"v_cvt_pk_bf16_f32 v221, v222, v223\n"
"ds_write_b64 v237, v[220:221] offset:11776\n"
"v_mov_b64 v[160:161], 0\n"
"v_mov_b64 v[162:163], 0\n"
"v_mov_b64 v[164:165], 0\n"
"v_mov_b64 v[166:167], 0\n"
"v_mov_b64 v[168:169], 0\n"
"v_mov_b64 v[170:171], 0\n"
"s_waitcnt vmcnt(16) lgkmcnt(0)\n"
"s_barrier\n"
"ds_read_b128 a[32:35], v238 offset:0\n"
"ds_read_b128 a[36:39], v238 offset:1024\n"
"ds_read_b128 a[40:43], v238 offset:2048\n"
"ds_read_b128 a[44:47], v238 offset:3072\n"
"ds_read_b128 a[192:195], v246 offset:0\n"
"ds_read_b128 a[196:199], v247 offset:0\n"
"ds_read_b128 a[200:203], v246 offset:512\n"
"ds_read_b128 a[204:207], v247 offset:512\n"
"ds_read_b128 a[208:211], v246 offset:4096\n"
"ds_read_b128 a[212:215], v247 offset:4096\n"
"ds_read_b128 a[216:219], v246 offset:4608\n"
"ds_read_b128 a[220:223], v247 offset:4608\n"
"ds_read_b128 a[224:227], v246 offset:8192\n"
"ds_read_b128 a[228:231], v247 offset:8192\n"
"ds_read_b128 a[232:235], v246 offset:8704\n"
"ds_read_b128 a[236:239], v247 offset:8704\n"
"ds_read_b128 a[240:243], v246 offset:12288\n"
"ds_read_b128 a[244:247], v247 offset:12288\n"
"ds_read_b128 a[248:251], v246 offset:12800\n"
"ds_read_b128 a[252:255], v247 offset:12800\n"
"s_waitcnt lgkmcnt(12)\n"
"v_mfma_f32_16x16x32_bf16 v[184:187], a[192:195], a[32:35], v[160:163]\n"
"ds_read_b128 a[48:51], v238 offset:4096\n"
"v_mov_b64 v[172:173], 0\n"
"v_mfma_f32_16x16x32_bf16 v[184:187], a[196:199], a[36:39], v[184:187]\n"
"ds_read_b128 a[52:55], v238 offset:5120\n"
"v_mov_b64 v[174:175], 0\n"
"v_mfma_f32_16x16x32_bf16 v[184:187], a[200:203], a[40:43], v[184:187]\n"
"ds_read_b128 a[56:59], v238 offset:6144\n"
"v_mov_b64 v[176:177], 0\n"
"v_mfma_f32_16x16x32_bf16 v[184:187], a[204:207], a[44:47], v[184:187]\n"
"ds_read_b128 a[60:63], v238 offset:7168\n"
"v_mov_b64 v[178:179], 0\n"
"s_waitcnt lgkmcnt(12)\n"
"v_mfma_f32_16x16x32_bf16 v[188:191], a[208:211], a[32:35], v[160:163]\n"
"ds_read_b128 a[64:67], v238 offset:8192\n"
"v_mov_b64 v[180:181], 0\n"
"v_mfma_f32_16x16x32_bf16 v[188:191], a[212:215], a[36:39], v[188:191]\n"
"ds_read_b128 a[68:71], v238 offset:9216\n"
"ds_read_b128 a[72:75], v238 offset:10240\n"
"v_mov_b64 v[182:183], 0\n"
"v_mfma_f32_16x16x32_bf16 v[188:191], a[216:219], a[40:43], v[188:191]\n"
"ds_read_b128 a[76:79], v238 offset:11264\n"
"v_mov_b64 v[128:129], 0\n"
"v_mfma_f32_16x16x32_bf16 v[188:191], a[220:223], a[44:47], v[188:191]\n"
"ds_read_b128 a[80:83], v239 offset:0\n"
"ds_read_b128 a[84:87], v239 offset:1024\n"
"v_mov_b64 v[130:131], 0\n"
"s_waitcnt lgkmcnt(14)\n"
"v_mfma_f32_16x16x32_bf16 v[192:195], a[224:227], a[32:35], v[160:163]\n"
"ds_read_b128 a[88:91], v239 offset:2048\n"
"v_mov_b64 v[132:133], 0\n"
"v_mfma_f32_16x16x32_bf16 v[192:195], a[228:231], a[36:39], v[192:195]\n"
"ds_read_b128 a[92:95], v239 offset:3072\n"
"ds_read_b128 a[96:99], v239 offset:4096\n"
"v_mov_b64 v[134:135], 0\n"
"v_mfma_f32_16x16x32_bf16 v[192:195], a[232:235], a[40:43], v[192:195]\n"
"ds_read_b128 a[100:103], v239 offset:5120\n"
"v_mov_b64 v[136:137], 0\n"
"v_mfma_f32_16x16x32_bf16 v[192:195], a[236:239], a[44:47], v[192:195]\n"
"ds_read_b128 a[104:107], v239 offset:6144\n"
"ds_read_b128 a[108:111], v239 offset:7168\n"
"v_mov_b64 v[138:139], 0\n"
"s_waitcnt lgkmcnt(15)\n"
"v_mfma_f32_16x16x32_bf16 v[196:199], a[240:243], a[32:35], v[160:163]\n"
"ds_read_b128 a[112:115], v239 offset:8192\n"
"v_mov_b64 v[140:141], 0\n"
"v_mfma_f32_16x16x32_bf16 v[196:199], a[244:247], a[36:39], v[196:199]\n"
"ds_read_b128 a[116:119], v239 offset:9216\n"
"ds_read_b128 a[120:123], v239 offset:10240\n"
"v_mov_b64 v[142:143], 0\n"
"v_mfma_f32_16x16x32_bf16 v[196:199], a[248:251], a[40:43], v[196:199]\n"
"ds_read_b128 a[124:127], v239 offset:11264\n"
"v_mov_b64 v[144:145], 0\n"
"v_mfma_f32_16x16x32_bf16 v[196:199], a[252:255], a[44:47], v[196:199]\n"
"v_mov_b64 v[146:147], 0\n"
"v_mov_b64 v[148:149], 0\n"
"v_mov_b64 v[150:151], 0\n"
"v_mov_b64 v[152:153], 0\n"
"v_mov_b64 v[154:155], 0\n"
"v_mov_b64 v[156:157], 0\n"
"v_mov_b64 v[158:159], 0\n"
"v_mov_b32 v236, v240\n"
"v_mov_b32 v237, v240\n"
"v_mov_b32 v238, v240\n"
"s_waitcnt vmcnt(8) lgkmcnt(0)\n"
"s_barrier\n"
"v_mov_b32 v239, v240\n"
"v_mov_b32 v240, 0\n"
"v_mov_b32 v241, 0\n"
"v_mov_b32 v242, 0\n"
"v_mov_b32 v243, 0\n"
"v_mov_b32 v244, 0\n"
"v_mov_b32 v245, 0\n"
"s_memrealtime s[98:99]\n"
"s_waitcnt lgkmcnt(0)\n"
"s_mov_b32 s63, 80\n"
"s_mov_b32 s99, -1\n"
"s_cmp_eq_u32 s40, 0\n"
"s_cselect_b32 s97, 60, -1\n"
"ds_read_b64_tr_b16 a[128:129], v248 offset:32768\n"
"ds_read_b64_tr_b16 a[130:131], v248 offset:36864\n"
"ds_read_b64_tr_b16 a[132:133], v248 offset:40960\n"
"ds_read_b64_tr_b16 a[134:135], v248 offset:45056\n"
"ds_read_b64_tr_b16 a[136:137], v249 offset:32768\n"
"ds_read_b64_tr_b16 a[138:139], v249 offset:36864\n"
"ds_read_b64_tr_b16 a[140:141], v249 offset:40960\n"
"ds_read_b64_tr_b16 a[142:143], v249 offset:45056\n"
"ds_read_b64_tr_b16 a[144:145], v250 offset:32768\n"
"ds_read_b64_tr_b16 a[146:147], v250 offset:36864\n"
"ds_read_b64_tr_b16 a[148:149], v250 offset:40960\n"
"ds_read_b64_tr_b16 a[150:151], v250 offset:45056\n"
"ds_read_b64_tr_b16 a[152:153], v251 offset:32768\n"
"ds_read_b64_tr_b16 a[154:155], v251 offset:36864\n"
"s_getpc_b64 s[66:67]\n"
".Lpc1:\n"
"s_lshr_b32 s53, %2, 3\n"
"s_lshl2_add_u32 s54, s53, s40\n"
"s_lshl_b32 s54, s54, 6\n"
"s_add_u32 s54, s54, .Lend-.Lpc1-24576\n"
"s_add_u32 s66, s66, s54\n"
"s_addc_u32 s67, s67, 0\n"
"s_lshl_b32 s52, s41, 7\n"
"s_add_u32 s52, s52, 33554432\n"
"s_add_u32 s52, s72, s52\n"
"s_addc_u32 s53, s73, 0\n"
"s_not_b32 s54, s74\n"
"v_mov_b32 v224, s54\n"
"v_mov_b32 v225, 0\n"
"s_mov_b64 exec, 1\n"
"global_store_dword v225, v224, s[52:53] sc1\n"
"s_mov_b64 exec, -1\n"
"s_add_u32 m0, s46, 98304\n"
"s_nop 0\n"
"buffer_load_dwordx4 v253, s[24:27], s51 offen nt lds\n"
"s_add_u32 s51, s51, 4096\n"
"s_add_u32 m0, s46, 102400\n"
"s_nop 0\n"
"buffer_load_dwordx4 v253, s[24:27], s51 offen nt lds\n"
"s_add_u32 s51, s51, 4096\n"
"s_add_u32 m0, s46, 106496\n"
"s_nop 0\n"
"buffer_load_dwordx4 v253, s[24:27], s51 offen nt lds\n"
"s_add_u32 s51, s51, 4096\n"
"s_add_u32 m0, s46, 110592\n"
"s_nop 0\n"
"buffer_load_dwordx4 v253, s[24:27], s51 offen nt lds\n"
"s_add_u32 s51, s51, 4096\n"
"v_exp_f32 v232, v184\n"
"v_exp_f32 v233, v185\n"
"v_exp_f32 v234, v186\n"
"v_exp_f32 v235, v187\n"
"s_nop 0\n"
"v_cvt_pk_bf16_f32 v216, v232, v233\n"
"s_nop 0\n"
"v_cvt_pk_bf16_f32 v217, v234, v235\n"
"v_exp_f32 v232, v188\n"
"v_exp_f32 v233, v189\n"
"v_exp_f32 v234, v190\n"
"s_nop 0\n"
"v_cvt_pk_bf16_f32 v218, v232, v233\n"
"v_exp_f32 v235, v191\n"
"s_nop 0\n"
"v_cvt_pk_bf16_f32 v219, v234, v235\n"
"v_exp_f32 v232, v192\n"
"v_exp_f32 v233, v193\n"
"v_exp_f32 v234, v194\n"
"s_nop 0\n"
"v_cvt_pk_bf16_f32 v220, v232, v233\n"
"v_exp_f32 v235, v195\n"
"s_nop 0\n"
"v_cvt_pk_bf16_f32 v221, v234, v235\n"
".Lloop:\n"
"v_mfma_f32_16x16x32_bf16 v[200:203], a[192:195], a[48:51], v[164:167]\n"
"v_exp_f32 v232, v196\n"
"v_mfma_f32_16x16x32_bf16 v[200:203], a[196:199], a[52:55], v[200:203]\n"
"v_exp_f32 v233, v197\n"
"v_mfma_f32_16x16x32_bf16 v[200:203], a[200:203], a[56:59], v[200:203]\n"
"ds_read_b64_tr_b16 a[156:157], v251 offset:40960\n"
"ds_read_b64_tr_b16 a[158:159], v251 offset:45056\n"
"v_mfma_f32_16x16x32_bf16 v[200:203], a[204:207], a[60:63], v[200:203]\n"
"v_exp_f32 v234, v198\n"
"v_mfma_f32_16x16x32_bf16 v[204:207], a[208:211], a[48:51], v[164:167]\n"
"ds_read_b64_tr_b16 a[160:161], v248 offset:33280\n"
"v_cvt_pk_bf16_f32 v222, v232, v233\n"
"v_mfma_f32_16x16x32_bf16 v[204:207], a[212:215], a[52:55], v[204:207]\n"
"v_exp_f32 v235, v199\n"
"v_mfma_f32_16x16x32_bf16 v[204:207], a[216:219], a[56:59], v[204:207]\n"
"ds_read_b64_tr_b16 a[162:163], v248 offset:37376\n"
"v_cvt_pk_bf16_f32 v223, v234, v235\n"
"v_mfma_f32_16x16x32_bf16 v[204:207], a[220:223], a[60:63], v[204:207]\n"
"v_exp_f32 v232, v200\n"
"v_mfma_f32_16x16x32_bf16 v[228:231], v[236:239], v[216:219], 0\n"
"s_add_u32 m0, s46, 114688\n"
"ds_read_b64_tr_b16 a[164:165], v248 offset:41472\n"
"ds_read_b64_tr_b16 a[166:167], v248 offset:45568\n"
"v_mfma_f32_16x16x32_bf16 v[228:231], v[236:239], v[220:223], v[228:231]\n"
"buffer_load_dwordx4 v253, s[24:27], s51 offen nt lds\n"
"v_mfma_f32_16x16x32_bf16 v[208:211], a[224:227], a[48:51], v[164:167]\n"
"s_add_u32 s51, s51, 4096\n"
"ds_read_b64_tr_b16 a[168:169], v249 offset:33280\n"
"ds_read_b64_tr_b16 a[170:171], v249 offset:37376\n"
"v_mfma_f32_16x16x32_bf16 v[208:211], a[228:231], a[52:55], v[208:211]\n"
"v_exp_f32 v233, v201\n"
"v_mfma_f32_16x16x32_bf16 v[208:211], a[232:235], a[56:59], v[208:211]\n"
"ds_read_b64_tr_b16 a[172:173], v249 offset:41472\n"
"ds_read_b64_tr_b16 a[174:175], v249 offset:45568\n"
"v_mfma_f32_16x16x32_bf16 v[208:211], a[236:239], a[60:63], v[208:211]\n"
"v_cmp_nge_f32 vcc, v254, v228\n"
"v_cvt_pk_bf16_f32 v224, v232, v233\n"
"v_mfma_f32_16x16x32_bf16 v[212:215], a[240:243], a[48:51], v[164:167]\n"
"ds_read_b64_tr_b16 a[176:177], v250 offset:33280\n"
"ds_read_b64_tr_b16 a[178:179], v250 offset:37376\n"
"v_mfma_f32_16x16x32_bf16 v[212:215], a[244:247], a[52:55], v[212:215]\n"
"v_exp_f32 v234, v202\n"
"v_mfma_f32_16x16x32_bf16 v[212:215], a[248:251], a[56:59], v[212:215]\n"
"s_waitcnt lgkmcnt(6)\n"
"ds_read_b64_tr_b16 a[180:181], v250 offset:41472\n"
"ds_read_b64_tr_b16 a[182:183], v250 offset:45568\n"
"v_mfma_f32_16x16x32_bf16 v[212:215], a[252:255], a[60:63], v[212:215]\n"
"s_cbranch_vccnz .Lstub_0\n"
".Lback_0:\n"
"v_mfma_f32_16x16x32_bf16 a[0:3], v[216:219], a[128:131], a[0:3]\n"
"ds_read_b64_tr_b16 a[184:185], v251 offset:33280\n"
"ds_read_b64_tr_b16 a[186:187], v251 offset:37376\n"
"v_mfma_f32_16x16x32_bf16 a[0:3], v[220:223], a[132:135], a[0:3]\n"
"v_exp_f32 v235, v203\n"
"v_mfma_f32_16x16x32_bf16 a[4:7], v[216:219], a[136:139], a[4:7]\n"
"ds_read_b64_tr_b16 a[188:189], v251 offset:41472\n"
"v_add_f32 v240, v240, |v228|\n"
"v_mfma_f32_16x16x32_bf16 a[4:7], v[220:223], a[140:143], a[4:7]\n"
"ds_read_b64_tr_b16 a[190:191], v251 offset:45568\n"
"v_cvt_pk_bf16_f32 v225, v234, v235\n"
"v_mfma_f32_16x16x32_bf16 a[8:11], v[216:219], a[144:147], a[8:11]\n"
"v_exp_f32 v232, v204\n"
"v_mfma_f32_16x16x32_bf16 a[8:11], v[220:223], a[148:151], a[8:11]\n"
"v_exp_f32 v233, v205\n"
"v_mfma_f32_16x16x32_bf16 a[12:15], v[216:219], a[152:155], a[12:15]\n"
"v_exp_f32 v234, v206\n"
"v_mfma_f32_16x16x32_bf16 a[12:15], v[220:223], a[156:159], a[12:15]\n"
"v_exp_f32 v235, v207\n"
"v_mfma_f32_16x16x32_bf16 a[16:19], v[216:219], a[160:163], a[16:19]\n"
"v_cvt_pk_bf16_f32 v226, v232, v233\n"
"v_cvt_pk_bf16_f32 v227, v234, v235\n"
"v_mfma_f32_16x16x32_bf16 a[16:19], v[220:223], a[164:167], a[16:19]\n"
"s_waitcnt lgkmcnt(1)\n"
"s_memrealtime s[94:95]\n"
"v_exp_f32 v232, v208\n"
"v_mfma_f32_16x16x32_bf16 a[20:23], v[216:219], a[168:171], a[20:23]\n"
"s_add_u32 m0, s46, 118784\n"
"v_exp_f32 v233, v209\n"
"v_mfma_f32_16x16x32_bf16 a[20:23], v[220:223], a[172:175], a[20:23]\n"
"buffer_load_dwordx4 v253, s[24:27], s51 offen nt lds\n"
"v_mfma_f32_16x16x32_bf16 a[24:27], v[216:219], a[176:179], a[24:27]\n"
"s_add_u32 s51, s51, 4096\n"
"v_exp_f32 v234, v210\n"
"v_mfma_f32_16x16x32_bf16 a[24:27], v[220:223], a[180:183], a[24:27]\n"
"v_exp_f32 v235, v211\n"
"v_mfma_f32_16x16x32_bf16 a[28:31], v[216:219], a[184:187], a[28:31]\n"
"s_waitcnt lgkmcnt(0)\n"
"v_cvt_pk_bf16_f32 v228, v232, v233\n"
"v_cvt_pk_bf16_f32 v229, v234, v235\n"
"v_mfma_f32_16x16x32_bf16 a[28:31], v[220:223], a[188:191], a[28:31]\n"
"s_sub_u32 s94, s94, s98\n"
"s_cmp_lt_i32 s94, 0\n"
"s_cbranch_scc1 .Lpb1\n"
".Lpb1x:\n"
"s_barrier\n"
"v_mfma_f32_16x16x32_bf16 v[184:187], a[192:195], a[64:67], v[168:171]\n"
"s_add_u32 s98, s98, 111\n"
"v_exp_f32 v232, v212\n"
"v_mfma_f32_16x16x32_bf16 v[184:187], a[196:199], a[68:71], v[184:187]\n"
"s_lshr_b32 s95, s63, 3\n"
"v_exp_f32 v233, v213\n"
"v_mfma_f32_16x16x32_bf16 v[184:187], a[200:203], a[72:75], v[184:187]\n"
"s_sub_u32 s63, s63, s95\n"
"v_exp_f32 v234, v214\n"
"v_mfma_f32_16x16x32_bf16 v[184:187], a[204:207], a[76:79], v[184:187]\n"
"s_nop 0\n"
"v_exp_f32 v235, v215\n"
"v_mfma_f32_16x16x32_bf16 v[188:191], a[208:211], a[64:67], v[168:171]\n"
"s_nop 0\n"
"v_cvt_pk_bf16_f32 v230, v232, v233\n"
"v_cvt_pk_bf16_f32 v231, v234, v235\n"
"v_mfma_f32_16x16x32_bf16 v[188:191], a[212:215], a[68:71], v[188:191]\n"
"s_add_u32 s98, s98, s95\n"
"v_mfma_f32_16x16x32_bf16 v[188:191], a[216:219], a[72:75], v[188:191]\n"
"s_sub_u32 s94, s94, 18\n"
"v_exp_f32 v232, v184\n"
"v_mfma_f32_16x16x32_bf16 v[188:191], a[220:223], a[76:79], v[188:191]\n"
"s_max_i32 s94, s94, 0\n"
"v_exp_f32 v233, v185\n"
"v_mfma_f32_16x16x32_bf16 v[220:223], v[236:239], v[224:227], 0\n"
"s_add_u32 m0, s46, 122880\n"
"v_exp_f32 v234, v186\n"
"v_mfma_f32_16x16x32_bf16 v[220:223], v[236:239], v[228:231], v[220:223]\n"
"buffer_load_dwordx4 v253, s[24:27], s51 offen nt lds\n"
"v_mfma_f32_16x16x32_bf16 v[192:195], a[224:227], a[64:67], v[168:171]\n"
"s_add_u32 s51, s51, 4096\n"
"v_exp_f32 v235, v187\n"
"v_mfma_f32_16x16x32_bf16 v[192:195], a[228:231], a[68:71], v[192:195]\n"
"v_cvt_pk_bf16_f32 v216, v232, v233\n"
"v_cvt_pk_bf16_f32 v217, v234, v235\n"
"v_mfma_f32_16x16x32_bf16 v[192:195], a[232:235], a[72:75], v[192:195]\n"
"v_exp_f32 v232, v188\n"
"v_mfma_f32_16x16x32_bf16 v[192:195], a[236:239], a[76:79], v[192:195]\n"
"v_exp_f32 v233, v189\n"
"v_mfma_f32_16x16x32_bf16 v[196:199], a[240:243], a[64:67], v[168:171]\n"
"v_cmp_nge_f32 vcc, v254, v220\n"
"v_cvt_pk_bf16_f32 v218, v232, v233\n"
"v_mfma_f32_16x16x32_bf16 v[196:199], a[244:247], a[68:71], v[196:199]\n"
"v_exp_f32 v234, v190\n"
"v_mfma_f32_16x16x32_bf16 v[196:199], a[248:251], a[72:75], v[196:199]\n"
"v_exp_f32 v235, v191\n"
"v_mfma_f32_16x16x32_bf16 v[196:199], a[252:255], a[76:79], v[196:199]\n"
"s_cbranch_vccnz .Lstub_1\n"
".Lback_1:\n"
"v_mfma_f32_16x16x32_bf16 v[0:3], v[224:227], a[128:131], v[0:3]\n"
"v_add_f32 v241, v241, |v220|\n"
"v_cvt_pk_bf16_f32 v219, v234, v235\n"
"v_mfma_f32_16x16x32_bf16 v[0:3], v[228:231], a[132:135], v[0:3]\n"
"v_exp_f32 v232, v192\n"
"v_mfma_f32_16x16x32_bf16 v[4:7], v[224:227], a[136:139], v[4:7]\n"
"s_add_u32 m0, s46, 126976\n"
"v_exp_f32 v233, v193\n"
"v_mfma_f32_16x16x32_bf16 v[4:7], v[228:231], a[140:143], v[4:7]\n"
"buffer_load_dwordx4 v253, s[24:27], s51 offen nt lds\n"
"v_mfma_f32_16x16x32_bf16 v[8:11], v[224:227], a[144:147], v[8:11]\n"
"s_add_u32 s51, s51, 4096\n"
"v_exp_f32 v234, v194\n"
"v_mfma_f32_16x16x32_bf16 v[8:11], v[228:231], a[148:151], v[8:11]\n"
"v_exp_f32 v235, v195\n"
"v_mfma_f32_16x16x32_bf16 v[12:15], v[224:227], a[152:155], v[12:15]\n"
"v_cvt_pk_bf16_f32 v220, v232, v233\n"
"v_cvt_pk_bf16_f32 v221, v234, v235\n"
"v_mfma_f32_16x16x32_bf16 v[12:15], v[228:231], a[156:159], v[12:15]\n"
"v_exp_f32 v232, v196\n"
"v_mfma_f32_16x16x32_bf16 v[16:19], v[224:227], a[160:163], v[16:19]\n"
"v_exp_f32 v233, v197\n"
"v_mfma_f32_16x16x32_bf16 v[16:19], v[228:231], a[164:167], v[16:19]\n"
"v_exp_f32 v234, v198\n"
"v_mfma_f32_16x16x32_bf16 v[20:23], v[224:227], a[168:171], v[20:23]\n"
"v_exp_f32 v235, v199\n"
"v_mfma_f32_16x16x32_bf16 v[20:23], v[228:231], a[172:175], v[20:23]\n"
"v_cvt_pk_bf16_f32 v222, v232, v233\n"
"v_cvt_pk_bf16_f32 v223, v234, v235\n"
"v_mfma_f32_16x16x32_bf16 v[24:27], v[224:227], a[176:179], v[24:27]\n"
"s_add_u32 m0, s46, 0\n"
"v_mfma_f32_16x16x32_bf16 v[24:27], v[228:231], a[180:183], v[24:27]\n"
"buffer_load_dwordx4 v252, s[20:23], s44 offen nt lds\n"
"v_mfma_f32_16x16x32_bf16 v[28:31], v[224:227], a[184:187], v[28:31]\n"
"s_add_u32 s44, s44, 4096\n"
"v_mfma_f32_16x16x32_bf16 v[28:31], v[228:231], a[188:191], v[28:31]\n"
"s_cmp_gt_u32 s94, s99\n"
"s_cselect_b32 s63, 110, s63\n"
"s_cselect_b32 s95, 100, 0\n"
"v_mfma_f32_16x16x32_bf16 v[200:203], a[192:195], a[80:83], v[172:175]\n"
"s_add_u32 s94, s94, s95\n"
"v_mfma_f32_16x16x32_bf16 v[200:203], a[196:199], a[84:87], v[200:203]\n"
"s_add_u32 s98, s98, s94\n"
"v_mfma_f32_16x16x32_bf16 v[200:203], a[200:203], a[88:91], v[200:203]\n"
"s_add_u32 m0, s46, 4096\n"
"v_mfma_f32_16x16x32_bf16 v[200:203], a[204:207], a[92:95], v[200:203]\n"
"buffer_load_dwordx4 v252, s[20:23], s44 offen nt lds\n"
"v_mfma_f32_16x16x32_bf16 v[204:207], a[208:211], a[80:83], v[172:175]\n"
"s_add_u32 s44, s44, 4096\n"
"v_mfma_f32_16x16x32_bf16 v[204:207], a[212:215], a[84:87], v[204:207]\n"
"s_nop 2\n"
"v_mfma_f32_16x16x32_bf16 v[204:207], a[216:219], a[88:91], v[204:207]\n"
"v_exp_f32 v232, v200\n"
"v_mfma_f32_16x16x32_bf16 v[204:207], a[220:223], a[92:95], v[204:207]\n"
"v_exp_f32 v233, v201\n"
"v_mfma_f32_16x16x32_bf16 v[228:231], v[236:239], v[216:219], 0\n"
"v_exp_f32 v234, v202\n"
"v_mfma_f32_16x16x32_bf16 v[228:231], v[236:239], v[220:223], v[228:231]\n"
"v_exp_f32 v235, v203\n"
"v_mfma_f32_16x16x32_bf16 v[208:211], a[224:227], a[80:83], v[172:175]\n"
"v_cvt_pk_bf16_f32 v224, v232, v233\n"
"v_cvt_pk_bf16_f32 v225, v234, v235\n"
"v_mfma_f32_16x16x32_bf16 v[208:211], a[228:231], a[84:87], v[208:211]\n"
"v_exp_f32 v232, v204\n"
"v_mfma_f32_16x16x32_bf16 v[208:211], a[232:235], a[88:91], v[208:211]\n"
"s_add_u32 m0, s46, 8192\n"
"v_exp_f32 v233, v205\n"
"v_mfma_f32_16x16x32_bf16 v[208:211], a[236:239], a[92:95], v[208:211]\n"
"buffer_load_dwordx4 v252, s[20:23], s44 offen nt lds\n"
"v_mfma_f32_16x16x32_bf16 v[212:215], a[240:243], a[80:83], v[172:175]\n"
"s_add_u32 s44, s44, 4096\n"
"v_cmp_nge_f32 vcc, v254, v228\n"
"v_cvt_pk_bf16_f32 v226, v232, v233\n"
"v_mfma_f32_16x16x32_bf16 v[212:215], a[244:247], a[84:87], v[212:215]\n"
"v_exp_f32 v234, v206\n"
"v_mfma_f32_16x16x32_bf16 v[212:215], a[248:251], a[88:91], v[212:215]\n"
"v_exp_f32 v235, v207\n"
"v_mfma_f32_16x16x32_bf16 v[212:215], a[252:255], a[92:95], v[212:215]\n"
"s_cbranch_vccnz .Lstub_2\n"
".Lback_2:\n"
"v_mfma_f32_16x16x32_bf16 v[32:35], v[216:219], a[128:131], v[32:35]\n"
"v_add_f32 v242, v242, |v228|\n"
"v_cvt_pk_bf16_f32 v227, v234, v235\n"
"v_mfma_f32_16x16x32_bf16 v[32:35], v[220:223], a[132:135], v[32:35]\n"
"v_exp_f32 v232, v208\n"
"v_mfma_f32_16x16x32_bf16 v[36:39], v[216:219], a[136:139], v[36:39]\n"
"v_exp_f32 v233, v209\n"
"v_mfma_f32_16x16x32_bf16 v[36:39], v[220:223], a[140:143], v[36:39]\n"
"v_exp_f32 v234, v210\n"
"v_mfma_f32_16x16x32_bf16 v[40:43], v[216:219], a[144:147], v[40:43]\n"
"s_add_u32 m0, s46, 12288\n"
"v_exp_f32 v235, v211\n"
"v_mfma_f32_16x16x32_bf16 v[40:43], v[220:223], a[148:151], v[40:43]\n"
"buffer_load_dwordx4 v252, s[20:23], s44 offen nt lds\n"
"v_mfma_f32_16x16x32_bf16 v[44:47], v[216:219], a[152:155], v[44:47]\n"
"s_add_u32 s44, s44, 4096\n"
"v_cvt_pk_bf16_f32 v228, v232, v233\n"
"v_cvt_pk_bf16_f32 v229, v234, v235\n"
"v_mfma_f32_16x16x32_bf16 v[44:47], v[220:223], a[156:159], v[44:47]\n"
"v_exp_f32 v232, v212\n"
"v_mfma_f32_16x16x32_bf16 v[48:51], v[216:219], a[160:163], v[48:51]\n"
"v_exp_f32 v233, v213\n"
"v_mfma_f32_16x16x32_bf16 v[48:51], v[220:223], a[164:167], v[48:51]\n"
"v_exp_f32 v234, v214\n"
"v_mfma_f32_16x16x32_bf16 v[52:55], v[216:219], a[168:171], v[52:55]\n"
"v_exp_f32 v235, v215\n"
"v_mfma_f32_16x16x32_bf16 v[52:55], v[220:223], a[172:175], v[52:55]\n"
"v_cvt_pk_bf16_f32 v230, v232, v233\n"
"v_cvt_pk_bf16_f32 v231, v234, v235\n"
"v_mfma_f32_16x16x32_bf16 v[56:59], v[216:219], a[176:179], v[56:59]\n"
"v_mfma_f32_16x16x32_bf16 v[56:59], v[220:223], a[180:183], v[56:59]\n"
"v_mfma_f32_16x16x32_bf16 v[60:63], v[216:219], a[184:187], v[60:63]\n"
"s_add_u32 m0, s46, 16384\n"
"v_mfma_f32_16x16x32_bf16 v[60:63], v[220:223], a[188:191], v[60:63]\n"
"buffer_load_dwordx4 v252, s[20:23], s44 offen nt lds\n"
"v_mfma_f32_16x16x32_bf16 v[184:187], a[192:195], a[96:99], v[176:179]\n"
"s_add_u32 s44, s44, 4096\n"
"v_mfma_f32_16x16x32_bf16 v[184:187], a[196:199], a[100:103], v[184:187]\n"
"v_mfma_f32_16x16x32_bf16 v[184:187], a[200:203], a[104:107], v[184:187]\n"
"v_mfma_f32_16x16x32_bf16 v[184:187], a[204:207], a[108:111], v[184:187]\n"
"s_nop 2\n"
"v_mfma_f32_16x16x32_bf16 v[188:191], a[208:211], a[96:99], v[176:179]\n"
"s_nop 0\n"
"v_mfma_f32_16x16x32_bf16 v[188:191], a[212:215], a[100:103], v[188:191]\n"
"v_mfma_f32_16x16x32_bf16 v[188:191], a[216:219], a[104:107], v[188:191]\n"
"s_add_u32 m0, s46, 20480\n"
"v_exp_f32 v232, v184\n"
"v_mfma_f32_16x16x32_bf16 v[188:191], a[220:223], a[108:111], v[188:191]\n"
"buffer_load_dwordx4 v252, s[20:23], s44 offen nt lds\n"
"v_mfma_f32_16x16x32_bf16 v[220:223], v[236:239], v[224:227], 0\n"
"s_add_u32 s44, s44, 4096\n"
"v_exp_f32 v233, v185\n"
"v_mfma_f32_16x16x32_bf16 v[220:223], v[236:239], v[228:231], v[220:223]\n"
"v_exp_f32 v234, v186\n"
"v_mfma_f32_16x16x32_bf16 v[192:195], a[224:227], a[96:99], v[176:179]\n"
"v_exp_f32 v235, v187\n"
"v_mfma_f32_16x16x32_bf16 v[192:195], a[228:231], a[100:103], v[192:195]\n"
"v_cvt_pk_bf16_f32 v216, v232, v233\n"
"v_cvt_pk_bf16_f32 v217, v234, v235\n"
"v_mfma_f32_16x16x32_bf16 v[192:195], a[232:235], a[104:107], v[192:195]\n"
"v_exp_f32 v232, v188\n"
"v_mfma_f32_16x16x32_bf16 v[192:195], a[236:239], a[108:111], v[192:195]\n"
"v_exp_f32 v233, v189\n"
"v_mfma_f32_16x16x32_bf16 v[196:199], a[240:243], a[96:99], v[176:179]\n"
"v_cmp_nge_f32 vcc, v254, v220\n"
"v_cvt_pk_bf16_f32 v218, v232, v233\n"
"v_mfma_f32_16x16x32_bf16 v[196:199], a[244:247], a[100:103], v[196:199]\n"
"v_exp_f32 v234, v190\n"
"v_mfma_f32_16x16x32_bf16 v[196:199], a[248:251], a[104:107], v[196:199]\n"
"v_exp_f32 v235, v191\n"
"v_mfma_f32_16x16x32_bf16 v[196:199], a[252:255], a[108:111], v[196:199]\n"
"s_cbranch_vccnz .Lstub_3\n"
".Lback_3:\n"
"v_mfma_f32_16x16x32_bf16 v[64:67], v[224:227], a[128:131], v[64:67]\n"
"s_add_u32 m0, s46, 24576\n"
"v_add_f32 v243, v243, |v220|\n"
"v_cvt_pk_bf16_f32 v219, v234, v235\n"
"v_mfma_f32_16x16x32_bf16 v[64:67], v[228:231], a[132:135], v[64:67]\n"
"buffer_load_dwordx4 v252, s[20:23], s44 offen nt lds\n"
"v_mfma_f32_16x16x32_bf16 v[68:71], v[224:227], a[136:139], v[68:71]\n"
"s_add_u32 s44, s44, 4096\n"
"v_exp_f32 v232, v192\n"
"v_mfma_f32_16x16x32_bf16 v[68:71], v[228:231], a[140:143], v[68:71]\n"
"v_exp_f32 v233, v193\n"
"v_mfma_f32_16x16x32_bf16 v[72:75], v[224:227], a[144:147], v[72:75]\n"
"v_exp_f32 v234, v194\n"
"v_mfma_f32_16x16x32_bf16 v[72:75], v[228:231], a[148:151], v[72:75]\n"
"v_exp_f32 v235, v195\n"
"v_mfma_f32_16x16x32_bf16 v[76:79], v[224:227], a[152:155], v[76:79]\n"
"v_cvt_pk_bf16_f32 v220, v232, v233\n"
"v_cvt_pk_bf16_f32 v221, v234, v235\n"
"v_mfma_f32_16x16x32_bf16 v[76:79], v[228:231], a[156:159], v[76:79]\n"
"v_exp_f32 v232, v196\n"
"v_mfma_f32_16x16x32_bf16 v[80:83], v[224:227], a[160:163], v[80:83]\n"
"v_exp_f32 v233, v197\n"
"v_mfma_f32_16x16x32_bf16 v[80:83], v[228:231], a[164:167], v[80:83]\n"
"s_memrealtime s[94:95]\n"
"v_exp_f32 v234, v198\n"
"v_mfma_f32_16x16x32_bf16 v[84:87], v[224:227], a[168:171], v[84:87]\n"
"s_add_u32 m0, s46, 28672\n"
"v_exp_f32 v235, v199\n"
"v_mfma_f32_16x16x32_bf16 v[84:87], v[228:231], a[172:175], v[84:87]\n"
"buffer_load_dwordx4 v252, s[20:23], s44 offen nt lds\n"
"v_mfma_f32_16x16x32_bf16 v[88:91], v[224:227], a[176:179], v[88:91]\n"
"s_add_u32 s44, s44, 4096\n"
"v_cvt_pk_bf16_f32 v222, v232, v233\n"
"v_cvt_pk_bf16_f32 v223, v234, v235\n"
"v_mfma_f32_16x16x32_bf16 v[88:91], v[228:231], a[180:183], v[88:91]\n"
"s_waitcnt lgkmcnt(0)\n"
"s_sub_u32 s94, s94, s98\n"
"s_cmp_lt_i32 s94, 0\n"
"v_mfma_f32_16x16x32_bf16 v[92:95], v[224:227], a[184:187], v[92:95]\n"
"s_cbranch_scc1 .Lpa0\n"
".Lpa0x:\n"
"v_mfma_f32_16x16x32_bf16 v[92:95], v[228:231], a[188:191], v[92:95]\n"
"s_waitcnt vmcnt(16)\n"
"s_barrier\n"
"v_mfma_f32_16x16x32_bf16 v[200:203], a[192:195], a[112:115], v[180:183]\n"
"s_add_u32 s98, s98, 111\n"
"ds_read_b128 a[192:195], v246 offset:32768\n"
"v_mfma_f32_16x16x32_bf16 v[200:203], a[196:199], a[116:119], v[200:203]\n"
"s_lshr_b32 s95, s63, 3\n"
"ds_read_b128 a[196:199], v247 offset:32768\n"
"v_mfma_f32_16x16x32_bf16 v[200:203], a[200:203], a[120:123], v[200:203]\n"
"s_sub_u32 s63, s63, s95\n"
"ds_read_b128 a[200:203], v246 offset:33280\n"
"v_mfma_f32_16x16x32_bf16 v[200:203], a[204:207], a[124:127], v[200:203]\n"
"s_nop 0\n"
"ds_read_b128 a[204:207], v247 offset:33280\n"
"v_mfma_f32_16x16x32_bf16 v[204:207], a[208:211], a[112:115], v[180:183]\n"
"s_add_u32 s98, s98, s95\n"
"ds_read_b128 a[208:211], v246 offset:36864\n"
"v_mfma_f32_16x16x32_bf16 v[204:207], a[212:215], a[116:119], v[204:207]\n"
"s_sub_u32 s94, s94, 18\n"
"ds_read_b128 a[212:215], v247 offset:36864\n"
"v_mfma_f32_16x16x32_bf16 v[204:207], a[216:219], a[120:123], v[204:207]\n"
"s_add_u32 m0, s46, 131072\n"
"v_exp_f32 v232, v200\n"
"v_mfma_f32_16x16x32_bf16 v[204:207], a[220:223], a[124:127], v[204:207]\n"
"buffer_load_dwordx4 v253, s[24:27], s51 offen nt lds\n"
"v_mfma_f32_16x16x32_bf16 v[228:231], v[236:239], v[216:219], 0\n"
"s_add_u32 s51, s51, 4096\n"
"v_exp_f32 v233, v201\n"
"v_mfma_f32_16x16x32_bf16 v[228:231], v[236:239], v[220:223], v[228:231]\n"
"s_max_i32 s94, s94, 0\n"
"v_exp_f32 v234, v202\n"
"v_mfma_f32_16x16x32_bf16 v[208:211], a[224:227], a[112:115], v[180:183]\n"
"ds_read_b128 a[216:219], v246 offset:37376\n"
"v_cvt_pk_bf16_f32 v224, v232, v233\n"
"v_mfma_f32_16x16x32_bf16 v[208:211], a[228:231], a[116:119], v[208:211]\n"
"v_exp_f32 v235, v203\n"
"v_mfma_f32_16x16x32_bf16 v[208:211], a[232:235], a[120:123], v[208:211]\n"
"ds_read_b128 a[220:223], v247 offset:37376\n"
"v_cvt_pk_bf16_f32 v225, v234, v235\n"
"v_mfma_f32_16x16x32_bf16 v[208:211], a[236:239], a[124:127], v[208:211]\n"
"ds_read_b128 a[224:227], v246 offset:40960\n"
"v_cmp_nge_f32 vcc, v254, v228\n"
"v_mfma_f32_16x16x32_bf16 v[212:215], a[240:243], a[112:115], v[180:183]\n"
"v_exp_f32 v232, v204\n"
"v_mfma_f32_16x16x32_bf16 v[212:215], a[244:247], a[116:119], v[212:215]\n"
"v_exp_f32 v233, v205\n"
"v_mfma_f32_16x16x32_bf16 v[212:215], a[248:251], a[120:123], v[212:215]\n"
"v_exp_f32 v234, v206\n"
"v_mfma_f32_16x16x32_bf16 v[212:215], a[252:255], a[124:127], v[212:215]\n"
"s_cbranch_vccnz .Lstub_4\n"
".Lback_4:\n"
"v_mfma_f32_16x16x32_bf16 v[96:99], v[216:219], a[128:131], v[96:99]\n"
"v_add_f32 v244, v244, |v228|\n"
"v_cvt_pk_bf16_f32 v226, v232, v233\n"
"v_mfma_f32_16x16x32_bf16 v[96:99], v[220:223], a[132:135], v[96:99]\n"
"v_exp_f32 v235, v207\n"
"v_mfma_f32_16x16x32_bf16 v[100:103], v[216:219], a[136:139], v[100:103]\n"
"s_add_u32 m0, s46, 135168\n"
"ds_read_b128 a[228:231], v247 offset:40960\n"
"v_cvt_pk_bf16_f32 v227, v234, v235\n"
"v_mfma_f32_16x16x32_bf16 v[100:103], v[220:223], a[140:143], v[100:103]\n"
"buffer_load_dwordx4 v253, s[24:27], s51 offen nt lds\n"
"v_mfma_f32_16x16x32_bf16 v[104:107], v[216:219], a[144:147], v[104:107]\n"
"s_add_u32 s51, s51, 4096\n"
"v_exp_f32 v232, v208\n"
"v_mfma_f32_16x16x32_bf16 v[104:107], v[220:223], a[148:151], v[104:107]\n"
"v_exp_f32 v233, v209\n"
"v_mfma_f32_16x16x32_bf16 v[108:111], v[216:219], a[152:155], v[108:111]\n"
"v_exp_f32 v234, v210\n"
"v_mfma_f32_16x16x32_bf16 v[108:111], v[220:223], a[156:159], v[108:111]\n"
"ds_read_b128 a[232:235], v246 offset:41472\n"
"v_cvt_pk_bf16_f32 v228, v232, v233\n"
"v_mfma_f32_16x16x32_bf16 v[112:115], v[216:219], a[160:163], v[112:115]\n"
"v_exp_f32 v235, v211\n"
"v_mfma_f32_16x16x32_bf16 v[112:115], v[220:223], a[164:167], v[112:115]\n"
"ds_read_b128 a[236:239], v247 offset:41472\n"
"v_cvt_pk_bf16_f32 v229, v234, v235\n"
"v_mfma_f32_16x16x32_bf16 v[116:119], v[216:219], a[168:171], v[116:119]\n"
"v_exp_f32 v232, v212\n"
"v_mfma_f32_16x16x32_bf16 v[116:119], v[220:223], a[172:175], v[116:119]\n"
"v_exp_f32 v233, v213\n"
"v_mfma_f32_16x16x32_bf16 v[120:123], v[216:219], a[176:179], v[120:123]\n"
"v_exp_f32 v234, v214\n"
"v_mfma_f32_16x16x32_bf16 v[120:123], v[220:223], a[180:183], v[120:123]\n"
"s_waitcnt lgkmcnt(2)\n"
"ds_read_b128 a[240:243], v246 offset:45056\n"
"v_cvt_pk_bf16_f32 v230, v232, v233\n"
"v_mfma_f32_16x16x32_bf16 v[124:127], v[216:219], a[184:187], v[124:127]\n"
"s_add_u32 m0, s46, 139264\n"
"v_exp_f32 v235, v215\n"
"v_mfma_f32_16x16x32_bf16 v[124:127], v[220:223], a[188:191], v[124:127]\n"
"buffer_load_dwordx4 v253, s[24:27], s51 offen nt lds\n"
"v_mfma_f32_16x16x32_bf16 v[184:187], a[192:195], a[32:35], v[160:163]\n"
"s_add_u32 s51, s51, 4096\n"
"ds_read_b128 a[244:247], v247 offset:45056\n"
"ds_read_b128 a[248:251], v246 offset:45568\n"
"v_mfma_f32_16x16x32_bf16 v[184:187], a[196:199], a[36:39], v[184:187]\n"
"v_cvt_pk_bf16_f32 v231, v234, v235\n"
"v_mfma_f32_16x16x32_bf16 v[184:187], a[200:203], a[40:43], v[184:187]\n"
"ds_read_b128 a[252:255], v247 offset:45568\n"
"v_mfma_f32_16x16x32_bf16 v[184:187], a[204:207], a[44:47], v[184:187]\n"
"s_nop 2\n"
"v_mfma_f32_16x16x32_bf16 v[188:191], a[208:211], a[32:35], v[160:163]\n"
"s_nop 0\n"
"v_mfma_f32_16x16x32_bf16 v[188:191], a[212:215], a[36:39], v[188:191]\n"
"s_cmp_gt_u32 s94, s99\n"
"s_cselect_b32 s63, 110, s63\n"
"s_cselect_b32 s95, 100, 0\n"
"v_mfma_f32_16x16x32_bf16 v[188:191], a[216:219], a[40:43], v[188:191]\n"
"s_add_u32 m0, s46, 143360\n"
"v_exp_f32 v232, v184\n"
"v_mfma_f32_16x16x32_bf16 v[188:191], a[220:223], a[44:47], v[188:191]\n"
"buffer_load_dwordx4 v253, s[24:27], s51 offen nt lds\n"
"v_mfma_f32_16x16x32_bf16 v[220:223], v[236:239], v[224:227], 0\n"
"s_add_u32 s51, s51, 4096\n"
"v_exp_f32 v233, v185\n"
"v_mfma_f32_16x16x32_bf16 v[220:223], v[236:239], v[228:231], v[220:223]\n"
"s_add_u32 s94, s94, s95\n"
"v_exp_f32 v234, v186\n"
"v_mfma_f32_16x16x32_bf16 v[192:195], a[224:227], a[32:35], v[160:163]\n"
"s_add_u32 s98, s98, s94\n"
"v_exp_f32 v235, v187\n"
"v_mfma_f32_16x16x32_bf16 v[192:195], a[228:231], a[36:39], v[192:195]\n"
"s_waitcnt lgkmcnt(0)\n"
"v_cvt_pk_bf16_f32 v216, v232, v233\n"
"v_cvt_pk_bf16_f32 v217, v234, v235\n"
"v_mfma_f32_16x16x32_bf16 v[192:195], a[232:235], a[40:43], v[192:195]\n"
"v_exp_f32 v232, v188\n"
"v_mfma_f32_16x16x32_bf16 v[192:195], a[236:239], a[44:47], v[192:195]\n"
"s_waitcnt vmcnt(12) lgkmcnt(0)\n"
"s_barrier\n"
"v_mfma_f32_16x16x32_bf16 v[196:199], a[240:243], a[32:35], v[160:163]\n"
"v_cmp_nge_f32 vcc, v254, v220\n"
"v_mfma_f32_16x16x32_bf16 v[196:199], a[244:247], a[36:39], v[196:199]\n"
"v_exp_f32 v233, v189\n"
"v_mfma_f32_16x16x32_bf16 v[196:199], a[248:251], a[40:43], v[196:199]\n"
"v_exp_f32 v234, v190\n"
"v_mfma_f32_16x16x32_bf16 v[196:199], a[252:255], a[44:47], v[196:199]\n"
"s_cbranch_vccnz .Lstub_5\n"
".Lback_5:\n"
"v_mfma_f32_16x16x32_bf16 v[128:131], v[224:227], a[128:131], v[128:131]\n"
"ds_read_b64_tr_b16 a[128:129], v248 offset:0\n"
"ds_read_b64_tr_b16 a[130:131], v248 offset:4096\n"
"v_mfma_f32_16x16x32_bf16 v[128:131], v[228:231], a[132:135], v[128:131]\n"
"s_mov_b32 s43, 0\n"
"v_add_f32 v245, v245, |v220|\n"
"v_cvt_pk_bf16_f32 v218, v232, v233\n"
"v_mfma_f32_16x16x32_bf16 v[132:135], v[224:227], a[136:139], v[132:135]\n"
"ds_read_b64_tr_b16 a[132:133], v248 offset:8192\n"
"ds_read_b64_tr_b16 a[134:135], v248 offset:12288\n"
"v_mfma_f32_16x16x32_bf16 v[132:135], v[228:231], a[140:143], v[132:135]\n"
"v_exp_f32 v235, v191\n"
"v_mfma_f32_16x16x32_bf16 v[136:139], v[224:227], a[144:147], v[136:139]\n"
"ds_read_b64_tr_b16 a[136:137], v249 offset:0\n"
"ds_read_b64_tr_b16 a[138:139], v249 offset:4096\n"
"v_mfma_f32_16x16x32_bf16 v[136:139], v[228:231], a[148:151], v[136:139]\n"
"v_cvt_pk_bf16_f32 v219, v234, v235\n"
"v_mov_b32 v254, s42\n"
"v_mfma_f32_16x16x32_bf16 v[140:143], v[224:227], a[152:155], v[140:143]\n"
"ds_read_b64_tr_b16 a[140:141], v249 offset:8192\n"
"ds_read_b64_tr_b16 a[142:143], v249 offset:12288\n"
"v_mfma_f32_16x16x32_bf16 v[140:143], v[228:231], a[156:159], v[140:143]\n"
"v_exp_f32 v232, v192\n"
"v_mfma_f32_16x16x32_bf16 v[144:147], v[224:227], a[160:163], v[144:147]\n"
"ds_read_b64_tr_b16 a[144:145], v250 offset:0\n"
"ds_read_b64_tr_b16 a[146:147], v250 offset:4096\n"
"v_mfma_f32_16x16x32_bf16 v[144:147], v[228:231], a[164:167], v[144:147]\n"
"v_exp_f32 v233, v193\n"
"v_mfma_f32_16x16x32_bf16 v[148:151], v[224:227], a[168:171], v[148:151]\n"
"ds_read_b64_tr_b16 a[148:149], v250 offset:8192\n"
"ds_read_b64_tr_b16 a[150:151], v250 offset:12288\n"
"v_mfma_f32_16x16x32_bf16 v[148:151], v[228:231], a[172:175], v[148:151]\n"
"v_exp_f32 v234, v194\n"
"v_mfma_f32_16x16x32_bf16 v[152:155], v[224:227], a[176:179], v[152:155]\n"
"ds_read_b64_tr_b16 a[152:153], v251 offset:0\n"
"v_cvt_pk_bf16_f32 v220, v232, v233\n"
"v_mfma_f32_16x16x32_bf16 v[152:155], v[228:231], a[180:183], v[152:155]\n"
"v_exp_f32 v235, v195\n"
"v_mfma_f32_16x16x32_bf16 v[156:159], v[224:227], a[184:187], v[156:159]\n"
"ds_read_b64_tr_b16 a[154:155], v251 offset:4096\n"
"v_cvt_pk_bf16_f32 v221, v234, v235\n"
"v_mfma_f32_16x16x32_bf16 v[156:159], v[228:231], a[188:191], v[156:159]\n"
"v_exp_f32 v232, v196\n"
"v_mfma_f32_16x16x32_bf16 v[200:203], a[192:195], a[48:51], v[164:167]\n"
"v_exp_f32 v233, v197\n"
"v_mfma_f32_16x16x32_bf16 v[200:203], a[196:199], a[52:55], v[200:203]\n"
"ds_read_b64_tr_b16 a[156:157], v251 offset:8192\n"
"ds_read_b64_tr_b16 a[158:159], v251 offset:12288\n"
"v_mfma_f32_16x16x32_bf16 v[200:203], a[200:203], a[56:59], v[200:203]\n"
"v_exp_f32 v234, v198\n"
"v_mfma_f32_16x16x32_bf16 v[200:203], a[204:207], a[60:63], v[200:203]\n"
"ds_read_b64_tr_b16 a[160:161], v248 offset:512\n"
"v_cvt_pk_bf16_f32 v222, v232, v233\n"
"v_mfma_f32_16x16x32_bf16 v[204:207], a[208:211], a[48:51], v[164:167]\n"
"v_exp_f32 v235, v199\n"
"v_mfma_f32_16x16x32_bf16 v[204:207], a[212:215], a[52:55], v[204:207]\n"
"ds_read_b64_tr_b16 a[162:163], v248 offset:4608\n"
"v_cvt_pk_bf16_f32 v223, v234, v235\n"
"v_mfma_f32_16x16x32_bf16 v[204:207], a[216:219], a[56:59], v[204:207]\n"
"v_exp_f32 v232, v200\n"
"v_mfma_f32_16x16x32_bf16 v[204:207], a[220:223], a[60:63], v[204:207]\n"
"ds_read_b64_tr_b16 a[164:165], v248 offset:8704\n"
"ds_read_b64_tr_b16 a[166:167], v248 offset:12800\n"
"v_mfma_f32_16x16x32_bf16 v[228:231], v[236:239], v[216:219], 0\n"
"s_add_u32 m0, s46, 147456\n"
"v_exp_f32 v233, v201\n"
"v_mfma_f32_16x16x32_bf16 v[228:231], v[236:239], v[220:223], v[228:231]\n"
"buffer_load_dwordx4 v253, s[24:27], s51 offen nt lds\n"
"v_mfma_f32_16x16x32_bf16 v[208:211], a[224:227], a[48:51], v[164:167]\n"
"s_add_u32 s51, s51, 4096\n"
"ds_read_b64_tr_b16 a[168:169], v249 offset:512\n"
"ds_read_b64_tr_b16 a[170:171], v249 offset:4608\n"
"v_mfma_f32_16x16x32_bf16 v[208:211], a[228:231], a[52:55], v[208:211]\n"
"v_exp_f32 v234, v202\n"
"v_mfma_f32_16x16x32_bf16 v[208:211], a[232:235], a[56:59], v[208:211]\n"
"ds_read_b64_tr_b16 a[172:173], v249 offset:8704\n"
"ds_read_b64_tr_b16 a[174:175], v249 offset:12800\n"
"v_mfma_f32_16x16x32_bf16 v[208:211], a[236:239], a[60:63], v[208:211]\n"
"v_cmp_nge_f32 vcc, v254, v228\n"
"v_cvt_pk_bf16_f32 v224, v232, v233\n"
"v_mfma_f32_16x16x32_bf16 v[212:215], a[240:243], a[48:51], v[164:167]\n"
"ds_read_b64_tr_b16 a[176:177], v250 offset:512\n"
"ds_read_b64_tr_b16 a[178:179], v250 offset:4608\n"
"v_mfma_f32_16x16x32_bf16 v[212:215], a[244:247], a[52:55], v[212:215]\n"
"v_exp_f32 v235, v203\n"
"v_mfma_f32_16x16x32_bf16 v[212:215], a[248:251], a[56:59], v[212:215]\n"
"s_waitcnt lgkmcnt(6)\n"
"ds_read_b64_tr_b16 a[180:181], v250 offset:8704\n"
"ds_read_b64_tr_b16 a[182:183], v250 offset:12800\n"
"v_mfma_f32_16x16x32_bf16 v[212:215], a[252:255], a[60:63], v[212:215]\n"
"s_cbranch_vccnz .Lstub_6\n"
".Lback_6:\n"
"v_mfma_f32_16x16x32_bf16 a[0:3], v[216:219], a[128:131], a[0:3]\n"
"ds_read_b64_tr_b16 a[184:185], v251 offset:512\n"
"ds_read_b64_tr_b16 a[186:187], v251 offset:4608\n"
"v_mfma_f32_16x16x32_bf16 a[0:3], v[220:223], a[132:135], a[0:3]\n"
"v_add_f32 v240, v240, |v228|\n"
"v_cvt_pk_bf16_f32 v225, v234, v235\n"
"v_mfma_f32_16x16x32_bf16 a[4:7], v[216:219], a[136:139], a[4:7]\n"
"ds_read_b64_tr_b16 a[188:189], v251 offset:8704\n"
"ds_read_b64_tr_b16 a[190:191], v251 offset:12800\n"
"v_mfma_f32_16x16x32_bf16 a[4:7], v[220:223], a[140:143], a[4:7]\n"
"v_exp_f32 v232, v204\n"
"v_mfma_f32_16x16x32_bf16 a[8:11], v[216:219], a[144:147], a[8:11]\n"
"v_exp_f32 v233, v205\n"
"v_mfma_f32_16x16x32_bf16 a[8:11], v[220:223], a[148:151], a[8:11]\n"
"v_exp_f32 v234, v206\n"
"v_mfma_f32_16x16x32_bf16 a[12:15], v[216:219], a[152:155], a[12:15]\n"
"v_exp_f32 v235, v207\n"
"v_mfma_f32_16x16x32_bf16 a[12:15], v[220:223], a[156:159], a[12:15]\n"
"v_cvt_pk_bf16_f32 v226, v232, v233\n"
"v_cvt_pk_bf16_f32 v227, v234, v235\n"
"v_mfma_f32_16x16x32_bf16 a[16:19], v[216:219], a[160:163], a[16:19]\n"
"v_exp_f32 v232, v208\n"
"v_mfma_f32_16x16x32_bf16 a[16:19], v[220:223], a[164:167], a[16:19]\n"
"s_waitcnt lgkmcnt(0)\n"
"s_memrealtime s[94:95]\n"
"v_exp_f32 v233, v209\n"
"v_mfma_f32_16x16x32_bf16 a[20:23], v[216:219], a[168:171], a[20:23]\n"
"s_add_u32 m0, s46, 151552\n"
"v_exp_f32 v234, v210\n"
"v_mfma_f32_16x16x32_bf16 a[20:23], v[220:223], a[172:175], a[20:23]\n"
"buffer_load_dwordx4 v253, s[24:27], s51 offen nt lds\n"
"v_mfma_f32_16x16x32_bf16 a[24:27], v[216:219], a[176:179], a[24:27]\n"
"s_add_u32 s51, s51, 4096\n"
"v_exp_f32 v235, v211\n"
"v_mfma_f32_16x16x32_bf16 a[24:27], v[220:223], a[180:183], a[24:27]\n"
"s_waitcnt lgkmcnt(0)\n"
"v_cvt_pk_bf16_f32 v228, v232, v233\n"
"v_cvt_pk_bf16_f32 v229, v234, v235\n"
"v_mfma_f32_16x16x32_bf16 a[28:31], v[216:219], a[184:187], a[28:31]\n"
"s_sub_u32 s94, s94, s98\n"
"v_exp_f32 v232, v212\n"
"v_mfma_f32_16x16x32_bf16 a[28:31], v[220:223], a[188:191], a[28:31]\n"
"s_cmp_lt_i32 s94, 0\n"
"s_cbranch_scc1 .Lpb0\n"
".Lpb0x:\n"
"s_barrier\n"
"v_mfma_f32_16x16x32_bf16 v[184:187], a[192:195], a[64:67], v[168:171]\n"
"s_add_u32 s98, s98, 111\n"
"v_exp_f32 v233, v213\n"
"v_mfma_f32_16x16x32_bf16 v[184:187], a[196:199], a[68:71], v[184:187]\n"
"s_lshr_b32 s95, s63, 3\n"
"v_exp_f32 v234, v214\n"
"v_mfma_f32_16x16x32_bf16 v[184:187], a[200:203], a[72:75], v[184:187]\n"
"s_sub_u32 s63, s63, s95\n"
"v_exp_f32 v235, v215\n"
"v_mfma_f32_16x16x32_bf16 v[184:187], a[204:207], a[76:79], v[184:187]\n"
"s_nop 0\n"
"v_cvt_pk_bf16_f32 v230, v232, v233\n"
"v_cvt_pk_bf16_f32 v231, v234, v235\n"
"v_mfma_f32_16x16x32_bf16 v[188:191], a[208:211], a[64:67], v[168:171]\n"
"s_nop 1\n"
"v_mfma_f32_16x16x32_bf16 v[188:191], a[212:215], a[68:71], v[188:191]\n"
"s_add_u32 s98, s98, s95\n"
"v_mfma_f32_16x16x32_bf16 v[188:191], a[216:219], a[72:75], v[188:191]\n"
"s_sub_u32 s94, s94, 18\n"
"v_exp_f32 v232, v184\n"
"v_mfma_f32_16x16x32_bf16 v[188:191], a[220:223], a[76:79], v[188:191]\n"
"s_max_i32 s94, s94, 0\n"
"v_exp_f32 v233, v185\n"
"v_mfma_f32_16x16x32_bf16 v[220:223], v[236:239], v[224:227], 0\n"
"s_add_u32 m0, s46, 155648\n"
"v_exp_f32 v234, v186\n"
"v_mfma_f32_16x16x32_bf16 v[220:223], v[236:239], v[228:231], v[220:223]\n"
"buffer_load_dwordx4 v253, s[24:27], s51 offen nt lds\n"
"v_mfma_f32_16x16x32_bf16 v[192:195], a[224:227], a[64:67], v[168:171]\n"
"s_add_u32 s51, s51, 4096\n"
"v_exp_f32 v235, v187\n"
"v_mfma_f32_16x16x32_bf16 v[192:195], a[228:231], a[68:71], v[192:195]\n"
"v_cvt_pk_bf16_f32 v216, v232, v233\n"
"v_cvt_pk_bf16_f32 v217, v234, v235\n"
"v_mfma_f32_16x16x32_bf16 v[192:195], a[232:235], a[72:75], v[192:195]\n"
"v_exp_f32 v232, v188\n"
"v_mfma_f32_16x16x32_bf16 v[192:195], a[236:239], a[76:79], v[192:195]\n"
"v_exp_f32 v233, v189\n"
"v_mfma_f32_16x16x32_bf16 v[196:199], a[240:243], a[64:67], v[168:171]\n"
"v_cmp_nge_f32 vcc, v254, v220\n"
"v_cvt_pk_bf16_f32 v218, v232, v233\n"
"v_mfma_f32_16x16x32_bf16 v[196:199], a[244:247], a[68:71], v[196:199]\n"
"v_exp_f32 v234, v190\n"
"v_mfma_f32_16x16x32_bf16 v[196:199], a[248:251], a[72:75], v[196:199]\n"
"v_exp_f32 v235, v191\n"
"v_mfma_f32_16x16x32_bf16 v[196:199], a[252:255], a[76:79], v[196:199]\n"
"s_cbranch_vccnz .Lstub_7\n"
".Lback_7:\n"
"v_mfma_f32_16x16x32_bf16 v[0:3], v[224:227], a[128:131], v[0:3]\n"
"v_add_f32 v241, v241, |v220|\n"
"v_cvt_pk_bf16_f32 v219, v234, v235\n"
"v_mfma_f32_16x16x32_bf16 v[0:3], v[228:231], a[132:135], v[0:3]\n"
"v_exp_f32 v232, v192\n"
"v_mfma_f32_16x16x32_bf16 v[4:7], v[224:227], a[136:139], v[4:7]\n"
"s_add_u32 m0, s46, 159744\n"
"v_exp_f32 v233, v193\n"
"v_mfma_f32_16x16x32_bf16 v[4:7], v[228:231], a[140:143], v[4:7]\n"
"buffer_load_dwordx4 v253, s[24:27], s51 offen nt lds\n"
"v_mfma_f32_16x16x32_bf16 v[8:11], v[224:227], a[144:147], v[8:11]\n"
"s_add_u32 s51, s51, 4096\n"
"v_exp_f32 v234, v194\n"
"v_mfma_f32_16x16x32_bf16 v[8:11], v[228:231], a[148:151], v[8:11]\n"
"v_exp_f32 v235, v195\n"
"v_mfma_f32_16x16x32_bf16 v[12:15], v[224:227], a[152:155], v[12:15]\n"
"v_cvt_pk_bf16_f32 v220, v232, v233\n"
"v_cvt_pk_bf16_f32 v221, v234, v235\n"
"v_mfma_f32_16x16x32_bf16 v[12:15], v[228:231], a[156:159], v[12:15]\n"
"v_exp_f32 v232, v196\n"
"v_mfma_f32_16x16x32_bf16 v[16:19], v[224:227], a[160:163], v[16:19]\n"
"v_exp_f32 v233, v197\n"
"v_mfma_f32_16x16x32_bf16 v[16:19], v[228:231], a[164:167], v[16:19]\n"
"v_exp_f32 v234, v198\n"
"v_mfma_f32_16x16x32_bf16 v[20:23], v[224:227], a[168:171], v[20:23]\n"
"v_exp_f32 v235, v199\n"
"v_mfma_f32_16x16x32_bf16 v[20:23], v[228:231], a[172:175], v[20:23]\n"
"v_cvt_pk_bf16_f32 v222, v232, v233\n"
"v_cvt_pk_bf16_f32 v223, v234, v235\n"
"v_mfma_f32_16x16x32_bf16 v[24:27], v[224:227], a[176:179], v[24:27]\n"
"s_add_u32 m0, s46, 32768\n"
"v_mfma_f32_16x16x32_bf16 v[24:27], v[228:231], a[180:183], v[24:27]\n"
"buffer_load_dwordx4 v252, s[20:23], s44 offen nt lds\n"
"v_mfma_f32_16x16x32_bf16 v[28:31], v[224:227], a[184:187], v[28:31]\n"
"s_add_u32 s44, s44, 4096\n"
"v_mfma_f32_16x16x32_bf16 v[28:31], v[228:231], a[188:191], v[28:31]\n"
"s_cmp_gt_u32 s94, s99\n"
"s_cselect_b32 s63, 110, s63\n"
"s_cselect_b32 s95, 100, 0\n"
"v_mfma_f32_16x16x32_bf16 v[200:203], a[192:195], a[80:83], v[172:175]\n"
"s_add_u32 s94, s94, s95\n"
"v_mfma_f32_16x16x32_bf16 v[200:203], a[196:199], a[84:87], v[200:203]\n"
"s_mov_b32 s99, s97\n"
"v_mfma_f32_16x16x32_bf16 v[200:203], a[200:203], a[88:91], v[200:203]\n"
"s_add_u32 m0, s46, 36864\n"
"v_mfma_f32_16x16x32_bf16 v[200:203], a[204:207], a[92:95], v[200:203]\n"
"buffer_load_dwordx4 v252, s[20:23], s44 offen nt lds\n"
"v_mfma_f32_16x16x32_bf16 v[204:207], a[208:211], a[80:83], v[172:175]\n"
"s_add_u32 s44, s44, 4096\n"
"v_mfma_f32_16x16x32_bf16 v[204:207], a[212:215], a[84:87], v[204:207]\n"
"s_nop 2\n"
"v_mfma_f32_16x16x32_bf16 v[204:207], a[216:219], a[88:91], v[204:207]\n"
"s_add_u32 s98, s98, s94\n"
"v_exp_f32 v232, v200\n"
"v_mfma_f32_16x16x32_bf16 v[204:207], a[220:223], a[92:95], v[204:207]\n"
"v_exp_f32 v233, v201\n"
"v_mfma_f32_16x16x32_bf16 v[228:231], v[236:239], v[216:219], 0\n"
"v_exp_f32 v234, v202\n"
"v_mfma_f32_16x16x32_bf16 v[228:231], v[236:239], v[220:223], v[228:231]\n"
"v_exp_f32 v235, v203\n"
"v_mfma_f32_16x16x32_bf16 v[208:211], a[224:227], a[80:83], v[172:175]\n"
"v_cvt_pk_bf16_f32 v224, v232, v233\n"
"v_cvt_pk_bf16_f32 v225, v234, v235\n"
"v_mfma_f32_16x16x32_bf16 v[208:211], a[228:231], a[84:87], v[208:211]\n"
"v_exp_f32 v232, v204\n"
"v_mfma_f32_16x16x32_bf16 v[208:211], a[232:235], a[88:91], v[208:211]\n"
"s_add_u32 m0, s46, 40960\n"
"v_exp_f32 v233, v205\n"
"v_mfma_f32_16x16x32_bf16 v[208:211], a[236:239], a[92:95], v[208:211]\n"
"buffer_load_dwordx4 v252, s[20:23], s44 offen nt lds\n"
"v_mfma_f32_16x16x32_bf16 v[212:215], a[240:243], a[80:83], v[172:175]\n"
"s_add_u32 s44, s44, 4096\n"
"v_cmp_nge_f32 vcc, v254, v228\n"
"v_cvt_pk_bf16_f32 v226, v232, v233\n"
"v_mfma_f32_16x16x32_bf16 v[212:215], a[244:247], a[84:87], v[212:215]\n"
"v_exp_f32 v234, v206\n"
"v_mfma_f32_16x16x32_bf16 v[212:215], a[248:251], a[88:91], v[212:215]\n"
"v_exp_f32 v235, v207\n"
"v_mfma_f32_16x16x32_bf16 v[212:215], a[252:255], a[92:95], v[212:215]\n"
"s_cbranch_vccnz .Lstub_8\n"
".Lback_8:\n"
"v_mfma_f32_16x16x32_bf16 v[32:35], v[216:219], a[128:131], v[32:35]\n"
"v_add_f32 v242, v242, |v228|\n"
"v_cvt_pk_bf16_f32 v227, v234, v235\n"
"v_mfma_f32_16x16x32_bf16 v[32:35], v[220:223], a[132:135], v[32:35]\n"
"v_exp_f32 v232, v208\n"
"v_mfma_f32_16x16x32_bf16 v[36:39], v[216:219], a[136:139], v[36:39]\n"
"v_exp_f32 v233, v209\n"
"v_mfma_f32_16x16x32_bf16 v[36:39], v[220:223], a[140:143], v[36:39]\n"
"v_exp_f32 v234, v210\n"
"v_mfma_f32_16x16x32_bf16 v[40:43], v[216:219], a[144:147], v[40:43]\n"
"s_add_u32 m0, s46, 45056\n"
"v_exp_f32 v235, v211\n"
"v_mfma_f32_16x16x32_bf16 v[40:43], v[220:223], a[148:151], v[40:43]\n"
"buffer_load_dwordx4 v252, s[20:23], s44 offen nt lds\n"
"v_mfma_f32_16x16x32_bf16 v[44:47], v[216:219], a[152:155], v[44:47]\n"
"s_add_u32 s44, s44, 4096\n"
"v_cvt_pk_bf16_f32 v228, v232, v233\n"
"v_cvt_pk_bf16_f32 v229, v234, v235\n"
"v_mfma_f32_16x16x32_bf16 v[44:47], v[220:223], a[156:159], v[44:47]\n"
"v_exp_f32 v232, v212\n"
"v_mfma_f32_16x16x32_bf16 v[48:51], v[216:219], a[160:163], v[48:51]\n"
"v_exp_f32 v233, v213\n"
"v_mfma_f32_16x16x32_bf16 v[48:51], v[220:223], a[164:167], v[48:51]\n"
"v_exp_f32 v234, v214\n"
"v_mfma_f32_16x16x32_bf16 v[52:55], v[216:219], a[168:171], v[52:55]\n"
"v_exp_f32 v235, v215\n"
"v_mfma_f32_16x16x32_bf16 v[52:55], v[220:223], a[172:175], v[52:55]\n"
"v_cvt_pk_bf16_f32 v230, v232, v233\n"
"v_cvt_pk_bf16_f32 v231, v234, v235\n"
"v_mfma_f32_16x16x32_bf16 v[56:59], v[216:219], a[176:179], v[56:59]\n"
"v_mfma_f32_16x16x32_bf16 v[56:59], v[220:223], a[180:183], v[56:59]\n"
"v_mfma_f32_16x16x32_bf16 v[60:63], v[216:219], a[184:187], v[60:63]\n"
"s_add_u32 m0, s46, 49152\n"
"v_mfma_f32_16x16x32_bf16 v[60:63], v[220:223], a[188:191], v[60:63]\n"
"buffer_load_dwordx4 v252, s[20:23], s44 offen nt lds\n"
"v_mfma_f32_16x16x32_bf16 v[184:187], a[192:195], a[96:99], v[176:179]\n"
"s_add_u32 s44, s44, 4096\n"
"v_mfma_f32_16x16x32_bf16 v[184:187], a[196:199], a[100:103], v[184:187]\n"
"v_mfma_f32_16x16x32_bf16 v[184:187], a[200:203], a[104:107], v[184:187]\n"
"v_mfma_f32_16x16x32_bf16 v[184:187], a[204:207], a[108:111], v[184:187]\n"
"s_nop 2\n"
"v_mfma_f32_16x16x32_bf16 v[188:191], a[208:211], a[96:99], v[176:179]\n"
"s_nop 0\n"
"v_mfma_f32_16x16x32_bf16 v[188:191], a[212:215], a[100:103], v[188:191]\n"
"v_mfma_f32_16x16x32_bf16 v[188:191], a[216:219], a[104:107], v[188:191]\n"
"s_add_u32 m0, s46, 53248\n"
"v_exp_f32 v232, v184\n"
"v_mfma_f32_16x16x32_bf16 v[188:191], a[220:223], a[108:111], v[188:191]\n"
"buffer_load_dwordx4 v252, s[20:23], s44 offen nt lds\n"
"v_mfma_f32_16x16x32_bf16 v[220:223], v[236:239], v[224:227], 0\n"
"s_add_u32 s44, s44, 4096\n"
"v_exp_f32 v233, v185\n"
"v_mfma_f32_16x16x32_bf16 v[220:223], v[236:239], v[228:231], v[220:223]\n"
"v_exp_f32 v234, v186\n"
"v_mfma_f32_16x16x32_bf16 v[192:195], a[224:227], a[96:99], v[176:179]\n"
"v_exp_f32 v235, v187\n"
"v_mfma_f32_16x16x32_bf16 v[192:195], a[228:231], a[100:103], v[192:195]\n"
"v_cvt_pk_bf16_f32 v216, v232, v233\n"
"v_cvt_pk_bf16_f32 v217, v234, v235\n"
"v_mfma_f32_16x16x32_bf16 v[192:195], a[232:235], a[104:107], v[192:195]\n"
"v_exp_f32 v232, v188\n"
"v_mfma_f32_16x16x32_bf16 v[192:195], a[236:239], a[108:111], v[192:195]\n"
"v_exp_f32 v233, v189\n"
"v_mfma_f32_16x16x32_bf16 v[196:199], a[240:243], a[96:99], v[176:179]\n"
"v_cmp_nge_f32 vcc, v254, v220\n"
"v_cvt_pk_bf16_f32 v218, v232, v233\n"
"v_mfma_f32_16x16x32_bf16 v[196:199], a[244:247], a[100:103], v[196:199]\n"
"v_exp_f32 v234, v190\n"
"v_mfma_f32_16x16x32_bf16 v[196:199], a[248:251], a[104:107], v[196:199]\n"
"v_exp_f32 v235, v191\n"
"v_mfma_f32_16x16x32_bf16 v[196:199], a[252:255], a[108:111], v[196:199]\n"
"s_cbranch_vccnz .Lstub_9\n"
".Lback_9:\n"
"v_mfma_f32_16x16x32_bf16 v[64:67], v[224:227], a[128:131], v[64:67]\n"
"s_add_u32 m0, s46, 57344\n"
"v_add_f32 v243, v243, |v220|\n"
"v_cvt_pk_bf16_f32 v219, v234, v235\n"
"v_mfma_f32_16x16x32_bf16 v[64:67], v[228:231], a[132:135], v[64:67]\n"
"buffer_load_dwordx4 v252, s[20:23], s44 offen nt lds\n"
"v_mfma_f32_16x16x32_bf16 v[68:71], v[224:227], a[136:139], v[68:71]\n"
"s_add_u32 s44, s44, 4096\n"
"v_exp_f32 v232, v192\n"
"v_mfma_f32_16x16x32_bf16 v[68:71], v[228:231], a[140:143], v[68:71]\n"
"v_exp_f32 v233, v193\n"
"v_mfma_f32_16x16x32_bf16 v[72:75], v[224:227], a[144:147], v[72:75]\n"
"v_exp_f32 v234, v194\n"
"v_mfma_f32_16x16x32_bf16 v[72:75], v[228:231], a[148:151], v[72:75]\n"
"v_exp_f32 v235, v195\n"
"v_mfma_f32_16x16x32_bf16 v[76:79], v[224:227], a[152:155], v[76:79]\n"
"v_cvt_pk_bf16_f32 v220, v232, v233\n"
"v_cvt_pk_bf16_f32 v221, v234, v235\n"
"v_mfma_f32_16x16x32_bf16 v[76:79], v[228:231], a[156:159], v[76:79]\n"
"v_exp_f32 v232, v196\n"
"v_mfma_f32_16x16x32_bf16 v[80:83], v[224:227], a[160:163], v[80:83]\n"
"v_exp_f32 v233, v197\n"
"v_mfma_f32_16x16x32_bf16 v[80:83], v[228:231], a[164:167], v[80:83]\n"
"s_memrealtime s[94:95]\n"
"v_exp_f32 v234, v198\n"
"v_mfma_f32_16x16x32_bf16 v[84:87], v[224:227], a[168:171], v[84:87]\n"
"s_add_u32 m0, s46, 61440\n"
"v_exp_f32 v235, v199\n"
"v_mfma_f32_16x16x32_bf16 v[84:87], v[228:231], a[172:175], v[84:87]\n"
"buffer_load_dwordx4 v252, s[20:23], s44 offen nt lds\n"
"v_mfma_f32_16x16x32_bf16 v[88:91], v[224:227], a[176:179], v[88:91]\n"
"s_add_u32 s44, s44, 4096\n"
"v_cvt_pk_bf16_f32 v222, v232, v233\n"
"v_cvt_pk_bf16_f32 v223, v234, v235\n"
"v_mfma_f32_16x16x32_bf16 v[88:91], v[228:231], a[180:183], v[88:91]\n"
"s_waitcnt lgkmcnt(0)\n"
"s_sub_u32 s94, s94, s98\n"
"s_cmp_lt_i32 s94, 0\n"
"v_mfma_f32_16x16x32_bf16 v[92:95], v[224:227], a[184:187], v[92:95]\n"
"s_cbranch_scc1 .Lpa1\n"
".Lpa1x:\n"
"v_mfma_f32_16x16x32_bf16 v[92:95], v[228:231], a[188:191], v[92:95]\n"
"s_waitcnt vmcnt(16)\n"
"s_barrier\n"
"v_mfma_f32_16x16x32_bf16 v[200:203], a[192:195], a[112:115], v[180:183]\n"
"s_add_u32 s98, s98, 111\n"
"ds_read_b128 a[192:195], v246 offset:0\n"
"v_mfma_f32_16x16x32_bf16 v[200:203], a[196:199], a[116:119], v[200:203]\n"
"s_lshr_b32 s95, s63, 3\n"
"ds_read_b128 a[196:199], v247 offset:0\n"
"v_mfma_f32_16x16x32_bf16 v[200:203], a[200:203], a[120:123], v[200:203]\n"
"s_sub_u32 s63, s63, s95\n"
"ds_read_b128 a[200:203], v246 offset:512\n"
"v_mfma_f32_16x16x32_bf16 v[200:203], a[204:207], a[124:127], v[200:203]\n"
"s_nop 0\n"
"ds_read_b128 a[204:207], v247 offset:512\n"
"v_mfma_f32_16x16x32_bf16 v[204:207], a[208:211], a[112:115], v[180:183]\n"
"s_add_u32 s98, s98, s95\n"
"ds_read_b128 a[208:211], v246 offset:4096\n"
"v_mfma_f32_16x16x32_bf16 v[204:207], a[212:215], a[116:119], v[204:207]\n"
"s_sub_u32 s94, s94, 18\n"
"ds_read_b128 a[212:215], v247 offset:4096\n"
"v_mfma_f32_16x16x32_bf16 v[204:207], a[216:219], a[120:123], v[204:207]\n"
"s_add_u32 m0, s46, 98304\n"
"v_exp_f32 v232, v200\n"
"v_mfma_f32_16x16x32_bf16 v[204:207], a[220:223], a[124:127], v[204:207]\n"
"buffer_load_dwordx4 v253, s[24:27], s51 offen nt lds\n"
"v_mfma_f32_16x16x32_bf16 v[228:231], v[236:239], v[216:219], 0\n"
"s_add_u32 s51, s51, 4096\n"
"v_exp_f32 v233, v201\n"
"v_mfma_f32_16x16x32_bf16 v[228:231], v[236:239], v[220:223], v[228:231]\n"
"s_max_i32 s94, s94, 0\n"
"v_exp_f32 v234, v202\n"
"v_mfma_f32_16x16x32_bf16 v[208:211], a[224:227], a[112:115], v[180:183]\n"
"ds_read_b128 a[216:219], v246 offset:4608\n"
"v_cvt_pk_bf16_f32 v224, v232, v233\n"
"v_mfma_f32_16x16x32_bf16 v[208:211], a[228:231], a[116:119], v[208:211]\n"
"v_exp_f32 v235, v203\n"
"v_mfma_f32_16x16x32_bf16 v[208:211], a[232:235], a[120:123], v[208:211]\n"
"ds_read_b128 a[220:223], v247 offset:4608\n"
"v_cvt_pk_bf16_f32 v225, v234, v235\n"
"v_mfma_f32_16x16x32_bf16 v[208:211], a[236:239], a[124:127], v[208:211]\n"
"ds_read_b128 a[224:227], v246 offset:8192\n"
"v_cmp_nge_f32 vcc, v254, v228\n"
"v_mfma_f32_16x16x32_bf16 v[212:215], a[240:243], a[112:115], v[180:183]\n"
"v_exp_f32 v232, v204\n"
"v_mfma_f32_16x16x32_bf16 v[212:215], a[244:247], a[116:119], v[212:215]\n"
"v_exp_f32 v233, v205\n"
"v_mfma_f32_16x16x32_bf16 v[212:215], a[248:251], a[120:123], v[212:215]\n"
"v_exp_f32 v234, v206\n"
"v_mfma_f32_16x16x32_bf16 v[212:215], a[252:255], a[124:127], v[212:215]\n"
"s_cbranch_vccnz .Lstub_10\n"
".Lback_10:\n"
"v_mfma_f32_16x16x32_bf16 v[96:99], v[216:219], a[128:131], v[96:99]\n"
"v_add_f32 v244, v244, |v228|\n"
"v_cvt_pk_bf16_f32 v226, v232, v233\n"
"v_mfma_f32_16x16x32_bf16 v[96:99], v[220:223], a[132:135], v[96:99]\n"
"v_exp_f32 v235, v207\n"
"v_mfma_f32_16x16x32_bf16 v[100:103], v[216:219], a[136:139], v[100:103]\n"
"s_add_u32 m0, s46, 102400\n"
"ds_read_b128 a[228:231], v247 offset:8192\n"
"v_cvt_pk_bf16_f32 v227, v234, v235\n"
"v_mfma_f32_16x16x32_bf16 v[100:103], v[220:223], a[140:143], v[100:103]\n"
"buffer_load_dwordx4 v253, s[24:27], s51 offen nt lds\n"
"v_mfma_f32_16x16x32_bf16 v[104:107], v[216:219], a[144:147], v[104:107]\n"
"s_add_u32 s51, s51, 4096\n"
"v_exp_f32 v232, v208\n"
"v_mfma_f32_16x16x32_bf16 v[104:107], v[220:223], a[148:151], v[104:107]\n"
"v_exp_f32 v233, v209\n"
"v_mfma_f32_16x16x32_bf16 v[108:111], v[216:219], a[152:155], v[108:111]\n"
"v_exp_f32 v234, v210\n"
"v_mfma_f32_16x16x32_bf16 v[108:111], v[220:223], a[156:159], v[108:111]\n"
"ds_read_b128 a[232:235], v246 offset:8704\n"
"v_cvt_pk_bf16_f32 v228, v232, v233\n"
"v_mfma_f32_16x16x32_bf16 v[112:115], v[216:219], a[160:163], v[112:115]\n"
"v_exp_f32 v235, v211\n"
"v_mfma_f32_16x16x32_bf16 v[112:115], v[220:223], a[164:167], v[112:115]\n"
"ds_read_b128 a[236:239], v247 offset:8704\n"
"v_cvt_pk_bf16_f32 v229, v234, v235\n"
"v_mfma_f32_16x16x32_bf16 v[116:119], v[216:219], a[168:171], v[116:119]\n"
"v_exp_f32 v232, v212\n"
"v_mfma_f32_16x16x32_bf16 v[116:119], v[220:223], a[172:175], v[116:119]\n"
"v_exp_f32 v233, v213\n"
"v_mfma_f32_16x16x32_bf16 v[120:123], v[216:219], a[176:179], v[120:123]\n"
"v_exp_f32 v234, v214\n"
"v_mfma_f32_16x16x32_bf16 v[120:123], v[220:223], a[180:183], v[120:123]\n"
"s_waitcnt lgkmcnt(2)\n"
"ds_read_b128 a[240:243], v246 offset:12288\n"
"v_cvt_pk_bf16_f32 v230, v232, v233\n"
"v_mfma_f32_16x16x32_bf16 v[124:127], v[216:219], a[184:187], v[124:127]\n"
"s_add_u32 m0, s46, 106496\n"
"v_exp_f32 v235, v215\n"
"v_mfma_f32_16x16x32_bf16 v[124:127], v[220:223], a[188:191], v[124:127]\n"
"buffer_load_dwordx4 v253, s[24:27], s51 offen nt lds\n"
"v_mfma_f32_16x16x32_bf16 v[184:187], a[192:195], a[32:35], v[160:163]\n"
"s_add_u32 s51, s51, 4096\n"
"ds_read_b128 a[244:247], v247 offset:12288\n"
"ds_read_b128 a[248:251], v246 offset:12800\n"
"v_mfma_f32_16x16x32_bf16 v[184:187], a[196:199], a[36:39], v[184:187]\n"
"v_cvt_pk_bf16_f32 v231, v234, v235\n"
"v_mfma_f32_16x16x32_bf16 v[184:187], a[200:203], a[40:43], v[184:187]\n"
"ds_read_b128 a[252:255], v247 offset:12800\n"
"v_mfma_f32_16x16x32_bf16 v[184:187], a[204:207], a[44:47], v[184:187]\n"
"s_nop 2\n"
"v_mfma_f32_16x16x32_bf16 v[188:191], a[208:211], a[32:35], v[160:163]\n"
"s_nop 0\n"
"v_mfma_f32_16x16x32_bf16 v[188:191], a[212:215], a[36:39], v[188:191]\n"
"s_cmp_gt_u32 s94, s99\n"
"s_cselect_b32 s63, 110, s63\n"
"s_cselect_b32 s95, 100, 0\n"
"v_mfma_f32_16x16x32_bf16 v[188:191], a[216:219], a[40:43], v[188:191]\n"
"s_add_u32 m0, s46, 110592\n"
"v_exp_f32 v232, v184\n"
"v_mfma_f32_16x16x32_bf16 v[188:191], a[220:223], a[44:47], v[188:191]\n"
"buffer_load_dwordx4 v253, s[24:27], s51 offen nt lds\n"
"v_mfma_f32_16x16x32_bf16 v[220:223], v[236:239], v[224:227], 0\n"
"s_add_u32 s51, s51, 4096\n"
"v_exp_f32 v233, v185\n"
"v_mfma_f32_16x16x32_bf16 v[220:223], v[236:239], v[228:231], v[220:223]\n"
"s_add_u32 s94, s94, s95\n"
"v_exp_f32 v234, v186\n"
"v_mfma_f32_16x16x32_bf16 v[192:195], a[224:227], a[32:35], v[160:163]\n"
"s_add_u32 s98, s98, s94\n"
"v_exp_f32 v235, v187\n"
"v_mfma_f32_16x16x32_bf16 v[192:195], a[228:231], a[36:39], v[192:195]\n"
"s_waitcnt lgkmcnt(0)\n"
"v_cvt_pk_bf16_f32 v216, v232, v233\n"
"v_cvt_pk_bf16_f32 v217, v234, v235\n"
"v_mfma_f32_16x16x32_bf16 v[192:195], a[232:235], a[40:43], v[192:195]\n"
"v_exp_f32 v232, v188\n"
"v_mfma_f32_16x16x32_bf16 v[192:195], a[236:239], a[44:47], v[192:195]\n"
"s_waitcnt vmcnt(12) lgkmcnt(0)\n"
"s_barrier\n"
"v_mfma_f32_16x16x32_bf16 v[196:199], a[240:243], a[32:35], v[160:163]\n"
"v_cmp_nge_f32 vcc, v254, v220\n"
"v_mfma_f32_16x16x32_bf16 v[196:199], a[244:247], a[36:39], v[196:199]\n"
"v_exp_f32 v233, v189\n"
"v_mfma_f32_16x16x32_bf16 v[196:199], a[248:251], a[40:43], v[196:199]\n"
"v_exp_f32 v234, v190\n"
"v_mfma_f32_16x16x32_bf16 v[196:199], a[252:255], a[44:47], v[196:199]\n"
"s_cbranch_vccnz .Lstub_11\n"
".Lback_11:\n"
"v_mfma_f32_16x16x32_bf16 v[128:131], v[224:227], a[128:131], v[128:131]\n"
"ds_read_b64_tr_b16 a[128:129], v248 offset:32768\n"
"ds_read_b64_tr_b16 a[130:131], v248 offset:36864\n"
"v_mfma_f32_16x16x32_bf16 v[128:131], v[228:231], a[132:135], v[128:131]\n"
"s_mov_b32 s43, 0\n"
"v_add_f32 v245, v245, |v220|\n"
"v_cvt_pk_bf16_f32 v218, v232, v233\n"
"v_mfma_f32_16x16x32_bf16 v[132:135], v[224:227], a[136:139], v[132:135]\n"
"ds_read_b64_tr_b16 a[132:133], v248 offset:40960\n"
"ds_read_b64_tr_b16 a[134:135], v248 offset:45056\n"
"v_mfma_f32_16x16x32_bf16 v[132:135], v[228:231], a[140:143], v[132:135]\n"
"v_exp_f32 v235, v191\n"
"v_mfma_f32_16x16x32_bf16 v[136:139], v[224:227], a[144:147], v[136:139]\n"
"ds_read_b64_tr_b16 a[136:137], v249 offset:32768\n"
"ds_read_b64_tr_b16 a[138:139], v249 offset:36864\n"
"v_mfma_f32_16x16x32_bf16 v[136:139], v[228:231], a[148:151], v[136:139]\n"
"v_cvt_pk_bf16_f32 v219, v234, v235\n"
"v_mov_b32 v254, s42\n"
"v_mfma_f32_16x16x32_bf16 v[140:143], v[224:227], a[152:155], v[140:143]\n"
"ds_read_b64_tr_b16 a[140:141], v249 offset:40960\n"
"ds_read_b64_tr_b16 a[142:143], v249 offset:45056\n"
"v_mfma_f32_16x16x32_bf16 v[140:143], v[228:231], a[156:159], v[140:143]\n"
"v_exp_f32 v232, v192\n"
"v_mfma_f32_16x16x32_bf16 v[144:147], v[224:227], a[160:163], v[144:147]\n"
"ds_read_b64_tr_b16 a[144:145], v250 offset:32768\n"
"ds_read_b64_tr_b16 a[146:147], v250 offset:36864\n"
"v_mfma_f32_16x16x32_bf16 v[144:147], v[228:231], a[164:167], v[144:147]\n"
"v_exp_f32 v233, v193\n"
"v_mfma_f32_16x16x32_bf16 v[148:151], v[224:227], a[168:171], v[148:151]\n"
"ds_read_b64_tr_b16 a[148:149], v250 offset:40960\n"
"ds_read_b64_tr_b16 a[150:151], v250 offset:45056\n"
"v_mfma_f32_16x16x32_bf16 v[148:151], v[228:231], a[172:175], v[148:151]\n"
"v_exp_f32 v234, v194\n"
"v_mfma_f32_16x16x32_bf16 v[152:155], v[224:227], a[176:179], v[152:155]\n"
"ds_read_b64_tr_b16 a[152:153], v251 offset:32768\n"
"v_cvt_pk_bf16_f32 v220, v232, v233\n"
"v_mfma_f32_16x16x32_bf16 v[152:155], v[228:231], a[180:183], v[152:155]\n"
"v_exp_f32 v235, v195\n"
"v_mfma_f32_16x16x32_bf16 v[156:159], v[224:227], a[184:187], v[156:159]\n"
"s_sub_u32 s45, s45, 1\n"
"ds_read_b64_tr_b16 a[154:155], v251 offset:36864\n"
"v_cvt_pk_bf16_f32 v221, v234, v235\n"
"v_mfma_f32_16x16x32_bf16 v[156:159], v[228:231], a[188:191], v[156:159]\n"
"s_cmp_lg_u32 s45, 0\n"
"s_cbranch_scc1 .Lloop\n"
"v_mfma_f32_16x16x32_bf16 v[200:203], a[192:195], a[48:51], v[164:167]\n"
"v_exp_f32 v232, v196\n"
"v_mfma_f32_16x16x32_bf16 v[200:203], a[196:199], a[52:55], v[200:203]\n"
"v_exp_f32 v233, v197\n"
"v_mfma_f32_16x16x32_bf16 v[200:203], a[200:203], a[56:59], v[200:203]\n"
"ds_read_b64_tr_b16 a[156:157], v251 offset:40960\n"
"ds_read_b64_tr_b16 a[158:159], v251 offset:45056\n"
"v_mfma_f32_16x16x32_bf16 v[200:203], a[204:207], a[60:63], v[200:203]\n"
"v_exp_f32 v234, v198\n"
"v_mfma_f32_16x16x32_bf16 v[204:207], a[208:211], a[48:51], v[164:167]\n"
"ds_read_b64_tr_b16 a[160:161], v248 offset:33280\n"
"v_cvt_pk_bf16_f32 v222, v232, v233\n"
"v_mfma_f32_16x16x32_bf16 v[204:207], a[212:215], a[52:55], v[204:207]\n"
"v_exp_f32 v235, v199\n"
"v_mfma_f32_16x16x32_bf16 v[204:207], a[216:219], a[56:59], v[204:207]\n"
"ds_read_b64_tr_b16 a[162:163], v248 offset:37376\n"
"v_cvt_pk_bf16_f32 v223, v234, v235\n"
"v_mfma_f32_16x16x32_bf16 v[204:207], a[220:223], a[60:63], v[204:207]\n"
"v_exp_f32 v232, v200\n"
"v_mfma_f32_16x16x32_bf16 v[228:231], v[236:239], v[216:219], 0\n"
"s_add_u32 m0, s46, 114688\n"
"ds_read_b64_tr_b16 a[164:165], v248 offset:41472\n"
"ds_read_b64_tr_b16 a[166:167], v248 offset:45568\n"
"v_mfma_f32_16x16x32_bf16 v[228:231], v[236:239], v[220:223], v[228:231]\n"
"buffer_load_dwordx4 v253, s[24:27], s51 offen nt lds\n"
"v_mfma_f32_16x16x32_bf16 v[208:211], a[224:227], a[48:51], v[164:167]\n"
"s_add_u32 s51, s51, 4096\n"
"ds_read_b64_tr_b16 a[168:169], v249 offset:33280\n"
"ds_read_b64_tr_b16 a[170:171], v249 offset:37376\n"
"v_mfma_f32_16x16x32_bf16 v[208:211], a[228:231], a[52:55], v[208:211]\n"
"v_exp_f32 v233, v201\n"
"v_mfma_f32_16x16x32_bf16 v[208:211], a[232:235], a[56:59], v[208:211]\n"
"ds_read_b64_tr_b16 a[172:173], v249 offset:41472\n"
"ds_read_b64_tr_b16 a[174:175], v249 offset:45568\n"
"v_mfma_f32_16x16x32_bf16 v[208:211], a[236:239], a[60:63], v[208:211]\n"
"v_cmp_nge_f32 vcc, v254, v228\n"
"v_cvt_pk_bf16_f32 v224, v232, v233\n"
"v_mfma_f32_16x16x32_bf16 v[212:215], a[240:243], a[48:51], v[164:167]\n"
"ds_read_b64_tr_b16 a[176:177], v250 offset:33280\n"
"ds_read_b64_tr_b16 a[178:179], v250 offset:37376\n"
"v_mfma_f32_16x16x32_bf16 v[212:215], a[244:247], a[52:55], v[212:215]\n"
"v_exp_f32 v234, v202\n"
"v_mfma_f32_16x16x32_bf16 v[212:215], a[248:251], a[56:59], v[212:215]\n"
"s_waitcnt lgkmcnt(6)\n"
"ds_read_b64_tr_b16 a[180:181], v250 offset:41472\n"
"ds_read_b64_tr_b16 a[182:183], v250 offset:45568\n"
"v_mfma_f32_16x16x32_bf16 v[212:215], a[252:255], a[60:63], v[212:215]\n"
"s_cbranch_vccnz .Lstub_12\n"
".Lback_12:\n"
"v_mfma_f32_16x16x32_bf16 a[0:3], v[216:219], a[128:131], a[0:3]\n"
"ds_read_b64_tr_b16 a[184:185], v251 offset:33280\n"
"ds_read_b64_tr_b16 a[186:187], v251 offset:37376\n"
"v_mfma_f32_16x16x32_bf16 a[0:3], v[220:223], a[132:135], a[0:3]\n"
"v_exp_f32 v235, v203\n"
"v_mfma_f32_16x16x32_bf16 a[4:7], v[216:219], a[136:139], a[4:7]\n"
"ds_read_b64_tr_b16 a[188:189], v251 offset:41472\n"
"v_add_f32 v240, v240, |v228|\n"
"v_mfma_f32_16x16x32_bf16 a[4:7], v[220:223], a[140:143], a[4:7]\n"
"ds_read_b64_tr_b16 a[190:191], v251 offset:45568\n"
"v_cvt_pk_bf16_f32 v225, v234, v235\n"
"v_mfma_f32_16x16x32_bf16 a[8:11], v[216:219], a[144:147], a[8:11]\n"
"v_exp_f32 v232, v204\n"
"v_mfma_f32_16x16x32_bf16 a[8:11], v[220:223], a[148:151], a[8:11]\n"
"v_exp_f32 v233, v205\n"
"v_mfma_f32_16x16x32_bf16 a[12:15], v[216:219], a[152:155], a[12:15]\n"
"v_exp_f32 v234, v206\n"
"v_mfma_f32_16x16x32_bf16 a[12:15], v[220:223], a[156:159], a[12:15]\n"
"v_exp_f32 v235, v207\n"
"v_mfma_f32_16x16x32_bf16 a[16:19], v[216:219], a[160:163], a[16:19]\n"
"v_cvt_pk_bf16_f32 v226, v232, v233\n"
"v_cvt_pk_bf16_f32 v227, v234, v235\n"
"v_mfma_f32_16x16x32_bf16 a[16:19], v[220:223], a[164:167], a[16:19]\n"
"s_waitcnt lgkmcnt(1)\n"
"s_memrealtime s[94:95]\n"
"v_exp_f32 v232, v208\n"
"v_mfma_f32_16x16x32_bf16 a[20:23], v[216:219], a[168:171], a[20:23]\n"
"s_add_u32 m0, s46, 118784\n"
"v_exp_f32 v233, v209\n"
"v_mfma_f32_16x16x32_bf16 a[20:23], v[220:223], a[172:175], a[20:23]\n"
"buffer_load_dwordx4 v253, s[24:27], s51 offen nt lds\n"
"v_mfma_f32_16x16x32_bf16 a[24:27], v[216:219], a[176:179], a[24:27]\n"
"s_add_u32 s51, s51, 4096\n"
"v_exp_f32 v234, v210\n"
"v_mfma_f32_16x16x32_bf16 a[24:27], v[220:223], a[180:183], a[24:27]\n"
"v_exp_f32 v235, v211\n"
"v_mfma_f32_16x16x32_bf16 a[28:31], v[216:219], a[184:187], a[28:31]\n"
"s_waitcnt lgkmcnt(0)\n"
"v_cvt_pk_bf16_f32 v228, v232, v233\n"
"v_cvt_pk_bf16_f32 v229, v234, v235\n"
"v_mfma_f32_16x16x32_bf16 a[28:31], v[220:223], a[188:191], a[28:31]\n"
"s_sub_u32 s94, s94, s98\n"
"s_cmp_lt_i32 s94, 0\n"
"s_cbranch_scc1 .Lpc3\n"
".Lpc3x:\n"
"s_barrier\n"
"v_mfma_f32_16x16x32_bf16 v[184:187], a[192:195], a[64:67], v[168:171]\n"
"s_add_u32 s98, s98, 111\n"
"v_exp_f32 v232, v212\n"
"v_mfma_f32_16x16x32_bf16 v[184:187], a[196:199], a[68:71], v[184:187]\n"
"s_lshr_b32 s95, s63, 3\n"
"v_exp_f32 v233, v213\n"
"v_mfma_f32_16x16x32_bf16 v[184:187], a[200:203], a[72:75], v[184:187]\n"
"s_sub_u32 s63, s63, s95\n"
"v_exp_f32 v234, v214\n"
"v_mfma_f32_16x16x32_bf16 v[184:187], a[204:207], a[76:79], v[184:187]\n"
"s_nop 0\n"
"v_exp_f32 v235, v215\n"
"v_mfma_f32_16x16x32_bf16 v[188:191], a[208:211], a[64:67], v[168:171]\n"
"s_nop 0\n"
"v_cvt_pk_bf16_f32 v230, v232, v233\n"
"v_cvt_pk_bf16_f32 v231, v234, v235\n"
"v_mfma_f32_16x16x32_bf16 v[188:191], a[212:215], a[68:71], v[188:191]\n"
"s_add_u32 s98, s98, s95\n"
"v_mfma_f32_16x16x32_bf16 v[188:191], a[216:219], a[72:75], v[188:191]\n"
"s_sub_u32 s94, s94, 18\n"
"v_exp_f32 v232, v184\n"
"v_mfma_f32_16x16x32_bf16 v[188:191], a[220:223], a[76:79], v[188:191]\n"
"s_mov_b32 s44, 4096\n"
"v_exp_f32 v233, v185\n"
"v_mfma_f32_16x16x32_bf16 v[220:223], v[236:239], v[224:227], 0\n"
"s_add_u32 m0, s46, 122880\n"
"v_exp_f32 v234, v186\n"
"v_mfma_f32_16x16x32_bf16 v[220:223], v[236:239], v[228:231], v[220:223]\n"
"buffer_load_dwordx4 v253, s[24:27], s51 offen nt lds\n"
"v_mfma_f32_16x16x32_bf16 v[192:195], a[224:227], a[64:67], v[168:171]\n"
"s_add_u32 s51, s51, 4096\n"
"v_exp_f32 v235, v187\n"
"v_mfma_f32_16x16x32_bf16 v[192:195], a[228:231], a[68:71], v[192:195]\n"
"s_max_i32 s94, s94, 0\n"
"v_cvt_pk_bf16_f32 v216, v232, v233\n"
"v_cvt_pk_bf16_f32 v217, v234, v235\n"
"v_mfma_f32_16x16x32_bf16 v[192:195], a[232:235], a[72:75], v[192:195]\n"
"v_exp_f32 v232, v188\n"
"v_mfma_f32_16x16x32_bf16 v[192:195], a[236:239], a[76:79], v[192:195]\n"
"v_exp_f32 v233, v189\n"
"v_mfma_f32_16x16x32_bf16 v[196:199], a[240:243], a[64:67], v[168:171]\n"
"v_cmp_nge_f32 vcc, v254, v220\n"
"v_cvt_pk_bf16_f32 v218, v232, v233\n"
"v_mfma_f32_16x16x32_bf16 v[196:199], a[244:247], a[68:71], v[196:199]\n"
"v_exp_f32 v234, v190\n"
"v_mfma_f32_16x16x32_bf16 v[196:199], a[248:251], a[72:75], v[196:199]\n"
"v_exp_f32 v235, v191\n"
"v_mfma_f32_16x16x32_bf16 v[196:199], a[252:255], a[76:79], v[196:199]\n"
"s_cbranch_vccnz .Lstub_13\n"
".Lback_13:\n"
"v_mfma_f32_16x16x32_bf16 v[0:3], v[224:227], a[128:131], v[0:3]\n"
"s_xor_b32 s52, s58, 49152\n"
"v_add_f32 v241, v241, |v220|\n"
"v_cvt_pk_bf16_f32 v219, v234, v235\n"
"v_mfma_f32_16x16x32_bf16 v[0:3], v[228:231], a[132:135], v[0:3]\n"
"v_exp_f32 v232, v192\n"
"v_mfma_f32_16x16x32_bf16 v[4:7], v[224:227], a[136:139], v[4:7]\n"
"s_add_u32 m0, s46, 126976\n"
"v_exp_f32 v233, v193\n"
"v_mfma_f32_16x16x32_bf16 v[4:7], v[228:231], a[140:143], v[4:7]\n"
"buffer_load_dwordx4 v253, s[24:27], s51 offen nt lds\n"
"v_mfma_f32_16x16x32_bf16 v[8:11], v[224:227], a[144:147], v[8:11]\n"
"s_add_u32 s51, s51, 4096\n"
"v_exp_f32 v234, v194\n"
"v_mfma_f32_16x16x32_bf16 v[8:11], v[228:231], a[148:151], v[8:11]\n"
"s_add_u32 s53, s52, 65536\n"
"v_cvt_pk_bf16_f32 v220, v232, v233\n"
"v_mbcnt_lo_u32_b32 v252, -1, 0\n"
"v_mfma_f32_16x16x32_bf16 v[12:15], v[224:227], a[152:155], v[12:15]\n"
"v_exp_f32 v235, v195\n"
"v_mfma_f32_16x16x32_bf16 v[12:15], v[228:231], a[156:159], v[12:15]\n"
"s_mov_b32 s46, 8192\n"
"v_cvt_pk_bf16_f32 v221, v234, v235\n"
"v_mbcnt_hi_u32_b32 v252, -1, v252\n"
"v_mfma_f32_16x16x32_bf16 v[16:19], v[224:227], a[160:163], v[16:19]\n"
"v_exp_f32 v232, v196\n"
"v_mfma_f32_16x16x32_bf16 v[16:19], v[228:231], a[164:167], v[16:19]\n"
"v_exp_f32 v233, v197\n"
"v_mfma_f32_16x16x32_bf16 v[20:23], v[224:227], a[168:171], v[20:23]\n"
"s_load_dword s97, s[66:67], 0\n"
"v_exp_f32 v234, v198\n"
"v_mfma_f32_16x16x32_bf16 v[20:23], v[228:231], a[172:175], v[20:23]\n"
"v_cvt_pk_bf16_f32 v222, v232, v233\n"
"v_lshrrev_b32 v253, 3, v252\n"
"v_mfma_f32_16x16x32_bf16 v[24:27], v[224:227], a[176:179], v[24:27]\n"
"v_exp_f32 v235, v199\n"
"v_mfma_f32_16x16x32_bf16 v[24:27], v[228:231], a[180:183], v[24:27]\n"
"v_cvt_pk_bf16_f32 v223, v234, v235\n"
"v_bitop3_b32 v252, v252, v253, 6 bitop3:0x78\n"
"v_mfma_f32_16x16x32_bf16 v[28:31], v[224:227], a[184:187], v[28:31]\n"
"v_and_b32 v252, 7, v252\n"
"v_mul_u32_u24 v253, 1280, v253\n"
"v_mfma_f32_16x16x32_bf16 v[28:31], v[228:231], a[188:191], v[28:31]\n"
"s_load_dword s96, s[66:67], 8192\n"
"v_lshl_add_u32 v252, v252, 4, v253\n"
"v_add_u32 v253, 9216, v252\n"
"v_mfma_f32_16x16x32_bf16 v[200:203], a[192:195], a[80:83], v[172:175]\n"
"s_cmp_gt_u32 s94, s99\n"
"s_cselect_b32 s63, 110, s63\n"
"s_cselect_b32 s95, 100, 0\n"
"v_mfma_f32_16x16x32_bf16 v[200:203], a[196:199], a[84:87], v[200:203]\n"
"s_add_u32 s94, s94, s95\n"
"v_mfma_f32_16x16x32_bf16 v[200:203], a[200:203], a[88:91], v[200:203]\n"
"s_add_u32 s98, s98, s94\n"
"v_mfma_f32_16x16x32_bf16 v[200:203], a[204:207], a[92:95], v[200:203]\n"
"s_nop 2\n"
"v_mfma_f32_16x16x32_bf16 v[204:207], a[208:211], a[80:83], v[172:175]\n"
"s_load_dword s99, s[66:67], 16384\n"
"v_mfma_f32_16x16x32_bf16 v[204:207], a[212:215], a[84:87], v[204:207]\n"
"s_nop 0\n"
"v_mfma_f32_16x16x32_bf16 v[204:207], a[216:219], a[88:91], v[204:207]\n"
"v_exp_f32 v232, v200\n"
"v_mfma_f32_16x16x32_bf16 v[204:207], a[220:223], a[92:95], v[204:207]\n"
"v_exp_f32 v233, v201\n"
"v_mfma_f32_16x16x32_bf16 v[228:231], v[236:239], v[216:219], 0\n"
"v_exp_f32 v234, v202\n"
"v_mfma_f32_16x16x32_bf16 v[228:231], v[236:239], v[220:223], v[228:231]\n"
"v_exp_f32 v235, v203\n"
"v_mfma_f32_16x16x32_bf16 v[208:211], a[224:227], a[80:83], v[172:175]\n"
"v_cvt_pk_bf16_f32 v224, v232, v233\n"
"v_cvt_pk_bf16_f32 v225, v234, v235\n"
"v_mfma_f32_16x16x32_bf16 v[208:211], a[228:231], a[84:87], v[208:211]\n"
"v_exp_f32 v232, v204\n"
"v_mfma_f32_16x16x32_bf16 v[208:211], a[232:235], a[88:91], v[208:211]\n"
"v_exp_f32 v233, v205\n"
"v_mfma_f32_16x16x32_bf16 v[208:211], a[236:239], a[92:95], v[208:211]\n"
"s_nop 0\n"
"v_cmp_nge_f32 vcc, v254, v228\n"
"v_cvt_pk_bf16_f32 v226, v232, v233\n"
"v_mfma_f32_16x16x32_bf16 v[212:215], a[240:243], a[80:83], v[172:175]\n"
"v_exp_f32 v234, v206\n"
"v_mfma_f32_16x16x32_bf16 v[212:215], a[244:247], a[84:87], v[212:215]\n"
"v_exp_f32 v235, v207\n"
"v_mfma_f32_16x16x32_bf16 v[212:215], a[248:251], a[88:91], v[212:215]\n"
"v_exp_f32 v232, v208\n"
"v_mfma_f32_16x16x32_bf16 v[212:215], a[252:255], a[92:95], v[212:215]\n"
"s_cbranch_vccnz .Lstub_14\n"
".Lback_14:\n"
"v_mfma_f32_16x16x32_bf16 v[32:35], v[216:219], a[128:131], v[32:35]\n"
"v_add_f32 v242, v242, |v228|\n"
"v_cvt_pk_bf16_f32 v227, v234, v235\n"
"v_mfma_f32_16x16x32_bf16 v[32:35], v[220:223], a[132:135], v[32:35]\n"
"v_exp_f32 v233, v209\n"
"v_mfma_f32_16x16x32_bf16 v[36:39], v[216:219], a[136:139], v[36:39]\n"
"v_exp_f32 v234, v210\n"
"v_mfma_f32_16x16x32_bf16 v[36:39], v[220:223], a[140:143], v[36:39]\n"
"v_exp_f32 v235, v211\n"
"v_mfma_f32_16x16x32_bf16 v[40:43], v[216:219], a[144:147], v[40:43]\n"
"s_add_u32 m0, s58, 0\n"
"v_cvt_pk_bf16_f32 v228, v232, v233\n"
"v_cvt_pk_bf16_f32 v229, v234, v235\n"
"v_mfma_f32_16x16x32_bf16 v[40:43], v[220:223], a[148:151], v[40:43]\n"
"buffer_load_dwordx4 v252, s[36:39], s56 offen offset:0  lds\n"
"buffer_load_dwordx4 v253, s[36:39], s56 offen offset:1024  lds\n"
"v_mfma_f32_16x16x32_bf16 v[44:47], v[216:219], a[152:155], v[44:47]\n"
"s_add_u32 s56, s56, 20480\n"
"v_exp_f32 v232, v212\n"
"v_mfma_f32_16x16x32_bf16 v[44:47], v[220:223], a[156:159], v[44:47]\n"
"v_exp_f32 v233, v213\n"
"v_mfma_f32_16x16x32_bf16 v[48:51], v[216:219], a[160:163], v[48:51]\n"
"v_exp_f32 v234, v214\n"
"v_mfma_f32_16x16x32_bf16 v[48:51], v[220:223], a[164:167], v[48:51]\n"
"v_exp_f32 v235, v215\n"
"v_mfma_f32_16x16x32_bf16 v[52:55], v[216:219], a[168:171], v[52:55]\n"
"v_cvt_pk_bf16_f32 v230, v232, v233\n"
"v_cvt_pk_bf16_f32 v231, v234, v235\n"
"v_mfma_f32_16x16x32_bf16 v[52:55], v[220:223], a[172:175], v[52:55]\n"
"v_mfma_f32_16x16x32_bf16 v[56:59], v[216:219], a[176:179], v[56:59]\n"
"v_mfma_f32_16x16x32_bf16 v[56:59], v[220:223], a[180:183], v[56:59]\n"
"v_mfma_f32_16x16x32_bf16 v[60:63], v[216:219], a[184:187], v[60:63]\n"
"v_mfma_f32_16x16x32_bf16 v[60:63], v[220:223], a[188:191], v[60:63]\n"
"v_mfma_f32_16x16x32_bf16 v[184:187], a[192:195], a[96:99], v[176:179]\n"
"v_mfma_f32_16x16x32_bf16 v[184:187], a[196:199], a[100:103], v[184:187]\n"
"v_mfma_f32_16x16x32_bf16 v[184:187], a[200:203], a[104:107], v[184:187]\n"
"s_add_u32 m0, s58, 2048\n"
"v_mfma_f32_16x16x32_bf16 v[184:187], a[204:207], a[108:111], v[184:187]\n"
"buffer_load_dwordx4 v252, s[36:39], s56 offen offset:0  lds\n"
"buffer_load_dwordx4 v253, s[36:39], s56 offen offset:1024  lds\n"
"v_mfma_f32_16x16x32_bf16 v[188:191], a[208:211], a[96:99], v[176:179]\n"
"s_add_u32 s56, s56, 20480\n"
"v_mfma_f32_16x16x32_bf16 v[188:191], a[212:215], a[100:103], v[188:191]\n"
"s_nop 1\n"
"v_mfma_f32_16x16x32_bf16 v[188:191], a[216:219], a[104:107], v[188:191]\n"
"v_exp_f32 v232, v184\n"
"v_mfma_f32_16x16x32_bf16 v[188:191], a[220:223], a[108:111], v[188:191]\n"
"v_exp_f32 v233, v185\n"
"v_mfma_f32_16x16x32_bf16 v[220:223], v[236:239], v[224:227], 0\n"
"v_exp_f32 v234, v186\n"
"v_mfma_f32_16x16x32_bf16 v[220:223], v[236:239], v[228:231], v[220:223]\n"
"v_exp_f32 v235, v187\n"
"v_mfma_f32_16x16x32_bf16 v[192:195], a[224:227], a[96:99], v[176:179]\n"
"v_cvt_pk_bf16_f32 v216, v232, v233\n"
"v_cvt_pk_bf16_f32 v217, v234, v235\n"
"v_mfma_f32_16x16x32_bf16 v[192:195], a[228:231], a[100:103], v[192:195]\n"
"v_exp_f32 v232, v188\n"
"v_mfma_f32_16x16x32_bf16 v[192:195], a[232:235], a[104:107], v[192:195]\n"
"v_exp_f32 v233, v189\n"
"v_mfma_f32_16x16x32_bf16 v[192:195], a[236:239], a[108:111], v[192:195]\n"
"s_nop 0\n"
"v_cmp_nge_f32 vcc, v254, v220\n"
"v_cvt_pk_bf16_f32 v218, v232, v233\n"
"v_mfma_f32_16x16x32_bf16 v[196:199], a[240:243], a[96:99], v[176:179]\n"
"v_exp_f32 v234, v190\n"
"v_mfma_f32_16x16x32_bf16 v[196:199], a[244:247], a[100:103], v[196:199]\n"
"v_exp_f32 v235, v191\n"
"v_mfma_f32_16x16x32_bf16 v[196:199], a[248:251], a[104:107], v[196:199]\n"
"v_exp_f32 v232, v192\n"
"v_mfma_f32_16x16x32_bf16 v[196:199], a[252:255], a[108:111], v[196:199]\n"
"s_cbranch_vccnz .Lstub_15\n"
".Lback_15:\n"
"v_mfma_f32_16x16x32_bf16 v[64:67], v[224:227], a[128:131], v[64:67]\n"
"s_add_u32 m0, s58, 4096\n"
"v_add_f32 v243, v243, |v220|\n"
"v_cvt_pk_bf16_f32 v219, v234, v235\n"
"v_mfma_f32_16x16x32_bf16 v[64:67], v[228:231], a[132:135], v[64:67]\n"
"buffer_load_dwordx4 v252, s[36:39], s56 offen offset:0  lds\n"
"buffer_load_dwordx4 v253, s[36:39], s56 offen offset:1024  lds\n"
"v_mfma_f32_16x16x32_bf16 v[68:71], v[224:227], a[136:139], v[68:71]\n"
"s_add_u32 s56, s56, 20480\n"
"v_exp_f32 v233, v193\n"
"v_mfma_f32_16x16x32_bf16 v[68:71], v[228:231], a[140:143], v[68:71]\n"
"v_exp_f32 v234, v194\n"
"v_mfma_f32_16x16x32_bf16 v[72:75], v[224:227], a[144:147], v[72:75]\n"
"v_exp_f32 v235, v195\n"
"v_mfma_f32_16x16x32_bf16 v[72:75], v[228:231], a[148:151], v[72:75]\n"
"v_cvt_pk_bf16_f32 v220, v232, v233\n"
"v_cvt_pk_bf16_f32 v221, v234, v235\n"
"v_mfma_f32_16x16x32_bf16 v[76:79], v[224:227], a[152:155], v[76:79]\n"
"s_memrealtime s[94:95]\n"
"v_exp_f32 v232, v196\n"
"v_mfma_f32_16x16x32_bf16 v[76:79], v[228:231], a[156:159], v[76:79]\n"
"v_exp_f32 v233, v197\n"
"v_mfma_f32_16x16x32_bf16 v[80:83], v[224:227], a[160:163], v[80:83]\n"
"v_exp_f32 v234, v198\n"
"v_mfma_f32_16x16x32_bf16 v[80:83], v[228:231], a[164:167], v[80:83]\n"
"v_exp_f32 v235, v199\n"
"v_mfma_f32_16x16x32_bf16 v[84:87], v[224:227], a[168:171], v[84:87]\n"
"s_waitcnt lgkmcnt(0)\n"
"v_cvt_pk_bf16_f32 v222, v232, v233\n"
"v_cvt_pk_bf16_f32 v223, v234, v235\n"
"v_mfma_f32_16x16x32_bf16 v[84:87], v[228:231], a[172:175], v[84:87]\n"
"s_sub_u32 s94, s94, s98\n"
"v_mfma_f32_16x16x32_bf16 v[88:91], v[224:227], a[176:179], v[88:91]\n"
"s_add_u32 m0, s58, 6144\n"
"v_mfma_f32_16x16x32_bf16 v[88:91], v[228:231], a[180:183], v[88:91]\n"
"buffer_load_dwordx4 v252, s[36:39], s56 offen offset:0  lds\n"
"buffer_load_dwordx4 v253, s[36:39], s56 offen offset:1024  lds\n"
"v_mfma_f32_16x16x32_bf16 v[92:95], v[224:227], a[184:187], v[92:95]\n"
"s_add_u32 s56, s56, 20480\n"
"v_mfma_f32_16x16x32_bf16 v[92:95], v[228:231], a[188:191], v[92:95]\n"
"s_cmp_lt_i32 s94, 0\n"
"s_cbranch_scc1 .Lpa4\n"
".Lpa4x:\n"
"s_waitcnt vmcnt(16)\n"
"s_barrier\n"
"v_mfma_f32_16x16x32_bf16 v[200:203], a[192:195], a[112:115], v[180:183]\n"
"s_add_u32 s98, s98, 111\n"
"ds_read_b128 a[192:195], v246 offset:32768\n"
"v_mfma_f32_16x16x32_bf16 v[200:203], a[196:199], a[116:119], v[200:203]\n"
"s_lshr_b32 s95, s63, 3\n"
"ds_read_b128 a[196:199], v247 offset:32768\n"
"v_mfma_f32_16x16x32_bf16 v[200:203], a[200:203], a[120:123], v[200:203]\n"
"s_sub_u32 s63, s63, s95\n"
"ds_read_b128 a[200:203], v246 offset:33280\n"
"v_mfma_f32_16x16x32_bf16 v[200:203], a[204:207], a[124:127], v[200:203]\n"
"s_nop 1\n"
"ds_read_b128 a[204:207], v247 offset:33280\n"
"v_mfma_f32_16x16x32_bf16 v[204:207], a[208:211], a[112:115], v[180:183]\n"
"s_add_u32 s98, s98, s95\n"
"ds_read_b128 a[208:211], v246 offset:36864\n"
"v_mfma_f32_16x16x32_bf16 v[204:207], a[212:215], a[116:119], v[204:207]\n"
"s_sub_u32 s94, s94, 18\n"
"ds_read_b128 a[212:215], v247 offset:36864\n"
"v_mfma_f32_16x16x32_bf16 v[204:207], a[216:219], a[120:123], v[204:207]\n"
"s_max_i32 s94, s94, 0\n"
"v_exp_f32 v232, v200\n"
"v_mfma_f32_16x16x32_bf16 v[204:207], a[220:223], a[124:127], v[204:207]\n"
"v_exp_f32 v233, v201\n"
"v_mfma_f32_16x16x32_bf16 v[228:231], v[236:239], v[216:219], 0\n"
"s_add_u32 m0, s58, 131072\n"
"v_exp_f32 v234, v202\n"
"v_mfma_f32_16x16x32_bf16 v[228:231], v[236:239], v[220:223], v[228:231]\n"
"buffer_load_dwordx4 v252, s[36:39], s56 offen offset:0  lds\n"
"buffer_load_dwordx4 v253, s[36:39], s56 offen offset:1024  lds\n"
"v_mfma_f32_16x16x32_bf16 v[208:211], a[224:227], a[112:115], v[180:183]\n"
"s_add_u32 s56, s56, 20480\n"
"ds_read_b128 a[216:219], v246 offset:37376\n"
"v_cvt_pk_bf16_f32 v224, v232, v233\n"
"v_mfma_f32_16x16x32_bf16 v[208:211], a[228:231], a[116:119], v[208:211]\n"
"v_exp_f32 v235, v203\n"
"v_mfma_f32_16x16x32_bf16 v[208:211], a[232:235], a[120:123], v[208:211]\n"
"ds_read_b128 a[220:223], v247 offset:37376\n"
"v_cvt_pk_bf16_f32 v225, v234, v235\n"
"v_mfma_f32_16x16x32_bf16 v[208:211], a[236:239], a[124:127], v[208:211]\n"
"ds_read_b128 a[224:227], v246 offset:40960\n"
"v_cmp_nge_f32 vcc, v254, v228\n"
"v_mfma_f32_16x16x32_bf16 v[212:215], a[240:243], a[112:115], v[180:183]\n"
"v_exp_f32 v232, v204\n"
"v_mfma_f32_16x16x32_bf16 v[212:215], a[244:247], a[116:119], v[212:215]\n"
"v_exp_f32 v233, v205\n"
"v_mfma_f32_16x16x32_bf16 v[212:215], a[248:251], a[120:123], v[212:215]\n"
"v_exp_f32 v234, v206\n"
"v_mfma_f32_16x16x32_bf16 v[212:215], a[252:255], a[124:127], v[212:215]\n"
"s_cbranch_vccnz .Lstub_16\n"
".Lback_16:\n"
"v_mfma_f32_16x16x32_bf16 v[96:99], v[216:219], a[128:131], v[96:99]\n"
"v_add_f32 v244, v244, |v228|\n"
"v_cvt_pk_bf16_f32 v226, v232, v233\n"
"v_mfma_f32_16x16x32_bf16 v[96:99], v[220:223], a[132:135], v[96:99]\n"
"v_exp_f32 v235, v207\n"
"v_mfma_f32_16x16x32_bf16 v[100:103], v[216:219], a[136:139], v[100:103]\n"
"ds_read_b128 a[228:231], v247 offset:40960\n"
"v_cvt_pk_bf16_f32 v227, v234, v235\n"
"v_mfma_f32_16x16x32_bf16 v[100:103], v[220:223], a[140:143], v[100:103]\n"
"v_exp_f32 v232, v208\n"
"v_mfma_f32_16x16x32_bf16 v[104:107], v[216:219], a[144:147], v[104:107]\n"
"s_add_u32 m0, s58, 133120\n"
"v_exp_f32 v233, v209\n"
"v_mfma_f32_16x16x32_bf16 v[104:107], v[220:223], a[148:151], v[104:107]\n"
"buffer_load_dwordx4 v252, s[36:39], s56 offen offset:0  lds\n"
"buffer_load_dwordx4 v253, s[36:39], s56 offen offset:1024  lds\n"
"v_mfma_f32_16x16x32_bf16 v[108:111], v[216:219], a[152:155], v[108:111]\n"
"s_add_u32 s56, s56, 20480\n"
"v_exp_f32 v234, v210\n"
"v_mfma_f32_16x16x32_bf16 v[108:111], v[220:223], a[156:159], v[108:111]\n"
"ds_read_b128 a[232:235], v246 offset:41472\n"
"v_cvt_pk_bf16_f32 v228, v232, v233\n"
"v_mfma_f32_16x16x32_bf16 v[112:115], v[216:219], a[160:163], v[112:115]\n"
"v_exp_f32 v235, v211\n"
"v_mfma_f32_16x16x32_bf16 v[112:115], v[220:223], a[164:167], v[112:115]\n"
"ds_read_b128 a[236:239], v247 offset:41472\n"
"v_cvt_pk_bf16_f32 v229, v234, v235\n"
"v_mfma_f32_16x16x32_bf16 v[116:119], v[216:219], a[168:171], v[116:119]\n"
"v_exp_f32 v232, v212\n"
"v_mfma_f32_16x16x32_bf16 v[116:119], v[220:223], a[172:175], v[116:119]\n"
"v_exp_f32 v233, v213\n"
"v_mfma_f32_16x16x32_bf16 v[120:123], v[216:219], a[176:179], v[120:123]\n"
"v_exp_f32 v234, v214\n"
"v_mfma_f32_16x16x32_bf16 v[120:123], v[220:223], a[180:183], v[120:123]\n"
"ds_read_b128 a[240:243], v246 offset:45056\n"
"v_cvt_pk_bf16_f32 v230, v232, v233\n"
"v_mfma_f32_16x16x32_bf16 v[124:127], v[216:219], a[184:187], v[124:127]\n"
"v_exp_f32 v235, v215\n"
"v_mfma_f32_16x16x32_bf16 v[124:127], v[220:223], a[188:191], v[124:127]\n"
"s_waitcnt lgkmcnt(2)\n"
"ds_read_b128 a[244:247], v247 offset:45056\n"
"ds_read_b128 a[248:251], v246 offset:45568\n"
"v_mfma_f32_16x16x32_bf16 v[184:187], a[192:195], a[32:35], v[160:163]\n"
"s_cmp_gt_u32 s94, s99\n"
"s_cselect_b32 s63, 110, s63\n"
"s_cselect_b32 s95, 100, 0\n"
"v_cvt_pk_bf16_f32 v231, v234, v235\n"
"v_mfma_f32_16x16x32_bf16 v[184:187], a[196:199], a[36:39], v[184:187]\n"
"s_add_u32 s94, s94, s95\n"
"ds_read_b128 a[252:255], v247 offset:45568\n"
"v_mfma_f32_16x16x32_bf16 v[184:187], a[200:203], a[40:43], v[184:187]\n"
"s_add_u32 m0, s58, 135168\n"
"v_mfma_f32_16x16x32_bf16 v[184:187], a[204:207], a[44:47], v[184:187]\n"
"buffer_load_dwordx4 v252, s[36:39], s56 offen offset:0  lds\n"
"buffer_load_dwordx4 v253, s[36:39], s56 offen offset:1024  lds\n"
"v_mfma_f32_16x16x32_bf16 v[188:191], a[208:211], a[32:35], v[160:163]\n"
"s_add_u32 s56, s56, 20480\n"
"v_mfma_f32_16x16x32_bf16 v[188:191], a[212:215], a[36:39], v[188:191]\n"
"s_nop 1\n"
"v_mfma_f32_16x16x32_bf16 v[188:191], a[216:219], a[40:43], v[188:191]\n"
"s_add_u32 s98, s98, s94\n"
"v_exp_f32 v232, v184\n"
"v_mfma_f32_16x16x32_bf16 v[188:191], a[220:223], a[44:47], v[188:191]\n"
"v_exp_f32 v233, v185\n"
"v_mfma_f32_16x16x32_bf16 v[220:223], v[236:239], v[224:227], 0\n"
"v_exp_f32 v234, v186\n"
"v_mfma_f32_16x16x32_bf16 v[220:223], v[236:239], v[228:231], v[220:223]\n"
"v_exp_f32 v235, v187\n"
"v_mfma_f32_16x16x32_bf16 v[192:195], a[224:227], a[32:35], v[160:163]\n"
"v_cvt_pk_bf16_f32 v216, v232, v233\n"
"v_cvt_pk_bf16_f32 v217, v234, v235\n"
"v_mfma_f32_16x16x32_bf16 v[192:195], a[228:231], a[36:39], v[192:195]\n"
"v_exp_f32 v232, v188\n"
"v_mfma_f32_16x16x32_bf16 v[192:195], a[232:235], a[40:43], v[192:195]\n"
"s_waitcnt lgkmcnt(0)\n"
"v_exp_f32 v233, v189\n"
"v_mfma_f32_16x16x32_bf16 v[192:195], a[236:239], a[44:47], v[192:195]\n"
"s_waitcnt vmcnt(14) lgkmcnt(0)\n"
"s_barrier\n"
"v_mfma_f32_16x16x32_bf16 v[196:199], a[240:243], a[32:35], v[160:163]\n"
"v_cmp_nge_f32 vcc, v254, v220\n"
"v_cvt_pk_bf16_f32 v218, v232, v233\n"
"v_mfma_f32_16x16x32_bf16 v[196:199], a[244:247], a[36:39], v[196:199]\n"
"v_exp_f32 v234, v190\n"
"v_mfma_f32_16x16x32_bf16 v[196:199], a[248:251], a[40:43], v[196:199]\n"
"v_exp_f32 v235, v191\n"
"v_mfma_f32_16x16x32_bf16 v[196:199], a[252:255], a[44:47], v[196:199]\n"
"s_cbranch_vccnz .Lstub_17\n"
".Lback_17:\n"
"v_mfma_f32_16x16x32_bf16 v[128:131], v[224:227], a[128:131], v[128:131]\n"
"s_add_u32 m0, s58, 137216\n"
"ds_read_b64_tr_b16 a[128:129], v248 offset:0\n"
"ds_read_b64_tr_b16 a[130:131], v248 offset:4096\n"
"v_mfma_f32_16x16x32_bf16 v[128:131], v[228:231], a[132:135], v[128:131]\n"
"buffer_load_dwordx4 v252, s[36:39], s56 offen offset:0  lds\n"
"buffer_load_dwordx4 v253, s[36:39], s56 offen offset:1024  lds\n"
"v_mfma_f32_16x16x32_bf16 v[132:135], v[224:227], a[136:139], v[132:135]\n"
"s_add_u32 s56, s56, 20480\n"
"ds_read_b64_tr_b16 a[132:133], v248 offset:8192\n"
"ds_read_b64_tr_b16 a[134:135], v248 offset:12288\n"
"v_mfma_f32_16x16x32_bf16 v[132:135], v[228:231], a[140:143], v[132:135]\n"
"v_add_f32 v245, v245, |v220|\n"
"v_cvt_pk_bf16_f32 v219, v234, v235\n"
"v_mfma_f32_16x16x32_bf16 v[136:139], v[224:227], a[144:147], v[136:139]\n"
"ds_read_b64_tr_b16 a[136:137], v249 offset:0\n"
"ds_read_b64_tr_b16 a[138:139], v249 offset:4096\n"
"v_mfma_f32_16x16x32_bf16 v[136:139], v[228:231], a[148:151], v[136:139]\n"
"v_exp_f32 v232, v192\n"
"v_mfma_f32_16x16x32_bf16 v[140:143], v[224:227], a[152:155], v[140:143]\n"
"ds_read_b64_tr_b16 a[140:141], v249 offset:8192\n"
"ds_read_b64_tr_b16 a[142:143], v249 offset:12288\n"
"v_mfma_f32_16x16x32_bf16 v[140:143], v[228:231], a[156:159], v[140:143]\n"
"v_exp_f32 v233, v193\n"
"v_mfma_f32_16x16x32_bf16 v[144:147], v[224:227], a[160:163], v[144:147]\n"
"ds_read_b64_tr_b16 a[144:145], v250 offset:0\n"
"ds_read_b64_tr_b16 a[146:147], v250 offset:4096\n"
"v_mfma_f32_16x16x32_bf16 v[144:147], v[228:231], a[164:167], v[144:147]\n"
"v_exp_f32 v234, v194\n"
"v_mfma_f32_16x16x32_bf16 v[148:151], v[224:227], a[168:171], v[148:151]\n"
"ds_read_b64_tr_b16 a[148:149], v250 offset:8192\n"
"ds_read_b64_tr_b16 a[150:151], v250 offset:12288\n"
"v_mfma_f32_16x16x32_bf16 v[148:151], v[228:231], a[172:175], v[148:151]\n"
"v_cvt_pk_bf16_f32 v220, v232, v233\n"
"v_mbcnt_lo_u32_b32 v246, -1, 0\n"
"v_mfma_f32_16x16x32_bf16 v[152:155], v[224:227], a[176:179], v[152:155]\n"
"s_add_u32 m0, s58, 65536\n"
"v_exp_f32 v235, v195\n"
"v_mfma_f32_16x16x32_bf16 v[152:155], v[228:231], a[180:183], v[152:155]\n"
"buffer_load_dwordx4 v252, s[36:39], s57 offen offset:0  lds\n"
"buffer_load_dwordx4 v253, s[36:39], s57 offen offset:1024  lds\n"
"v_mfma_f32_16x16x32_bf16 v[156:159], v[224:227], a[184:187], v[156:159]\n"
"s_add_u32 s57, s57, 20480\n"
"ds_read_b64_tr_b16 a[152:153], v251 offset:0\n"
"v_cvt_pk_bf16_f32 v221, v234, v235\n"
"v_mfma_f32_16x16x32_bf16 v[156:159], v[228:231], a[188:191], v[156:159]\n"
"v_exp_f32 v232, v196\n"
"v_mfma_f32_16x16x32_bf16 v[200:203], a[192:195], a[48:51], v[164:167]\n"
"ds_read_b64_tr_b16 a[154:155], v251 offset:4096\n"
"ds_read_b64_tr_b16 a[156:157], v251 offset:8192\n"
"v_mfma_f32_16x16x32_bf16 v[200:203], a[196:199], a[52:55], v[200:203]\n"
"v_exp_f32 v233, v197\n"
"v_mfma_f32_16x16x32_bf16 v[200:203], a[200:203], a[56:59], v[200:203]\n"
"v_exp_f32 v234, v198\n"
"v_mfma_f32_16x16x32_bf16 v[200:203], a[204:207], a[60:63], v[200:203]\n"
"ds_read_b64_tr_b16 a[158:159], v251 offset:12288\n"
"v_cvt_pk_bf16_f32 v222, v232, v233\n"
"v_mfma_f32_16x16x32_bf16 v[204:207], a[208:211], a[48:51], v[164:167]\n"
"v_exp_f32 v235, v199\n"
"v_mfma_f32_16x16x32_bf16 v[204:207], a[212:215], a[52:55], v[204:207]\n"
"ds_read_b64_tr_b16 a[160:161], v248 offset:512\n"
"v_cvt_pk_bf16_f32 v223, v234, v235\n"
"v_mfma_f32_16x16x32_bf16 v[204:207], a[216:219], a[56:59], v[204:207]\n"
"ds_read_b64_tr_b16 a[162:163], v248 offset:4608\n"
"v_mbcnt_hi_u32_b32 v246, -1, v246\n"
"v_mfma_f32_16x16x32_bf16 v[204:207], a[220:223], a[60:63], v[204:207]\n"
"ds_read_b64_tr_b16 a[164:165], v248 offset:8704\n"
"v_lshl_add_u32 v247, v246, 4, s53\n"
"v_mfma_f32_16x16x32_bf16 v[228:231], v[236:239], v[216:219], 0\n"
"s_add_u32 m0, s58, 67584\n"
"ds_read_b64_tr_b16 a[166:167], v248 offset:12800\n"
"v_lshl_add_u32 v246, v246, 3, s52\n"
"v_mfma_f32_16x16x32_bf16 v[228:231], v[236:239], v[220:223], v[228:231]\n"
"buffer_load_dwordx4 v252, s[36:39], s57 offen offset:0  lds\n"
"buffer_load_dwordx4 v253, s[36:39], s57 offen offset:1024  lds\n"
"v_mfma_f32_16x16x32_bf16 v[208:211], a[224:227], a[48:51], v[164:167]\n"
"s_add_u32 s57, s57, 20480\n"
"ds_read_b64_tr_b16 a[168:169], v249 offset:512\n"
"ds_read_b64_tr_b16 a[170:171], v249 offset:4608\n"
"v_mfma_f32_16x16x32_bf16 v[208:211], a[228:231], a[52:55], v[208:211]\n"
"v_exp_f32 v232, v200\n"
"v_mfma_f32_16x16x32_bf16 v[208:211], a[232:235], a[56:59], v[208:211]\n"
"ds_read_b64_tr_b16 a[172:173], v249 offset:8704\n"
"ds_read_b64_tr_b16 a[174:175], v249 offset:12800\n"
"v_mfma_f32_16x16x32_bf16 v[208:211], a[236:239], a[60:63], v[208:211]\n"
"v_exp_f32 v233, v201\n"
"v_mfma_f32_16x16x32_bf16 v[212:215], a[240:243], a[48:51], v[164:167]\n"
"ds_read_b64_tr_b16 a[176:177], v250 offset:512\n"
"v_cmp_nge_f32 vcc, v254, v228\n"
"v_mfma_f32_16x16x32_bf16 v[212:215], a[244:247], a[52:55], v[212:215]\n"
"ds_read_b64_tr_b16 a[178:179], v250 offset:4608\n"
"v_cvt_pk_bf16_f32 v224, v232, v233\n"
"v_mfma_f32_16x16x32_bf16 v[212:215], a[248:251], a[56:59], v[212:215]\n"
"s_waitcnt lgkmcnt(6)\n"
"v_exp_f32 v234, v202\n"
"v_mfma_f32_16x16x32_bf16 v[212:215], a[252:255], a[60:63], v[212:215]\n"
"s_cbranch_vccnz .Lstub_18\n"
".Lback_18:\n"
"v_mfma_f32_16x16x32_bf16 a[0:3], v[216:219], a[128:131], a[0:3]\n"
"ds_read_b64_tr_b16 a[180:181], v250 offset:8704\n"
"ds_read_b64_tr_b16 a[182:183], v250 offset:12800\n"
"v_mfma_f32_16x16x32_bf16 a[0:3], v[220:223], a[132:135], a[0:3]\n"
"v_exp_f32 v235, v203\n"
"v_mfma_f32_16x16x32_bf16 a[4:7], v[216:219], a[136:139], a[4:7]\n"
"ds_read_b64_tr_b16 a[184:185], v251 offset:512\n"
"ds_read_b64_tr_b16 a[186:187], v251 offset:4608\n"
"v_mfma_f32_16x16x32_bf16 a[4:7], v[220:223], a[140:143], a[4:7]\n"
"v_add_f32 v240, v240, |v228|\n"
"v_cvt_pk_bf16_f32 v225, v234, v235\n"
"v_mfma_f32_16x16x32_bf16 a[8:11], v[216:219], a[144:147], a[8:11]\n"
"ds_read_b64_tr_b16 a[188:189], v251 offset:8704\n"
"ds_read_b64_tr_b16 a[190:191], v251 offset:12800\n"
"v_mfma_f32_16x16x32_bf16 a[8:11], v[220:223], a[148:151], a[8:11]\n"
"v_exp_f32 v232, v204\n"
"v_mfma_f32_16x16x32_bf16 a[12:15], v[216:219], a[152:155], a[12:15]\n"
"s_add_u32 m0, s58, 69632\n"
"v_exp_f32 v233, v205\n"
"v_mfma_f32_16x16x32_bf16 a[12:15], v[220:223], a[156:159], a[12:15]\n"
"buffer_load_dwordx4 v252, s[36:39], s57 offen offset:0  lds\n"
"buffer_load_dwordx4 v253, s[36:39], s57 offen offset:1024  lds\n"
"v_mfma_f32_16x16x32_bf16 a[16:19], v[216:219], a[160:163], a[16:19]\n"
"s_add_u32 s57, s57, 20480\n"
"v_exp_f32 v234, v206\n"
"v_mfma_f32_16x16x32_bf16 a[16:19], v[220:223], a[164:167], a[16:19]\n"
"s_waitcnt lgkmcnt(2)\n"
"ds_write_b32 v246, v160 offset:38912\n"
"v_cvt_pk_bf16_f32 v226, v232, v233\n"
"v_mfma_f32_16x16x32_bf16 a[20:23], v[216:219], a[168:171], a[20:23]\n"
"v_exp_f32 v235, v207\n"
"v_mfma_f32_16x16x32_bf16 a[20:23], v[220:223], a[172:175], a[20:23]\n"
"s_memrealtime s[94:95]\n"
"ds_write_b32 v246, v240 offset:38916\n"
"v_cvt_pk_bf16_f32 v227, v234, v235\n"
"v_mfma_f32_16x16x32_bf16 a[24:27], v[216:219], a[176:179], a[24:27]\n"
"v_exp_f32 v232, v208\n"
"v_mfma_f32_16x16x32_bf16 a[24:27], v[220:223], a[180:183], a[24:27]\n"
"v_exp_f32 v233, v209\n"
"v_mfma_f32_16x16x32_bf16 a[28:31], v[216:219], a[184:187], a[28:31]\n"
"v_exp_f32 v234, v210\n"
"s_waitcnt lgkmcnt(0)\n"
"v_mfma_f32_16x16x32_bf16 a[28:31], v[220:223], a[188:191], a[28:31]\n"
"s_waitcnt lgkmcnt(0)\n"
"s_sub_u32 s94, s94, s98\n"
"s_cmp_lt_i32 s94, 0\n"
"s_cbranch_scc1 .Lpc4\n"
".Lpc4x:\n"
"s_waitcnt lgkmcnt(0)\n"
"s_barrier\n"
"v_mfma_f32_16x16x32_bf16 v[184:187], a[192:195], a[64:67], v[168:171]\n"
"s_add_u32 s98, s98, 111\n"
"ds_write_b128 v247, a[0:3] offset:0\n"
"v_cvt_pk_bf16_f32 v228, v232, v233\n"
"v_exp_f32 v235, v211\n"
"v_mfma_f32_16x16x32_bf16 v[184:187], a[196:199], a[68:71], v[184:187]\n"
"s_lshr_b32 s95, s63, 3\n"
"ds_write_b128 v247, a[4:7] offset:1024\n"
"v_cvt_pk_bf16_f32 v229, v234, v235\n"
"v_mfma_f32_16x16x32_bf16 v[184:187], a[200:203], a[72:75], v[184:187]\n"
"s_sub_u32 s63, s63, s95\n"
"v_exp_f32 v232, v212\n"
"v_mfma_f32_16x16x32_bf16 v[184:187], a[204:207], a[76:79], v[184:187]\n"
"s_add_u32 s98, s98, s95\n"
"v_exp_f32 v233, v213\n"
"v_mfma_f32_16x16x32_bf16 v[188:191], a[208:211], a[64:67], v[168:171]\n"
"s_sub_u32 s94, s94, 18\n"
"v_exp_f32 v234, v214\n"
"v_mfma_f32_16x16x32_bf16 v[188:191], a[212:215], a[68:71], v[188:191]\n"
"s_max_i32 s94, s94, 0\n"
"ds_write_b128 v247, a[8:11] offset:2048\n"
"v_cvt_pk_bf16_f32 v230, v232, v233\n"
"v_exp_f32 v235, v215\n"
"v_mfma_f32_16x16x32_bf16 v[188:191], a[216:219], a[72:75], v[188:191]\n"
"s_cmp_gt_u32 s94, s99\n"
"s_cselect_b32 s63, 110, s63\n"
"s_cselect_b32 s95, 100, 0\n"
"ds_write_b128 v247, a[12:15] offset:3072\n"
"v_cvt_pk_bf16_f32 v231, v234, v235\n"
"v_mfma_f32_16x16x32_bf16 v[188:191], a[220:223], a[76:79], v[188:191]\n"
"s_add_u32 s94, s94, s95\n"
"ds_write_b128 v247, a[16:19] offset:4096\n"
"v_and_b32 v248, 1023, v247\n"
"v_mfma_f32_16x16x32_bf16 v[220:223], v[236:239], v[224:227], 0\n"
"s_add_u32 s98, s98, s94\n"
"ds_write_b128 v247, a[20:23] offset:5120\n"
"v_bfe_u32 v249, v248, 6, 2\n"
"v_mfma_f32_16x16x32_bf16 v[220:223], v[236:239], v[228:231], v[220:223]\n"
"ds_write_b128 v247, a[24:27] offset:6144\n"
"v_lshrrev_b32 v250, 8, v248\n"
"v_mfma_f32_16x16x32_bf16 v[192:195], a[224:227], a[64:67], v[168:171]\n"
"v_exp_f32 v232, v184\n"
"v_mfma_f32_16x16x32_bf16 v[192:195], a[228:231], a[68:71], v[192:195]\n"
"v_exp_f32 v233, v185\n"
"v_mfma_f32_16x16x32_bf16 v[192:195], a[232:235], a[72:75], v[192:195]\n"
"ds_write_b128 v247, a[28:31] offset:7168\n"
"v_cvt_pk_bf16_f32 v216, v232, v233\n"
"v_mfma_f32_16x16x32_bf16 v[192:195], a[236:239], a[76:79], v[192:195]\n"
"v_cmp_nge_f32 vcc, v254, v220\n"
"v_cmp_eq_u32 s[54:55], v249, v250\n"
"v_mfma_f32_16x16x32_bf16 v[196:199], a[240:243], a[64:67], v[168:171]\n"
"v_exp_f32 v234, v186\n"
"v_mfma_f32_16x16x32_bf16 v[196:199], a[244:247], a[68:71], v[196:199]\n"
"v_exp_f32 v235, v187\n"
"v_mfma_f32_16x16x32_bf16 v[196:199], a[248:251], a[72:75], v[196:199]\n"
"v_cvt_pk_bf16_f32 v217, v234, v235\n"
"v_and_b32 v249, 16, v248\n"
"v_mfma_f32_16x16x32_bf16 v[196:199], a[252:255], a[76:79], v[196:199]\n"
"s_cbranch_vccnz .Lstub_19\n"
".Lback_19:\n"
"v_mfma_f32_16x16x32_bf16 v[0:3], v[224:227], a[128:131], v[0:3]\n"
"v_add_f32 v241, v241, |v220|\n"
"v_mov_b32 v250, 0xffff\n"
"v_mfma_f32_16x16x32_bf16 v[0:3], v[228:231], a[132:135], v[0:3]\n"
"s_nop 0\n"
"v_exp_f32 v232, v188\n"
"v_mfma_f32_16x16x32_bf16 v[4:7], v[224:227], a[136:139], v[4:7]\n"
"v_exp_f32 v233, v189\n"
"v_mfma_f32_16x16x32_bf16 v[4:7], v[228:231], a[140:143], v[4:7]\n"
"v_exp_f32 v234, v190\n"
"v_mfma_f32_16x16x32_bf16 v[8:11], v[224:227], a[144:147], v[8:11]\n"
"v_cvt_pk_bf16_f32 v218, v232, v233\n"
"v_cvt_pk_bf16_f32 v0, v0, v1\n"
"v_mfma_f32_16x16x32_bf16 v[8:11], v[228:231], a[148:151], v[8:11]\n"
"v_exp_f32 v235, v191\n"
"v_mfma_f32_16x16x32_bf16 v[12:15], v[224:227], a[152:155], v[12:15]\n"
"v_cvt_pk_bf16_f32 v219, v234, v235\n"
"v_cvt_pk_bf16_f32 v1, v2, v3\n"
"v_mfma_f32_16x16x32_bf16 v[12:15], v[228:231], a[156:159], v[12:15]\n"
"v_exp_f32 v232, v192\n"
"v_mfma_f32_16x16x32_bf16 v[16:19], v[224:227], a[160:163], v[16:19]\n"
"v_exp_f32 v233, v193\n"
"v_mfma_f32_16x16x32_bf16 v[16:19], v[228:231], a[164:167], v[16:19]\n"
"v_exp_f32 v234, v194\n"
"v_mfma_f32_16x16x32_bf16 v[20:23], v[224:227], a[168:171], v[20:23]\n"
"ds_write_b64 v246, v[0:1] offset:0\n"
"v_cvt_pk_bf16_f32 v220, v232, v233\n"
"v_mfma_f32_16x16x32_bf16 v[20:23], v[228:231], a[172:175], v[20:23]\n"
"v_exp_f32 v235, v195\n"
"v_mfma_f32_16x16x32_bf16 v[24:27], v[224:227], a[176:179], v[24:27]\n"
"v_cvt_pk_bf16_f32 v221, v234, v235\n"
"v_cvt_pk_bf16_f32 v4, v4, v5\n"
"v_mfma_f32_16x16x32_bf16 v[24:27], v[228:231], a[180:183], v[24:27]\n"
"v_exp_f32 v232, v196\n"
"v_mfma_f32_16x16x32_bf16 v[28:31], v[224:227], a[184:187], v[28:31]\n"
"v_cvt_pk_bf16_f32 v5, v6, v7\n"
"v_cvt_pk_bf16_f32 v8, v8, v9\n"
"v_mfma_f32_16x16x32_bf16 v[28:31], v[228:231], a[188:191], v[28:31]\n"
"v_exp_f32 v233, v197\n"
"v_mfma_f32_16x16x32_bf16 v[200:203], a[192:195], a[80:83], v[172:175]\n"
"ds_write_b64 v246, v[4:5] offset:512\n"
"v_cvt_pk_bf16_f32 v9, v10, v11\n"
"v_mfma_f32_16x16x32_bf16 v[200:203], a[196:199], a[84:87], v[200:203]\n"
"v_exp_f32 v234, v198\n"
"v_mfma_f32_16x16x32_bf16 v[200:203], a[200:203], a[88:91], v[200:203]\n"
"ds_write_b64 v246, v[8:9] offset:1024\n"
"v_cvt_pk_bf16_f32 v222, v232, v233\n"
"v_mfma_f32_16x16x32_bf16 v[200:203], a[204:207], a[92:95], v[200:203]\n"
"v_exp_f32 v235, v199\n"
"v_mfma_f32_16x16x32_bf16 v[204:207], a[208:211], a[80:83], v[172:175]\n"
"ds_write_b32 v246, v164 offset:39424\n"
"v_cvt_pk_bf16_f32 v223, v234, v235\n"
"v_mfma_f32_16x16x32_bf16 v[204:207], a[212:215], a[84:87], v[204:207]\n"
"ds_write_b32 v246, v241 offset:39428\n"
"v_cvt_pk_bf16_f32 v12, v12, v13\n"
"v_mfma_f32_16x16x32_bf16 v[204:207], a[216:219], a[88:91], v[204:207]\n"
"v_cvt_pk_bf16_f32 v13, v14, v15\n"
"v_cvt_pk_bf16_f32 v16, v16, v17\n"
"v_mfma_f32_16x16x32_bf16 v[204:207], a[220:223], a[92:95], v[204:207]\n"
"ds_write_b64 v246, v[12:13] offset:1536\n"
"v_cvt_pk_bf16_f32 v17, v18, v19\n"
"v_mfma_f32_16x16x32_bf16 v[228:231], v[236:239], v[216:219], 0\n"
"ds_write_b64 v246, v[16:17] offset:2048\n"
"v_cvt_pk_bf16_f32 v20, v20, v21\n"
"v_mfma_f32_16x16x32_bf16 v[228:231], v[236:239], v[220:223], v[228:231]\n"
"v_cvt_pk_bf16_f32 v21, v22, v23\n"
"v_cvt_pk_bf16_f32 v24, v24, v25\n"
"v_mfma_f32_16x16x32_bf16 v[208:211], a[224:227], a[80:83], v[172:175]\n"
"ds_write_b64 v246, v[20:21] offset:2560\n"
"v_cvt_pk_bf16_f32 v25, v26, v27\n"
"v_mfma_f32_16x16x32_bf16 v[208:211], a[228:231], a[84:87], v[208:211]\n"
"ds_write_b64 v246, v[24:25] offset:3072\n"
"v_cvt_pk_bf16_f32 v28, v28, v29\n"
"v_mfma_f32_16x16x32_bf16 v[208:211], a[232:235], a[88:91], v[208:211]\n"
"v_cvt_pk_bf16_f32 v29, v30, v31\n"
"v_lshlrev_b32 v249, v249, v250\n"
"v_mfma_f32_16x16x32_bf16 v[208:211], a[236:239], a[92:95], v[208:211]\n"
"ds_write_b64 v246, v[28:29] offset:3584\n"
"v_cmp_nge_f32 vcc, v254, v228\n"
"v_mfma_f32_16x16x32_bf16 v[212:215], a[240:243], a[80:83], v[172:175]\n"
"v_exp_f32 v232, v200\n"
"v_mfma_f32_16x16x32_bf16 v[212:215], a[244:247], a[84:87], v[212:215]\n"
"v_exp_f32 v233, v201\n"
"v_mfma_f32_16x16x32_bf16 v[212:215], a[248:251], a[88:91], v[212:215]\n"
"v_exp_f32 v234, v202\n"
"v_mfma_f32_16x16x32_bf16 v[212:215], a[252:255], a[92:95], v[212:215]\n"
"s_cbranch_vccnz .Lstub_20\n"
".Lback_20:\n"
"v_mfma_f32_16x16x32_bf16 v[32:35], v[216:219], a[128:131], v[32:35]\n"
"v_add_f32 v242, v242, |v228|\n"
"v_cvt_pk_bf16_f32 v224, v232, v233\n"
"v_mfma_f32_16x16x32_bf16 v[32:35], v[220:223], a[132:135], v[32:35]\n"
"v_exp_f32 v235, v203\n"
"v_mfma_f32_16x16x32_bf16 v[36:39], v[216:219], a[136:139], v[36:39]\n"
"ds_write_b32 v246, v168 offset:39936\n"
"v_cvt_pk_bf16_f32 v225, v234, v235\n"
"v_mfma_f32_16x16x32_bf16 v[36:39], v[220:223], a[140:143], v[36:39]\n"
"v_exp_f32 v232, v204\n"
"v_mfma_f32_16x16x32_bf16 v[40:43], v[216:219], a[144:147], v[40:43]\n"
"v_exp_f32 v233, v205\n"
"v_mfma_f32_16x16x32_bf16 v[40:43], v[220:223], a[148:151], v[40:43]\n"
"v_exp_f32 v234, v206\n"
"v_mfma_f32_16x16x32_bf16 v[44:47], v[216:219], a[152:155], v[44:47]\n"
"v_cvt_pk_bf16_f32 v226, v232, v233\n"
"v_cvt_pk_bf16_f32 v32, v32, v33\n"
"v_mfma_f32_16x16x32_bf16 v[44:47], v[220:223], a[156:159], v[44:47]\n"
"v_exp_f32 v235, v207\n"
"v_mfma_f32_16x16x32_bf16 v[48:51], v[216:219], a[160:163], v[48:51]\n"
"v_cvt_pk_bf16_f32 v227, v234, v235\n"
"v_cvt_pk_bf16_f32 v33, v34, v35\n"
"v_mfma_f32_16x16x32_bf16 v[48:51], v[220:223], a[164:167], v[48:51]\n"
"v_exp_f32 v232, v208\n"
"v_mfma_f32_16x16x32_bf16 v[52:55], v[216:219], a[168:171], v[52:55]\n"
"v_exp_f32 v233, v209\n"
"v_mfma_f32_16x16x32_bf16 v[52:55], v[220:223], a[172:175], v[52:55]\n"
"v_exp_f32 v234, v210\n"
"v_mfma_f32_16x16x32_bf16 v[56:59], v[216:219], a[176:179], v[56:59]\n"
"ds_write_b64 v246, v[32:33] offset:4096\n"
"v_cvt_pk_bf16_f32 v228, v232, v233\n"
"v_mfma_f32_16x16x32_bf16 v[56:59], v[220:223], a[180:183], v[56:59]\n"
"v_exp_f32 v235, v211\n"
"v_mfma_f32_16x16x32_bf16 v[60:63], v[216:219], a[184:187], v[60:63]\n"
"v_cvt_pk_bf16_f32 v229, v234, v235\n"
"v_cvt_pk_bf16_f32 v36, v36, v37\n"
"v_mfma_f32_16x16x32_bf16 v[60:63], v[220:223], a[188:191], v[60:63]\n"
"v_exp_f32 v232, v212\n"
"v_mfma_f32_16x16x32_bf16 v[184:187], a[192:195], a[96:99], v[176:179]\n"
"v_cvt_pk_bf16_f32 v37, v38, v39\n"
"v_cvt_pk_bf16_f32 v40, v40, v41\n"
"v_mfma_f32_16x16x32_bf16 v[184:187], a[196:199], a[100:103], v[184:187]\n"
"v_exp_f32 v233, v213\n"
"v_mfma_f32_16x16x32_bf16 v[184:187], a[200:203], a[104:107], v[184:187]\n"
"ds_write_b64 v246, v[36:37] offset:4608\n"
"v_cvt_pk_bf16_f32 v41, v42, v43\n"
"v_mfma_f32_16x16x32_bf16 v[184:187], a[204:207], a[108:111], v[184:187]\n"
"v_exp_f32 v234, v214\n"
"v_mfma_f32_16x16x32_bf16 v[188:191], a[208:211], a[96:99], v[176:179]\n"
"ds_write_b64 v246, v[40:41] offset:5120\n"
"v_cvt_pk_bf16_f32 v230, v232, v233\n"
"v_mfma_f32_16x16x32_bf16 v[188:191], a[212:215], a[100:103], v[188:191]\n"
"v_exp_f32 v235, v215\n"
"v_mfma_f32_16x16x32_bf16 v[188:191], a[216:219], a[104:107], v[188:191]\n"
"ds_write_b32 v246, v242 offset:39940\n"
"v_cvt_pk_bf16_f32 v231, v234, v235\n"
"v_cvt_pk_bf16_f32 v44, v44, v45\n"
"v_cvt_pk_bf16_f32 v45, v46, v47\n"
"v_mfma_f32_16x16x32_bf16 v[188:191], a[220:223], a[108:111], v[188:191]\n"
"ds_write_b64 v246, v[44:45] offset:5632\n"
"v_cvt_pk_bf16_f32 v48, v48, v49\n"
"v_mfma_f32_16x16x32_bf16 v[220:223], v[236:239], v[224:227], 0\n"
"v_cvt_pk_bf16_f32 v49, v50, v51\n"
"v_cvt_pk_bf16_f32 v52, v52, v53\n"
"v_mfma_f32_16x16x32_bf16 v[220:223], v[236:239], v[228:231], v[220:223]\n"
"ds_write_b64 v246, v[48:49] offset:6144\n"
"v_cvt_pk_bf16_f32 v53, v54, v55\n"
"v_mfma_f32_16x16x32_bf16 v[192:195], a[224:227], a[96:99], v[176:179]\n"
"ds_write_b64 v246, v[52:53] offset:6656\n"
"v_cvt_pk_bf16_f32 v56, v56, v57\n"
"v_mfma_f32_16x16x32_bf16 v[192:195], a[228:231], a[100:103], v[192:195]\n"
"v_cvt_pk_bf16_f32 v57, v58, v59\n"
"v_cvt_pk_bf16_f32 v60, v60, v61\n"
"v_mfma_f32_16x16x32_bf16 v[192:195], a[232:235], a[104:107], v[192:195]\n"
"ds_write_b64 v246, v[56:57] offset:7168\n"
"v_cvt_pk_bf16_f32 v61, v62, v63\n"
"v_mfma_f32_16x16x32_bf16 v[192:195], a[236:239], a[108:111], v[192:195]\n"
"ds_write_b64 v246, v[60:61] offset:7680\n"
"v_cmp_nge_f32 vcc, v254, v220\n"
"v_mfma_f32_16x16x32_bf16 v[196:199], a[240:243], a[96:99], v[176:179]\n"
"v_exp_f32 v232, v184\n"
"v_mfma_f32_16x16x32_bf16 v[196:199], a[244:247], a[100:103], v[196:199]\n"
"v_exp_f32 v233, v185\n"
"v_mfma_f32_16x16x32_bf16 v[196:199], a[248:251], a[104:107], v[196:199]\n"
"v_exp_f32 v234, v186\n"
"v_mfma_f32_16x16x32_bf16 v[196:199], a[252:255], a[108:111], v[196:199]\n"
"s_cbranch_vccnz .Lstub_21\n"
".Lback_21:\n"
"v_mfma_f32_16x16x32_bf16 v[64:67], v[224:227], a[128:131], v[64:67]\n"
"v_add_f32 v243, v243, |v220|\n"
"v_cvt_pk_bf16_f32 v216, v232, v233\n"
"v_mfma_f32_16x16x32_bf16 v[64:67], v[228:231], a[132:135], v[64:67]\n"
"v_exp_f32 v235, v187\n"
"v_mfma_f32_16x16x32_bf16 v[68:71], v[224:227], a[136:139], v[68:71]\n"
"v_cvt_pk_bf16_f32 v217, v234, v235\n"
"v_cndmask_b32 v249, 0, v249, s[54:55]\n"
"v_mfma_f32_16x16x32_bf16 v[68:71], v[228:231], a[140:143], v[68:71]\n"
"v_exp_f32 v232, v188\n"
"v_mfma_f32_16x16x32_bf16 v[72:75], v[224:227], a[144:147], v[72:75]\n"
"v_exp_f32 v233, v189\n"
"v_mfma_f32_16x16x32_bf16 v[72:75], v[228:231], a[148:151], v[72:75]\n"
"v_exp_f32 v234, v190\n"
"v_mfma_f32_16x16x32_bf16 v[76:79], v[224:227], a[152:155], v[76:79]\n"
"v_bfe_i32 v251, v248, 5, 1\n"
"v_cvt_pk_bf16_f32 v218, v232, v233\n"
"v_mfma_f32_16x16x32_bf16 v[76:79], v[228:231], a[156:159], v[76:79]\n"
"v_exp_f32 v235, v191\n"
"v_mfma_f32_16x16x32_bf16 v[80:83], v[224:227], a[160:163], v[80:83]\n"
"v_and_b32 v250, v249, v251\n"
"v_xor_b32 v249, v249, v250\n"
"v_mfma_f32_16x16x32_bf16 v[80:83], v[228:231], a[164:167], v[80:83]\n"
"v_cvt_pk_bf16_f32 v219, v234, v235\n"
"v_xor_b32 v246, 16384, v246\n"
"v_mfma_f32_16x16x32_bf16 v[84:87], v[224:227], a[168:171], v[84:87]\n"
"v_exp_f32 v232, v192\n"
"v_mfma_f32_16x16x32_bf16 v[84:87], v[228:231], a[172:175], v[84:87]\n"
"v_exp_f32 v233, v193\n"
"v_mfma_f32_16x16x32_bf16 v[88:91], v[224:227], a[176:179], v[88:91]\n"
"v_xor_b32 v247, 16384, v247\n"
"v_cvt_pk_bf16_f32 v220, v232, v233\n"
"v_mfma_f32_16x16x32_bf16 v[88:91], v[228:231], a[180:183], v[88:91]\n"
"v_exp_f32 v234, v194\n"
"v_mfma_f32_16x16x32_bf16 v[92:95], v[224:227], a[184:187], v[92:95]\n"
"v_exp_f32 v235, v195\n"
"v_mfma_f32_16x16x32_bf16 v[92:95], v[228:231], a[188:191], v[92:95]\n"
"s_waitcnt lgkmcnt(0)\n"
"s_barrier\n"
"v_mfma_f32_16x16x32_bf16 v[200:203], a[192:195], a[112:115], v[180:183]\n"
"ds_read_b64 v[164:165], v246 offset:38912\n"
"ds_read_b64 v[166:167], v246 offset:39424\n"
"v_mfma_f32_16x16x32_bf16 v[200:203], a[196:199], a[116:119], v[200:203]\n"
"ds_read_b64 v[168:169], v246 offset:39936\n"
"v_cvt_pk_bf16_f32 v221, v234, v235\n"
"v_cvt_pk_bf16_f32 v64, v64, v65\n"
"v_mfma_f32_16x16x32_bf16 v[200:203], a[200:203], a[120:123], v[200:203]\n"
"ds_read_b128 v[32:35], v247 offset:0\n"
"v_exp_f32 v232, v196\n"
"v_cvt_pk_bf16_f32 v65, v66, v67\n"
"v_mfma_f32_16x16x32_bf16 v[200:203], a[204:207], a[124:127], v[200:203]\n"
"ds_read_b128 v[36:39], v247 offset:1024\n"
"v_exp_f32 v233, v197\n"
"v_mfma_f32_16x16x32_bf16 v[204:207], a[208:211], a[112:115], v[180:183]\n"
"ds_read_b128 v[40:43], v247 offset:2048\n"
"v_exp_f32 v234, v198\n"
"v_cvt_pk_bf16_f32 v68, v68, v69\n"
"v_cvt_pk_bf16_f32 v69, v70, v71\n"
"v_mfma_f32_16x16x32_bf16 v[204:207], a[212:215], a[116:119], v[204:207]\n"
"ds_read_b128 v[44:47], v247 offset:3072\n"
"v_cvt_pk_bf16_f32 v222, v232, v233\n"
"v_exp_f32 v235, v199\n"
"v_mfma_f32_16x16x32_bf16 v[204:207], a[216:219], a[120:123], v[204:207]\n"
"ds_read_b128 v[48:51], v247 offset:4096\n"
"v_cvt_pk_bf16_f32 v223, v234, v235\n"
"v_cvt_pk_bf16_f32 v72, v72, v73\n"
"v_cvt_pk_bf16_f32 v73, v74, v75\n"
"v_mfma_f32_16x16x32_bf16 v[204:207], a[220:223], a[124:127], v[204:207]\n"
"ds_read_b128 v[52:55], v247 offset:5120\n"
"v_cvt_pk_bf16_f32 v76, v76, v77\n"
"v_mfma_f32_16x16x32_bf16 v[228:231], v[236:239], v[216:219], 0\n"
"ds_read_b128 v[56:59], v247 offset:6144\n"
"v_cvt_pk_bf16_f32 v77, v78, v79\n"
"v_mfma_f32_16x16x32_bf16 v[228:231], v[236:239], v[220:223], v[228:231]\n"
"ds_read_b128 v[60:63], v247 offset:7168\n"
"v_cvt_pk_bf16_f32 v80, v80, v81\n"
"v_mfma_f32_16x16x32_bf16 v[208:211], a[224:227], a[112:115], v[180:183]\n"
"v_cvt_pk_bf16_f32 v81, v82, v83\n"
"v_cvt_pk_bf16_f32 v84, v84, v85\n"
"v_mfma_f32_16x16x32_bf16 v[208:211], a[228:231], a[116:119], v[208:211]\n"
"ds_read_b64 v[2:3], v246 offset:0\n"
"v_cvt_pk_bf16_f32 v85, v86, v87\n"
"v_mfma_f32_16x16x32_bf16 v[208:211], a[232:235], a[120:123], v[208:211]\n"
"v_cvt_pk_bf16_f32 v88, v88, v89\n"
"v_cvt_pk_bf16_f32 v89, v90, v91\n"
"v_mfma_f32_16x16x32_bf16 v[208:211], a[236:239], a[124:127], v[208:211]\n"
"v_cmp_nge_f32 vcc, v254, v228\n"
"s_waitcnt lgkmcnt(8)\n"
"v_cvt_pk_bf16_f32 v66, v32, v33\n"
"v_mfma_f32_16x16x32_bf16 v[212:215], a[240:243], a[112:115], v[180:183]\n"
"ds_read_b64 v[6:7], v246 offset:512\n"
"v_cvt_pk_bf16_f32 v67, v34, v35\n"
"v_mfma_f32_16x16x32_bf16 v[212:215], a[244:247], a[116:119], v[212:215]\n"
"v_cvt_pk_bf16_f32 v92, v92, v93\n"
"v_cvt_pk_bf16_f32 v93, v94, v95\n"
"v_mfma_f32_16x16x32_bf16 v[212:215], a[248:251], a[120:123], v[212:215]\n"
"ds_read_b64 v[10:11], v246 offset:1024\n"
"s_waitcnt lgkmcnt(9)\n"
"v_cvt_pk_bf16_f32 v70, v36, v37\n"
"v_cvt_pk_bf16_f32 v71, v38, v39\n"
"v_mfma_f32_16x16x32_bf16 v[212:215], a[252:255], a[124:127], v[212:215]\n"
"s_cbranch_vccnz .Lstub_22\n"
".Lback_22:\n"
"v_mfma_f32_16x16x32_bf16 v[96:99], v[216:219], a[128:131], v[96:99]\n"
"s_add_u32 m0, s58, 71680\n"
"ds_read_b64 v[14:15], v246 offset:1536\n"
"s_waitcnt lgkmcnt(9)\n"
"v_cvt_pk_bf16_f32 v74, v40, v41\n"
"v_cvt_pk_bf16_f32 v75, v42, v43\n"
"v_mfma_f32_16x16x32_bf16 v[96:99], v[220:223], a[132:135], v[96:99]\n"
"buffer_load_dwordx4 v252, s[36:39], s57 offen offset:0  lds\n"
"buffer_load_dwordx4 v253, s[36:39], s57 offen offset:1024  lds\n"
"v_mfma_f32_16x16x32_bf16 v[100:103], v[216:219], a[136:139], v[100:103]\n"
"s_add_u32 s57, s57, 20480\n"
"v_exp_f32 v232, v200\n"
"v_mfma_f32_16x16x32_bf16 v[100:103], v[220:223], a[140:143], v[100:103]\n"
"ds_read_b64 v[18:19], v246 offset:2048\n"
"s_waitcnt lgkmcnt(9)\n"
"v_cvt_pk_bf16_f32 v78, v44, v45\n"
"v_cvt_pk_bf16_f32 v79, v46, v47\n"
"v_mfma_f32_16x16x32_bf16 v[104:107], v[216:219], a[144:147], v[104:107]\n"
"v_exp_f32 v233, v201\n"
"v_mfma_f32_16x16x32_bf16 v[104:107], v[220:223], a[148:151], v[104:107]\n"
"ds_read_b64 v[22:23], v246 offset:2560\n"
"v_exp_f32 v234, v202\n"
"s_waitcnt lgkmcnt(9)\n"
"v_cvt_pk_bf16_f32 v82, v48, v49\n"
"v_cvt_pk_bf16_f32 v83, v50, v51\n"
"v_mfma_f32_16x16x32_bf16 v[108:111], v[216:219], a[152:155], v[108:111]\n"
"v_add_f32 v244, v244, |v228|\n"
"v_cvt_pk_bf16_f32 v224, v232, v233\n"
"v_exp_f32 v235, v203\n"
"v_min_f32 v173, v172, v164\n"
"v_sub_f32 v172, v173, v172\n"
"v_mfma_f32_16x16x32_bf16 v[108:111], v[220:223], a[156:159], v[108:111]\n"
"ds_read_b64 v[26:27], v246 offset:3072\n"
"s_waitcnt lgkmcnt(9)\n"
"v_cvt_pk_bf16_f32 v86, v52, v53\n"
"v_cvt_pk_bf16_f32 v87, v54, v55\n"
"v_sub_f32 v164, v173, v164\n"
"v_exp_f32 v172, v172\n"
"v_mfma_f32_16x16x32_bf16 v[112:115], v[216:219], a[160:163], v[112:115]\n"
"v_cvt_pk_bf16_f32 v225, v234, v235\n"
"s_waitcnt lgkmcnt(8)\n"
"v_cvt_pk_bf16_f32 v90, v56, v57\n"
"v_exp_f32 v232, v204\n"
"v_exp_f32 v164, v164\n"
"v_mul_f32 v243, v243, v172\n"
"v_mfma_f32_16x16x32_bf16 v[112:115], v[220:223], a[164:167], v[112:115]\n"
"ds_read_b64 v[30:31], v246 offset:3584\n"
"v_exp_f32 v233, v205\n"
"v_cvt_pk_bf16_f32 v91, v58, v59\n"
"v_fmac_f32 v243, v165, v164\n"
"v_rcp_f32 v243, v243\n"
"v_mfma_f32_16x16x32_bf16 v[116:119], v[216:219], a[168:171], v[116:119]\n"
"v_exp_f32 v234, v206\n"
"v_mul_f32 v172, v172, v243\n"
"v_mul_f32 v164, v164, v243\n"
"v_mfma_f32_16x16x32_bf16 v[116:119], v[220:223], a[172:175], v[116:119]\n"
"v_cvt_pk_bf16_f32 v226, v232, v233\n"
"s_waitcnt lgkmcnt(8)\n"
"v_cvt_pk_bf16_f32 v94, v60, v61\n"
"v_exp_f32 v235, v207\n"
"v_cvt_pk_bf16_f32 v95, v62, v63\n"
"v_cvt_pk_bf16_f32 v172, v172, v172\n"
"v_cvt_pk_bf16_f32 v164, v164, v164\n"
"v_mfma_f32_16x16x32_bf16 v[120:123], v[216:219], a[176:179], v[120:123]\n"
"v_and_b32 v160, v172, v249\n"
"v_and_b32 v161, v172, v250\n"
"v_mfma_f32_16x16x32_bf16 v[120:123], v[220:223], a[180:183], v[120:123]\n"
"v_cvt_pk_bf16_f32 v227, v234, v235\n"
"v_and_b32 v162, v164, v249\n"
"v_exp_f32 v232, v208\n"
"v_and_b32 v163, v164, v250\n"
"v_mfma_f32_16x16x32_bf16 v[124:127], v[216:219], a[184:187], v[124:127]\n"
"v_exp_f32 v233, v209\n"
"v_mfma_f32_16x16x32_bf16 v[124:127], v[220:223], a[188:191], v[124:127]\n"
"v_exp_f32 v234, v210\n"
"v_mfma_f32_16x16x32_bf16 v[64:67], v[64:67], v[160:163], 0\n"
"v_cvt_pk_bf16_f32 v228, v232, v233\n"
"v_min_f32 v177, v176, v166\n"
"v_exp_f32 v235, v211\n"
"v_sub_f32 v176, v177, v176\n"
"v_mfma_f32_16x16x32_bf16 v[68:71], v[68:71], v[160:163], 0\n"
"v_sub_f32 v166, v177, v166\n"
"v_cvt_pk_bf16_f32 v229, v234, v235\n"
"v_exp_f32 v176, v176\n"
"v_mfma_f32_16x16x32_bf16 v[72:75], v[72:75], v[160:163], 0\n"
"ds_read_b64 v[34:35], v246 offset:4096\n"
"v_exp_f32 v232, v212\n"
"v_exp_f32 v166, v166\n"
"v_mul_f32 v244, v244, v176\n"
"v_mfma_f32_16x16x32_bf16 v[76:79], v[76:79], v[160:163], 0\n"
"ds_read_b64 v[38:39], v246 offset:4608\n"
"v_exp_f32 v233, v213\n"
"v_fmac_f32 v244, v167, v166\n"
"v_rcp_f32 v244, v244\n"
"v_mfma_f32_16x16x32_bf16 v[80:83], v[80:83], v[160:163], 0\n"
"ds_read_b64 v[42:43], v246 offset:5120\n"
"v_exp_f32 v234, v214\n"
"v_mul_f32 v176, v176, v244\n"
"v_mul_f32 v166, v166, v244\n"
"v_mfma_f32_16x16x32_bf16 v[84:87], v[84:87], v[160:163], 0\n"
"ds_read_b64 v[46:47], v246 offset:5632\n"
"v_cvt_pk_bf16_f32 v230, v232, v233\n"
"v_exp_f32 v235, v215\n"
"v_cvt_pk_bf16_f32 v176, v176, v176\n"
"v_cvt_pk_bf16_f32 v166, v166, v166\n"
"v_mfma_f32_16x16x32_bf16 v[88:91], v[88:91], v[160:163], 0\n"
"ds_read_b64 v[50:51], v246 offset:6144\n"
"v_cvt_pk_bf16_f32 v231, v234, v235\n"
"v_and_b32 v184, v176, v249\n"
"v_and_b32 v185, v176, v250\n"
"v_mfma_f32_16x16x32_bf16 v[92:95], v[92:95], v[160:163], 0\n"
"ds_read_b64 v[54:55], v246 offset:6656\n"
"v_and_b32 v186, v166, v249\n"
"v_and_b32 v187, v166, v250\n"
"v_mfma_f32_16x16x32_bf16 v[220:223], v[236:239], v[224:227], 0\n"
"ds_read_b64 v[58:59], v246 offset:7168\n"
"v_cvt_pk_bf16_f32 v0, v96, v97\n"
"v_mfma_f32_16x16x32_bf16 v[220:223], v[236:239], v[228:231], v[220:223]\n"
"ds_read_b64 v[62:63], v246 offset:7680\n"
"v_cvt_pk_bf16_f32 v1, v98, v99\n"
"s_waitcnt lgkmcnt(15)\n"
"s_nop 0\n"
"v_mfma_f32_16x16x32_bf16 v[0:3], v[0:3], v[184:187], 0\n"
"v_cvt_pk_bf16_f32 v4, v100, v101\n"
"v_cvt_pk_bf16_f32 v5, v102, v103\n"
"s_waitcnt lgkmcnt(14)\n"
"s_nop 0\n"
"v_mfma_f32_16x16x32_bf16 v[4:7], v[4:7], v[184:187], 0\n"
"v_cvt_pk_bf16_f32 v8, v104, v105\n"
"v_cvt_pk_bf16_f32 v9, v106, v107\n"
"s_waitcnt lgkmcnt(13)\n"
"s_nop 0\n"
"v_mfma_f32_16x16x32_bf16 v[8:11], v[8:11], v[184:187], 0\n"
"v_cvt_pk_bf16_f32 v12, v108, v109\n"
"v_cvt_pk_bf16_f32 v13, v110, v111\n"
"v_cvt_pk_bf16_f32 v64, v64, v65\n"
"v_cvt_pk_bf16_f32 v65, v66, v67\n"
"v_cvt_pk_bf16_f32 v66, v68, v69\n"
"v_cvt_pk_bf16_f32 v67, v70, v71\n"
"s_waitcnt lgkmcnt(12)\n"
"v_mfma_f32_16x16x32_bf16 v[12:15], v[12:15], v[184:187], 0\n"
"v_cvt_pk_bf16_f32 v16, v112, v113\n"
"v_cvt_pk_bf16_f32 v17, v114, v115\n"
"buffer_store_dwordx4 v[64:67], v248, s[28:31], 0 offen offset:0 sc1\n"
"s_waitcnt lgkmcnt(11)\n"
"v_mfma_f32_16x16x32_bf16 v[16:19], v[16:19], v[184:187], 0\n"
"v_cvt_pk_bf16_f32 v20, v116, v117\n"
"v_cvt_pk_bf16_f32 v21, v118, v119\n"
"v_cvt_pk_bf16_f32 v72, v72, v73\n"
"v_cvt_pk_bf16_f32 v73, v74, v75\n"
"v_cvt_pk_bf16_f32 v74, v76, v77\n"
"v_cvt_pk_bf16_f32 v75, v78, v79\n"
"s_waitcnt lgkmcnt(10)\n"
"v_mfma_f32_16x16x32_bf16 v[20:23], v[20:23], v[184:187], 0\n"
"v_cmp_nge_f32 vcc, v254, v220\n"
"v_cvt_pk_bf16_f32 v24, v120, v121\n"
"v_cvt_pk_bf16_f32 v25, v122, v123\n"
"buffer_store_dwordx4 v[72:75], v248, s[28:31], 0 offen offset:1024 sc1\n"
"s_waitcnt lgkmcnt(9)\n"
"v_mfma_f32_16x16x32_bf16 v[24:27], v[24:27], v[184:187], 0\n"
"v_cvt_pk_bf16_f32 v28, v124, v125\n"
"v_cvt_pk_bf16_f32 v29, v126, v127\n"
"v_cvt_pk_bf16_f32 v80, v80, v81\n"
"v_cvt_pk_bf16_f32 v81, v82, v83\n"
"v_cvt_pk_bf16_f32 v82, v84, v85\n"
"v_cvt_pk_bf16_f32 v83, v86, v87\n"
"buffer_store_dwordx4 v[80:83], v248, s[28:31], 0 offen offset:2048 sc1\n"
"s_waitcnt lgkmcnt(8)\n"
"v_mfma_f32_16x16x32_bf16 v[28:31], v[28:31], v[184:187], 0\n"
"s_cbranch_vccnz .Lstub_23\n"
".Lback_23:\n"
"v_mfma_f32_16x16x32_bf16 v[128:131], v[224:227], a[128:131], v[128:131]\n"
"v_cvt_pk_bf16_f32 v88, v88, v89\n"
"v_cvt_pk_bf16_f32 v89, v90, v91\n"
"v_cvt_pk_bf16_f32 v90, v92, v93\n"
"v_cvt_pk_bf16_f32 v91, v94, v95\n"
"v_mfma_f32_16x16x32_bf16 v[128:131], v[228:231], a[132:135], v[128:131]\n"
"buffer_store_dwordx4 v[88:91], v248, s[28:31], 0 offen offset:3072 sc1\n"
"v_mfma_f32_16x16x32_bf16 v[132:135], v[224:227], a[136:139], v[132:135]\n"
"v_add_f32 v245, v245, |v220|\n"
"v_cvt_pk_bf16_f32 v0, v0, v1\n"
"v_mfma_f32_16x16x32_bf16 v[132:135], v[228:231], a[140:143], v[132:135]\n"
"v_cvt_pk_bf16_f32 v1, v2, v3\n"
"v_cvt_pk_bf16_f32 v2, v4, v5\n"
"v_mfma_f32_16x16x32_bf16 v[136:139], v[224:227], a[144:147], v[136:139]\n"
"v_cvt_pk_bf16_f32 v3, v6, v7\n"
"v_cvt_pk_bf16_f32 v8, v8, v9\n"
"v_mfma_f32_16x16x32_bf16 v[136:139], v[228:231], a[148:151], v[136:139]\n"
"buffer_store_dwordx4 v[0:3], v248, s[28:31], s44 offen offset:0 sc1\n"
"v_mfma_f32_16x16x32_bf16 v[140:143], v[224:227], a[152:155], v[140:143]\n"
"v_cvt_pk_bf16_f32 v9, v10, v11\n"
"v_cvt_pk_bf16_f32 v10, v12, v13\n"
"v_mfma_f32_16x16x32_bf16 v[140:143], v[228:231], a[156:159], v[140:143]\n"
"v_cvt_pk_bf16_f32 v11, v14, v15\n"
"v_min_f32 v181, v180, v168\n"
"v_mfma_f32_16x16x32_bf16 v[144:147], v[224:227], a[160:163], v[144:147]\n"
"v_sub_f32 v180, v181, v180\n"
"v_sub_f32 v168, v181, v168\n"
"v_mfma_f32_16x16x32_bf16 v[144:147], v[228:231], a[164:167], v[144:147]\n"
"v_exp_f32 v180, v180\n"
"buffer_store_dwordx4 v[8:11], v248, s[28:31], s44 offen offset:1024 sc1\n"
"v_mfma_f32_16x16x32_bf16 v[148:151], v[224:227], a[168:171], v[148:151]\n"
"v_cvt_pk_bf16_f32 v16, v16, v17\n"
"v_cvt_pk_bf16_f32 v17, v18, v19\n"
"v_cvt_pk_bf16_f32 v18, v20, v21\n"
"v_cvt_pk_bf16_f32 v19, v22, v23\n"
"v_exp_f32 v168, v168\n"
"v_mul_f32 v245, v245, v180\n"
"v_mfma_f32_16x16x32_bf16 v[148:151], v[228:231], a[172:175], v[148:151]\n"
"v_fmac_f32 v245, v169, v168\n"
"v_rcp_f32 v245, v245\n"
"buffer_store_dwordx4 v[16:19], v248, s[28:31], s44 offen offset:2048 sc1\n"
"v_mfma_f32_16x16x32_bf16 v[152:155], v[224:227], a[176:179], v[152:155]\n"
"v_cvt_pk_bf16_f32 v24, v24, v25\n"
"v_cvt_pk_bf16_f32 v25, v26, v27\n"
"v_cvt_pk_bf16_f32 v26, v28, v29\n"
"v_cvt_pk_bf16_f32 v27, v30, v31\n"
"v_mul_f32 v180, v180, v245\n"
"v_mul_f32 v168, v168, v245\n"
"v_mfma_f32_16x16x32_bf16 v[152:155], v[228:231], a[180:183], v[152:155]\n"
"v_cvt_pk_bf16_f32 v180, v180, v180\n"
"v_cvt_pk_bf16_f32 v168, v168, v168\n"
"buffer_store_dwordx4 v[24:27], v248, s[28:31], s44 offen offset:3072 sc1\n"
"v_mfma_f32_16x16x32_bf16 v[156:159], v[224:227], a[184:187], v[156:159]\n"
"v_and_b32 v188, v180, v249\n"
"v_and_b32 v189, v180, v250\n"
"v_mfma_f32_16x16x32_bf16 v[156:159], v[228:231], a[188:191], v[156:159]\n"
"v_cvt_pk_bf16_f32 v32, v128, v129\n"
"v_cvt_pk_bf16_f32 v33, v130, v131\n"
"v_and_b32 v190, v168, v249\n"
"v_and_b32 v191, v168, v250\n"
"s_waitcnt lgkmcnt(7)\n"
"s_nop 0\n"
"v_mfma_f32_16x16x32_bf16 v[32:35], v[32:35], v[188:191], 0\n"
"v_cvt_pk_bf16_f32 v36, v132, v133\n"
"v_cvt_pk_bf16_f32 v37, v134, v135\n"
"s_waitcnt lgkmcnt(6)\n"
"s_nop 0\n"
"v_mfma_f32_16x16x32_bf16 v[36:39], v[36:39], v[188:191], 0\n"
"v_cvt_pk_bf16_f32 v40, v136, v137\n"
"v_cvt_pk_bf16_f32 v41, v138, v139\n"
"s_waitcnt lgkmcnt(5)\n"
"s_nop 0\n"
"v_mfma_f32_16x16x32_bf16 v[40:43], v[40:43], v[188:191], 0\n"
"v_cvt_pk_bf16_f32 v44, v140, v141\n"
"v_cvt_pk_bf16_f32 v45, v142, v143\n"
"s_waitcnt lgkmcnt(4)\n"
"s_nop 0\n"
"v_mfma_f32_16x16x32_bf16 v[44:47], v[44:47], v[188:191], 0\n"
"v_cvt_pk_bf16_f32 v48, v144, v145\n"
"v_cvt_pk_bf16_f32 v49, v146, v147\n"
"s_waitcnt lgkmcnt(3)\n"
"s_nop 0\n"
"v_mfma_f32_16x16x32_bf16 v[48:51], v[48:51], v[188:191], 0\n"
"v_cvt_pk_bf16_f32 v52, v148, v149\n"
"v_cvt_pk_bf16_f32 v53, v150, v151\n"
"v_cvt_pk_bf16_f32 v32, v32, v33\n"
"v_cvt_pk_bf16_f32 v33, v34, v35\n"
"v_cvt_pk_bf16_f32 v34, v36, v37\n"
"v_cvt_pk_bf16_f32 v35, v38, v39\n"
"buffer_store_dwordx4 v[32:35], v248, s[28:31], s46 offen offset:0 sc1\n"
"s_waitcnt lgkmcnt(2)\n"
"v_mfma_f32_16x16x32_bf16 v[52:55], v[52:55], v[188:191], 0\n"
"v_cvt_pk_bf16_f32 v56, v152, v153\n"
"v_cvt_pk_bf16_f32 v57, v154, v155\n"
"s_waitcnt lgkmcnt(1)\n"
"s_nop 0\n"
"v_mfma_f32_16x16x32_bf16 v[56:59], v[56:59], v[188:191], 0\n"
"v_cvt_pk_bf16_f32 v60, v156, v157\n"
"v_cvt_pk_bf16_f32 v61, v158, v159\n"
"v_cvt_pk_bf16_f32 v40, v40, v41\n"
"v_cvt_pk_bf16_f32 v41, v42, v43\n"
"v_cvt_pk_bf16_f32 v42, v44, v45\n"
"v_cvt_pk_bf16_f32 v43, v46, v47\n"
"buffer_store_dwordx4 v[40:43], v248, s[28:31], s46 offen offset:1024 sc1\n"
"s_waitcnt lgkmcnt(0)\n"
"v_mfma_f32_16x16x32_bf16 v[60:63], v[60:63], v[188:191], 0\n"
"v_cvt_pk_bf16_f32 v48, v48, v49\n"
"v_cvt_pk_bf16_f32 v49, v50, v51\n"
"v_cvt_pk_bf16_f32 v50, v52, v53\n"
"v_cvt_pk_bf16_f32 v51, v54, v55\n"
"buffer_store_dwordx4 v[48:51], v248, s[28:31], s46 offen offset:2048 sc1\n"
"v_cvt_pk_bf16_f32 v56, v56, v57\n"
"v_cvt_pk_bf16_f32 v57, v58, v59\n"
"s_nop 0\n"
"v_cvt_pk_bf16_f32 v58, v60, v61\n"
"v_cvt_pk_bf16_f32 v59, v62, v63\n"
"buffer_store_dwordx4 v[56:59], v248, s[28:31], s46 offen offset:3072 sc1\n"
"s_add_u32 m0, s58, 98304\n"
"s_nop 0\n"
"buffer_load_dwordx4 v252, s[36:39], s57 offen offset:0  lds\n"
"buffer_load_dwordx4 v253, s[36:39], s57 offen offset:1024  lds\n"
"s_add_u32 s57, s57, 20480\n"
"s_add_u32 m0, s58, 100352\n"
"s_nop 0\n"
"buffer_load_dwordx4 v252, s[36:39], s57 offen offset:0  lds\n"
"buffer_load_dwordx4 v253, s[36:39], s57 offen offset:1024  lds\n"
"s_add_u32 s57, s57, 20480\n"
"v_bfe_u32 v240, v248, 6, 4\n"
"v_bfe_u32 v241, v248, 4, 2\n"
"v_bfe_u32 v242, v248, 7, 2\n"
"v_lshl_add_u32 v240, v240, 7, s58\n"
"v_lshl_add_u32 v240, v241, 3, v240\n"
"v_lshl_add_u32 v224, v242, 5, v240\n"
"v_xor_b32 v225, 32, v224\n"
"v_xor_b32 v226, 64, v224\n"
"v_xor_b32 v227, 96, v224\n"
"s_lshr_b32 s45, %2, 3\n"
"s_waitcnt vmcnt(32)\n"
"ds_read_b64_tr_b16 a[0:1], v224 offset:0\n"
"ds_read_b64_tr_b16 a[2:3], v224 offset:2048\n"
"ds_read_b64_tr_b16 a[4:5], v225 offset:0\n"
"ds_read_b64_tr_b16 a[6:7], v225 offset:2048\n"
"ds_read_b64_tr_b16 a[8:9], v226 offset:0\n"
"ds_read_b64_tr_b16 a[10:11], v226 offset:2048\n"
"ds_read_b64_tr_b16 a[12:13], v227 offset:0\n"
"ds_read_b64_tr_b16 a[14:15], v227 offset:2048\n"
"ds_read_b64_tr_b16 a[16:17], v224 offset:4096\n"
"ds_read_b64_tr_b16 a[18:19], v224 offset:6144\n"
"ds_read_b64_tr_b16 a[20:21], v225 offset:4096\n"
"ds_read_b64_tr_b16 a[22:23], v225 offset:6144\n"
"ds_read_b64_tr_b16 a[24:25], v226 offset:4096\n"
"ds_read_b64_tr_b16 a[26:27], v226 offset:6144\n"
"ds_read_b64_tr_b16 a[28:29], v227 offset:4096\n"
"ds_read_b64_tr_b16 a[30:31], v227 offset:6144\n"
"v_add_u32 v232, 131072, v224\n"
"v_add_u32 v233, 131072, v225\n"
"v_add_u32 v234, 131072, v226\n"
"v_add_u32 v235, 131072, v227\n"
"v_add_u32 v228, 65536, v224\n"
"v_add_u32 v229, 65536, v225\n"
"v_add_u32 v230, 65536, v226\n"
"v_add_u32 v231, 65536, v227\n"
"s_waitcnt vmcnt(24)\n"
"ds_read_b64_tr_b16 a[32:33], v232 offset:0\n"
"ds_read_b64_tr_b16 a[34:35], v232 offset:2048\n"
"ds_read_b64_tr_b16 a[36:37], v233 offset:0\n"
"ds_read_b64_tr_b16 a[38:39], v233 offset:2048\n"
"ds_read_b64_tr_b16 a[40:41], v234 offset:0\n"
"ds_read_b64_tr_b16 a[42:43], v234 offset:2048\n"
"ds_read_b64_tr_b16 a[44:45], v235 offset:0\n"
"ds_read_b64_tr_b16 a[46:47], v235 offset:2048\n"
"ds_read_b64_tr_b16 a[48:49], v232 offset:4096\n"
"ds_read_b64_tr_b16 a[50:51], v232 offset:6144\n"
"ds_read_b64_tr_b16 a[52:53], v233 offset:4096\n"
"ds_read_b64_tr_b16 a[54:55], v233 offset:6144\n"
"ds_read_b64_tr_b16 a[56:57], v234 offset:4096\n"
"ds_read_b64_tr_b16 a[58:59], v234 offset:6144\n"
"ds_read_b64_tr_b16 a[60:61], v235 offset:4096\n"
"ds_read_b64_tr_b16 a[62:63], v235 offset:6144\n"
"s_waitcnt vmcnt(16)\n"
"ds_read_b64_tr_b16 a[64:65], v228 offset:0\n"
"ds_read_b64_tr_b16 a[66:67], v228 offset:2048\n"
"ds_read_b64_tr_b16 a[68:69], v229 offset:0\n"
"ds_read_b64_tr_b16 a[70:71], v229 offset:2048\n"
"ds_read_b64_tr_b16 a[72:73], v230 offset:0\n"
"ds_read_b64_tr_b16 a[74:75], v230 offset:2048\n"
"ds_read_b64_tr_b16 a[76:77], v231 offset:0\n"
"ds_read_b64_tr_b16 a[78:79], v231 offset:2048\n"
"ds_read_b64_tr_b16 a[80:81], v228 offset:4096\n"
"ds_read_b64_tr_b16 a[82:83], v228 offset:6144\n"
"ds_read_b64_tr_b16 a[84:85], v229 offset:4096\n"
"ds_read_b64_tr_b16 a[86:87], v229 offset:6144\n"
"ds_read_b64_tr_b16 a[88:89], v230 offset:4096\n"
"ds_read_b64_tr_b16 a[90:91], v230 offset:6144\n"
"ds_read_b64_tr_b16 a[92:93], v231 offset:4096\n"
"ds_read_b64_tr_b16 a[94:95], v231 offset:6144\n"
"s_add_u32 s62, s72, 33554432\n"
"s_addc_u32 s63, s73, 0\n"
"s_lshl_b32 s52, s41, 7\n"
"v_mov_b32 v240, s52\n"
"v_mov_b32 v242, s74\n"
"v_mov_b32 v243, s75\n"
"s_and_b32 s61, %2, 3\n"
"s_lshl2_add_u32 s47, s47, s61\n"
"s_lshr_b32 s54, s40, 1\n"
"s_lshl1_add_u32 s55, s47, s54\n"
"s_mul_i32 s55, s55, 20480\n"
"s_add_u32 s55, s55, s60\n"
"s_add_u32 s24, s70, s55\n"
"s_addc_u32 s25, s71, 0\n"
"s_sub_u32 s26, 1966080, s55\n"
"s_add_u32 s20, s72, 16777216\n"
"s_addc_u32 s21, s73, 0\n"
"s_mov_b32 s22, 12615680\n"
"s_lshl_b32 s54, s54, 12\n"
"s_xor_b32 s52, s50, 0\n"
"s_mul_i32 s52, s52, 49280\n"
"s_xor_b32 s53, s54, 4096\n"
"s_add_u32 s53, s53, s52\n"
"v_add_u32 v236, s53, v248\n"
"s_xor_b32 s53, s54, 0\n"
"s_add_u32 s53, s53, s52\n"
"v_add_u32 v237, s53, v248\n"
"s_xor_b32 s52, s50, 1\n"
"s_mul_i32 s52, s52, 49280\n"
"s_xor_b32 s53, s54, 4096\n"
"s_add_u32 s53, s53, s52\n"
"v_add_u32 v238, s53, v248\n"
"s_xor_b32 s53, s54, 0\n"
"s_add_u32 s53, s53, s52\n"
"v_add_u32 v239, s53, v248\n"
"s_xor_b32 s52, s47, 3\n"
"s_mul_i32 s53, s52, 43\n"
"s_lshr_b32 s53, s53, 8\n"
"s_mul_i32 s53, s53, 1527808\n"
"s_lshl_b32 s52, s52, 13\n"
"s_add_u32 s43, s52, s53\n"
"s_xor_b32 s52, s47, 2\n"
"s_mul_i32 s53, s52, 43\n"
"s_lshr_b32 s53, s53, 8\n"
"s_mul_i32 s53, s53, 1527808\n"
"s_lshl_b32 s52, s52, 13\n"
"s_add_u32 s48, s52, s53\n"
"s_xor_b32 s52, s47, 1\n"
"s_mul_i32 s53, s52, 43\n"
"s_lshr_b32 s53, s53, 8\n"
"s_mul_i32 s53, s53, 1527808\n"
"s_lshl_b32 s52, s52, 13\n"
"s_add_u32 s49, s52, s53\n"
"s_xor_b32 s52, s47, 0\n"
"s_mul_i32 s53, s52, 43\n"
"s_lshr_b32 s53, s53, 8\n"
"s_mul_i32 s53, s53, 1527808\n"
"s_lshl_b32 s52, s52, 13\n"
"s_add_u32 s51, s52, s53\n"
"s_andn2_b32 s52, s47, 3\n"
"s_mul_i32 s52, s52, 43\n"
"s_add_u32 s53, s52, 129\n"
"s_lshr_b32 s52, s52, 8\n"
"s_lshr_b32 s53, s53, 8\n"
"s_sub_u32 s53, s53, s52\n"
"s_lshl_b32 s53, s53, 7\n"
"s_lshl_b32 s52, s52, 5\n"
"s_add_u32 s52, s52, s50\n"
"s_lshl_b32 s52, s52, 7\n"
"v_and_b32 v249, 16, v248\n"
"v_lshlrev_b32 v249, 3, v249\n"
"v_xor_b32 v249, s52, v249\n"
"v_and_b32 v241, 32, v248\n"
"v_mad_u32_u24 v249, v241, s53, v249\n"
"v_bfe_u32 v241, v248, 4, 4\n"
"v_mul_u32_u24 v241, 1280, v241\n"
"v_lshrrev_b32 v244, 9, v248\n"
"v_lshl_add_u32 v241, v244, 4, v241\n"
"v_bfe_u32 v244, v248, 8, 1\n"
"v_lshl_add_u32 v241, v244, 5, v241\n"
"v_lshrrev_b32 v244, 1, v248\n"
"v_add_u32 v244, s58, v244\n"
"v_xor_b32 v245, 8192, v244\n"
"v_xor_b32 v246, 16384, v244\n"
"v_xor_b32 v247, 24576, v244\n"
"s_andn2_b32 s52, %2, 3\n"
"s_lshl2_add_u32 s52, s52, s40\n"
"s_lshl_b32 s52, s52, 14\n"
"s_add_u32 s52, s52, 41943040\n"
"s_add_u32 s28, s72, s52\n"
"s_addc_u32 s29, s73, 0\n"
"s_mov_b32 s30, 262144\n"
"s_lshl_b32 s46, s61, 16\n"
"s_xor_b32 s44, s46, 196608\n"
"s_add_u32 m0, s58, 102400\n"
"s_nop 0\n"
"buffer_load_dwordx4 v252, s[36:39], s57 offen offset:0  lds\n"
"buffer_load_dwordx4 v253, s[36:39], s57 offen offset:1024  lds\n"
"s_add_u32 s57, s57, 20480\n"
"s_add_u32 m0, s58, 104448\n"
"s_nop 0\n"
"buffer_load_dwordx4 v252, s[36:39], s57 offen offset:0  lds\n"
"buffer_load_dwordx4 v253, s[36:39], s57 offen offset:1024  lds\n"
"s_add_u32 s57, s57, 20480\n"
"s_waitcnt vmcnt(8)\n"
"s_barrier\n"
"s_cmp_lg_u32 s40, 0\n"
"s_cbranch_scc1 .Lnoflag\n"
"s_mov_b64 exec, 1\n"
"global_store_dwordx2 v240, v[242:243], s[62:63] sc1\n"
"s_mov_b64 exec, -1\n"
".Lnoflag:\n"
"s_cmp_ge_u32 s45, 30\n"
"s_cbranch_scc1 .Lidle\n"
"s_add_u32 m0, s58, 32768\n"
"s_nop 0\n"
"buffer_load_dwordx4 v252, s[36:39], s59 offen offset:0  lds\n"
"buffer_load_dwordx4 v253, s[36:39], s59 offen offset:1024  lds\n"
"s_add_u32 s59, s59, 20480\n"
"s_add_u32 m0, s58, 34816\n"
"s_nop 0\n"
"buffer_load_dwordx4 v252, s[36:39], s59 offen offset:0  lds\n"
"buffer_load_dwordx4 v253, s[36:39], s59 offen offset:1024  lds\n"
"s_add_u32 s59, s59, 20480\n"
"s_add_u32 m0, s58, 36864\n"
"s_nop 0\n"
"buffer_load_dwordx4 v252, s[36:39], s59 offen offset:0  lds\n"
"buffer_load_dwordx4 v253, s[36:39], s59 offen offset:1024  lds\n"
"s_add_u32 s59, s59, 20480\n"
"s_add_u32 m0, s58, 38912\n"
"s_nop 0\n"
"buffer_load_dwordx4 v252, s[36:39], s59 offen offset:0  lds\n"
"buffer_load_dwordx4 v253, s[36:39], s59 offen offset:1024  lds\n"
"s_add_u32 s59, s59, 20480\n"
"s_add_u32 m0, s58, 0\n"
"s_waitcnt lgkmcnt(15)\n"
"buffer_load_dwordx4 v252, s[36:39], s59 offen offset:0  lds\n"
"buffer_load_dwordx4 v253, s[36:39], s59 offen offset:1024  lds\n"
"s_add_u32 s59, s59, 20480\n"
"s_add_u32 m0, s58, 2048\n"
"s_nop 0\n"
"buffer_load_dwordx4 v252, s[36:39], s59 offen offset:0  lds\n"
"buffer_load_dwordx4 v253, s[36:39], s59 offen offset:1024  lds\n"
"s_add_u32 s59, s59, 20480\n"
"s_add_u32 m0, s58, 4096\n"
"s_nop 0\n"
"buffer_load_dwordx4 v252, s[36:39], s59 offen offset:0  lds\n"
"buffer_load_dwordx4 v253, s[36:39], s59 offen offset:1024  lds\n"
"s_add_u32 s59, s59, 20480\n"
"s_add_u32 m0, s58, 6144\n"
"s_nop 0\n"
"buffer_load_dwordx4 v252, s[36:39], s59 offen offset:0  lds\n"
"buffer_load_dwordx4 v253, s[36:39], s59 offen offset:1024  lds\n"
"s_add_u32 s59, s59, 20480\n"
"s_xor_b32 s56, s50, 1\n"
"s_mul_i32 s56, s56, 163840\n"
"s_add_u32 s56, s56, s60\n"
"s_add_u32 m0, s58, 131072\n"
"s_nop 0\n"
"buffer_load_dwordx4 v252, s[36:39], s56 offen offset:0  lds\n"
"buffer_load_dwordx4 v253, s[36:39], s56 offen offset:1024  lds\n"
"s_add_u32 s56, s56, 20480\n"
"s_add_u32 m0, s58, 133120\n"
"s_nop 0\n"
"buffer_load_dwordx4 v252, s[36:39], s56 offen offset:0  lds\n"
"buffer_load_dwordx4 v253, s[36:39], s56 offen offset:1024  lds\n"
"s_add_u32 s56, s56, 20480\n"
"s_add_u32 m0, s58, 135168\n"
"s_nop 0\n"
"buffer_load_dwordx4 v252, s[36:39], s56 offen offset:0  lds\n"
"buffer_load_dwordx4 v253, s[36:39], s56 offen offset:1024  lds\n"
"s_add_u32 s56, s56, 20480\n"
"s_add_u32 m0, s58, 137216\n"
"s_nop 0\n"
"buffer_load_dwordx4 v252, s[36:39], s56 offen offset:0  lds\n"
"buffer_load_dwordx4 v253, s[36:39], s56 offen offset:1024  lds\n"
"s_add_u32 s56, s56, 20480\n"
"s_add_u32 m0, s58, 65536\n"
"s_waitcnt lgkmcnt(0)\n"
"buffer_load_dwordx4 v252, s[36:39], s56 offen offset:0  lds\n"
"buffer_load_dwordx4 v253, s[36:39], s56 offen offset:1024  lds\n"
"s_add_u32 s56, s56, 20480\n"
"global_load_dwordx2 v[250:251], v249, s[62:63] sc1\n"
"s_add_u32 m0, s58, 67584\n"
"s_nop 0\n"
"buffer_load_dwordx4 v252, s[36:39], s56 offen offset:0  lds\n"
"buffer_load_dwordx4 v253, s[36:39], s56 offen offset:1024  lds\n"
"s_add_u32 s56, s56, 20480\n"
"s_add_u32 m0, s58, 69632\n"
"s_nop 0\n"
"buffer_load_dwordx4 v252, s[36:39], s56 offen offset:0  lds\n"
"buffer_load_dwordx4 v253, s[36:39], s56 offen offset:1024  lds\n"
"s_add_u32 s56, s56, 20480\n"
"s_add_u32 m0, s58, 71680\n"
"s_nop 0\n"
"buffer_load_dwordx4 v252, s[36:39], s56 offen offset:0  lds\n"
"buffer_load_dwordx4 v253, s[36:39], s56 offen offset:1024  lds\n"
"s_add_u32 s56, s56, 20480\n"
"s_waitcnt vmcnt(33)\n"
"ds_read_b64_tr_b16 a[96:97], v228 offset:32768\n"
"ds_read_b64_tr_b16 a[98:99], v228 offset:34816\n"
"ds_read_b64_tr_b16 a[100:101], v229 offset:32768\n"
"ds_read_b64_tr_b16 a[102:103], v229 offset:34816\n"
"ds_read_b64_tr_b16 a[104:105], v230 offset:32768\n"
"ds_read_b64_tr_b16 a[106:107], v230 offset:34816\n"
"ds_read_b64_tr_b16 a[108:109], v231 offset:32768\n"
"ds_read_b64_tr_b16 a[110:111], v231 offset:34816\n"
"ds_read_b64_tr_b16 a[112:113], v228 offset:36864\n"
"ds_read_b64_tr_b16 a[114:115], v228 offset:38912\n"
"ds_read_b64_tr_b16 a[116:117], v229 offset:36864\n"
"ds_read_b64_tr_b16 a[118:119], v229 offset:38912\n"
"ds_read_b64_tr_b16 a[120:121], v230 offset:36864\n"
"ds_read_b64_tr_b16 a[122:123], v230 offset:38912\n"
"ds_read_b64_tr_b16 a[124:125], v231 offset:36864\n"
"ds_read_b64_tr_b16 a[126:127], v231 offset:38912\n"
"s_waitcnt vmcnt(25)\n"
"ds_read_b64_tr_b16 a[128:129], v224 offset:32768\n"
"ds_read_b64_tr_b16 a[130:131], v224 offset:34816\n"
"ds_read_b64_tr_b16 a[132:133], v225 offset:32768\n"
"ds_read_b64_tr_b16 a[134:135], v225 offset:34816\n"
"ds_read_b64_tr_b16 a[136:137], v226 offset:32768\n"
"ds_read_b64_tr_b16 a[138:139], v226 offset:34816\n"
"ds_read_b64_tr_b16 a[140:141], v227 offset:32768\n"
"ds_read_b64_tr_b16 a[142:143], v227 offset:34816\n"
"ds_read_b64_tr_b16 a[144:145], v224 offset:36864\n"
"ds_read_b64_tr_b16 a[146:147], v224 offset:38912\n"
"ds_read_b64_tr_b16 a[148:149], v225 offset:36864\n"
"ds_read_b64_tr_b16 a[150:151], v225 offset:38912\n"
"ds_read_b64_tr_b16 a[152:153], v226 offset:36864\n"
"ds_read_b64_tr_b16 a[154:155], v226 offset:38912\n"
"ds_read_b64_tr_b16 a[156:157], v227 offset:36864\n"
"ds_read_b64_tr_b16 a[158:159], v227 offset:38912\n"
"s_waitcnt vmcnt(6)\n"
"v_cmp_ne_u64 vcc, s[74:75], v[250:251]\n"
"s_nop 1\n"
"s_cbranch_vccnz .Lslow\n"
".Lpolledp:\n"
"buffer_load_dwordx4 v[96:99], v236, s[20:23], s43 offen offset:0 sc1\n"
"buffer_load_dwordx4 v[100:103], v236, s[20:23], s43 offen offset:1024 sc1\n"
"buffer_load_dwordx4 v[104:107], v236, s[20:23], s43 offen offset:2048 sc1\n"
"buffer_load_dwordx4 v[108:111], v236, s[20:23], s43 offen offset:3072 sc1\n"
"buffer_load_dwordx4 v[112:115], v237, s[20:23], s43 offen offset:0 sc1\n"
"buffer_load_dwordx4 v[116:119], v237, s[20:23], s43 offen offset:1024 sc1\n"
"buffer_load_dwordx4 v[120:123], v237, s[20:23], s43 offen offset:2048 sc1\n"
"buffer_load_dwordx4 v[124:127], v237, s[20:23], s43 offen offset:3072 sc1\n"
"buffer_load_dwordx4 v[192:195], v238, s[20:23], s43 offen offset:0 sc1\n"
"buffer_load_dwordx4 v[196:199], v238, s[20:23], s43 offen offset:1024 sc1\n"
"buffer_load_dwordx4 v[200:203], v238, s[20:23], s43 offen offset:2048 sc1\n"
"buffer_load_dwordx4 v[204:207], v238, s[20:23], s43 offen offset:3072 sc1\n"
"ds_read_b64_tr_b16 a[160:161], v224 offset:0\n"
"ds_read_b64_tr_b16 a[162:163], v224 offset:2048\n"
"ds_read_b64_tr_b16 a[164:165], v225 offset:0\n"
"ds_read_b64_tr_b16 a[166:167], v225 offset:2048\n"
"ds_read_b64_tr_b16 a[168:169], v226 offset:0\n"
"ds_read_b64_tr_b16 a[170:171], v226 offset:2048\n"
"ds_read_b64_tr_b16 a[172:173], v227 offset:0\n"
"ds_read_b64_tr_b16 a[174:175], v227 offset:2048\n"
"ds_read_b64_tr_b16 a[176:177], v224 offset:4096\n"
"ds_read_b64_tr_b16 a[178:179], v224 offset:6144\n"
"ds_read_b64_tr_b16 a[180:181], v225 offset:4096\n"
"ds_read_b64_tr_b16 a[182:183], v225 offset:6144\n"
"ds_read_b64_tr_b16 a[184:185], v226 offset:4096\n"
"ds_read_b64_tr_b16 a[186:187], v226 offset:6144\n"
"ds_read_b64_tr_b16 a[188:189], v227 offset:4096\n"
"ds_read_b64_tr_b16 a[190:191], v227 offset:6144\n"
"ds_read_b64_tr_b16 a[192:193], v232 offset:0\n"
"ds_read_b64_tr_b16 a[194:195], v232 offset:2048\n"
"ds_read_b64_tr_b16 a[196:197], v233 offset:0\n"
"ds_read_b64_tr_b16 a[198:199], v233 offset:2048\n"
"ds_read_b64_tr_b16 a[200:201], v234 offset:0\n"
"ds_read_b64_tr_b16 a[202:203], v234 offset:2048\n"
"ds_read_b64_tr_b16 a[204:205], v235 offset:0\n"
"ds_read_b64_tr_b16 a[206:207], v235 offset:2048\n"
"ds_read_b64_tr_b16 a[208:209], v232 offset:4096\n"
"ds_read_b64_tr_b16 a[210:211], v232 offset:6144\n"
"ds_read_b64_tr_b16 a[212:213], v233 offset:4096\n"
"ds_read_b64_tr_b16 a[214:215], v233 offset:6144\n"
"ds_read_b64_tr_b16 a[216:217], v234 offset:4096\n"
"ds_read_b64_tr_b16 a[218:219], v234 offset:6144\n"
"ds_read_b64_tr_b16 a[220:221], v235 offset:4096\n"
"ds_read_b64_tr_b16 a[222:223], v235 offset:6144\n"
"s_waitcnt vmcnt(12)\n"
"ds_read_b64_tr_b16 a[224:225], v228 offset:0\n"
"ds_read_b64_tr_b16 a[226:227], v228 offset:2048\n"
"ds_read_b64_tr_b16 a[228:229], v229 offset:0\n"
"ds_read_b64_tr_b16 a[230:231], v229 offset:2048\n"
"ds_read_b64_tr_b16 a[232:233], v230 offset:0\n"
"ds_read_b64_tr_b16 a[234:235], v230 offset:2048\n"
"ds_read_b64_tr_b16 a[236:237], v231 offset:0\n"
"ds_read_b64_tr_b16 a[238:239], v231 offset:2048\n"
"ds_read_b64_tr_b16 a[240:241], v228 offset:4096\n"
"ds_read_b64_tr_b16 a[242:243], v228 offset:6144\n"
"ds_read_b64_tr_b16 a[244:245], v229 offset:4096\n"
"ds_read_b64_tr_b16 a[246:247], v229 offset:6144\n"
"ds_read_b64_tr_b16 a[248:249], v230 offset:4096\n"
"ds_read_b64_tr_b16 a[250:251], v230 offset:6144\n"
"ds_read_b64_tr_b16 a[252:253], v231 offset:4096\n"
"ds_read_b64_tr_b16 a[254:255], v231 offset:6144\n"
"s_waitcnt vmcnt(8)\n"
"v_mfma_f32_16x16x32_bf16 v[176:179], a[0:3], v[96:99], 0\n"
"buffer_load_dwordx4 v[0:3], v239, s[20:23], s43 offen offset:0 sc1\n"
"v_mfma_f32_16x16x32_bf16 v[176:179], a[16:19], v[100:103], v[176:179]\n"
"v_mfma_f32_16x16x32_bf16 v[176:179], a[32:35], v[104:107], v[176:179]\n"
"v_mfma_f32_16x16x32_bf16 v[176:179], a[48:51], v[108:111], v[176:179]\n"
"v_mfma_f32_16x16x32_bf16 v[180:183], a[4:7], v[96:99], 0\n"
"buffer_load_dwordx4 v[4:7], v239, s[20:23], s43 offen offset:1024 sc1\n"
"v_mfma_f32_16x16x32_bf16 v[180:183], a[20:23], v[100:103], v[180:183]\n"
"v_mfma_f32_16x16x32_bf16 v[180:183], a[36:39], v[104:107], v[180:183]\n"
"v_mfma_f32_16x16x32_bf16 v[180:183], a[52:55], v[108:111], v[180:183]\n"
"v_mfma_f32_16x16x32_bf16 v[184:187], a[8:11], v[96:99], 0\n"
"buffer_load_dwordx4 v[8:11], v239, s[20:23], s43 offen offset:2048 sc1\n"
"v_mfma_f32_16x16x32_bf16 v[184:187], a[24:27], v[100:103], v[184:187]\n"
"v_mfma_f32_16x16x32_bf16 v[184:187], a[40:43], v[104:107], v[184:187]\n"
"v_mfma_f32_16x16x32_bf16 v[184:187], a[56:59], v[108:111], v[184:187]\n"
"v_mfma_f32_16x16x32_bf16 v[188:191], a[12:15], v[96:99], 0\n"
"buffer_load_dwordx4 v[12:15], v239, s[20:23], s43 offen offset:3072 sc1\n"
"v_mfma_f32_16x16x32_bf16 v[188:191], a[28:31], v[100:103], v[188:191]\n"
"v_mfma_f32_16x16x32_bf16 v[188:191], a[44:47], v[104:107], v[188:191]\n"
"v_mfma_f32_16x16x32_bf16 v[188:191], a[60:63], v[108:111], v[188:191]\n"
"s_waitcnt vmcnt(8)\n"
"v_mfma_f32_16x16x32_bf16 v[144:147], a[0:3], v[112:115], 0\n"
"buffer_load_dwordx4 v[16:19], v236, s[20:23], s48 offen offset:0 sc1\n"
"v_mfma_f32_16x16x32_bf16 v[144:147], a[16:19], v[116:119], v[144:147]\n"
"v_mfma_f32_16x16x32_bf16 v[144:147], a[32:35], v[120:123], v[144:147]\n"
"v_mfma_f32_16x16x32_bf16 v[144:147], a[48:51], v[124:127], v[144:147]\n"
"v_mfma_f32_16x16x32_bf16 v[148:151], a[4:7], v[112:115], 0\n"
"buffer_load_dwordx4 v[20:23], v236, s[20:23], s48 offen offset:1024 sc1\n"
"v_mfma_f32_16x16x32_bf16 v[148:151], a[20:23], v[116:119], v[148:151]\n"
"v_mfma_f32_16x16x32_bf16 v[148:151], a[36:39], v[120:123], v[148:151]\n"
"v_mfma_f32_16x16x32_bf16 v[148:151], a[52:55], v[124:127], v[148:151]\n"
"v_mfma_f32_16x16x32_bf16 v[152:155], a[8:11], v[112:115], 0\n"
"buffer_load_dwordx4 v[24:27], v236, s[20:23], s48 offen offset:2048 sc1\n"
"v_mfma_f32_16x16x32_bf16 v[152:155], a[24:27], v[116:119], v[152:155]\n"
"v_mfma_f32_16x16x32_bf16 v[152:155], a[40:43], v[120:123], v[152:155]\n"
"v_mfma_f32_16x16x32_bf16 v[152:155], a[56:59], v[124:127], v[152:155]\n"
"v_mfma_f32_16x16x32_bf16 v[156:159], a[12:15], v[112:115], 0\n"
"buffer_load_dwordx4 v[28:31], v236, s[20:23], s48 offen offset:3072 sc1\n"
"v_mfma_f32_16x16x32_bf16 v[156:159], a[28:31], v[116:119], v[156:159]\n"
"v_mfma_f32_16x16x32_bf16 v[156:159], a[44:47], v[120:123], v[156:159]\n"
"v_mfma_f32_16x16x32_bf16 v[156:159], a[60:63], v[124:127], v[156:159]\n"
"v_mfma_f32_16x16x32_bf16 v[160:163], a[64:67], v[96:99], 0\n"
"buffer_load_dwordx4 v[32:35], v237, s[20:23], s48 offen offset:0 sc1\n"
"v_mfma_f32_16x16x32_bf16 v[160:163], a[80:83], v[100:103], v[160:163]\n"
"s_waitcnt lgkmcnt(15)\n"
"v_mfma_f32_16x16x32_bf16 v[160:163], a[96:99], v[104:107], v[160:163]\n"
"v_mfma_f32_16x16x32_bf16 v[160:163], a[112:115], v[108:111], v[160:163]\n"
"v_mfma_f32_16x16x32_bf16 v[164:167], a[68:71], v[96:99], 0\n"
"buffer_load_dwordx4 v[36:39], v237, s[20:23], s48 offen offset:1024 sc1\n"
"v_mfma_f32_16x16x32_bf16 v[164:167], a[84:87], v[100:103], v[164:167]\n"
"v_mfma_f32_16x16x32_bf16 v[164:167], a[100:103], v[104:107], v[164:167]\n"
"v_mfma_f32_16x16x32_bf16 v[164:167], a[116:119], v[108:111], v[164:167]\n"
"v_mfma_f32_16x16x32_bf16 v[168:171], a[72:75], v[96:99], 0\n"
"buffer_load_dwordx4 v[40:43], v237, s[20:23], s48 offen offset:2048 sc1\n"
"v_mfma_f32_16x16x32_bf16 v[168:171], a[88:91], v[100:103], v[168:171]\n"
"v_mfma_f32_16x16x32_bf16 v[168:171], a[104:107], v[104:107], v[168:171]\n"
"v_mfma_f32_16x16x32_bf16 v[168:171], a[120:123], v[108:111], v[168:171]\n"
"buffer_load_dwordx4 v[44:47], v237, s[20:23], s48 offen offset:3072 sc1\n"
"v_mfma_f32_16x16x32_bf16 v[172:175], a[76:79], v[96:99], 0\n"
"v_mfma_f32_16x16x32_bf16 v[172:175], a[92:95], v[100:103], v[172:175]\n"
"v_mfma_f32_16x16x32_bf16 v[172:175], a[108:111], v[104:107], v[172:175]\n"
"buffer_load_dwordx4 v[48:51], v238, s[20:23], s48 offen offset:0 sc1\n"
"v_mfma_f32_16x16x32_bf16 v[172:175], a[124:127], v[108:111], v[172:175]\n"
"v_mfma_f32_16x16x32_bf16 v[128:131], a[64:67], v[112:115], 0\n"
"v_mfma_f32_16x16x32_bf16 v[128:131], a[80:83], v[116:119], v[128:131]\n"
"buffer_load_dwordx4 v[52:55], v238, s[20:23], s48 offen offset:1024 sc1\n"
"v_mfma_f32_16x16x32_bf16 v[128:131], a[96:99], v[120:123], v[128:131]\n"
"v_mfma_f32_16x16x32_bf16 v[128:131], a[112:115], v[124:127], v[128:131]\n"
"v_mfma_f32_16x16x32_bf16 v[132:135], a[68:71], v[112:115], 0\n"
"buffer_load_dwordx4 v[56:59], v238, s[20:23], s48 offen offset:2048 sc1\n"
"v_mfma_f32_16x16x32_bf16 v[132:135], a[84:87], v[116:119], v[132:135]\n"
"v_mfma_f32_16x16x32_bf16 v[132:135], a[100:103], v[120:123], v[132:135]\n"
"v_mfma_f32_16x16x32_bf16 v[132:135], a[116:119], v[124:127], v[132:135]\n"
"buffer_load_dwordx4 v[60:63], v238, s[20:23], s48 offen offset:3072 sc1\n"
"v_mfma_f32_16x16x32_bf16 v[136:139], a[72:75], v[112:115], 0\n"
"v_mfma_f32_16x16x32_bf16 v[136:139], a[88:91], v[116:119], v[136:139]\n"
"v_mfma_f32_16x16x32_bf16 v[136:139], a[104:107], v[120:123], v[136:139]\n"
"buffer_load_dwordx4 v[64:67], v239, s[20:23], s48 offen offset:0 sc1\n"
"v_mfma_f32_16x16x32_bf16 v[136:139], a[120:123], v[124:127], v[136:139]\n"
"v_mfma_f32_16x16x32_bf16 v[140:143], a[76:79], v[112:115], 0\n"
"v_mfma_f32_16x16x32_bf16 v[140:143], a[92:95], v[116:119], v[140:143]\n"
"buffer_load_dwordx4 v[68:71], v239, s[20:23], s48 offen offset:1024 sc1\n"
"v_mfma_f32_16x16x32_bf16 v[140:143], a[108:111], v[120:123], v[140:143]\n"
"v_mfma_f32_16x16x32_bf16 v[140:143], a[124:127], v[124:127], v[140:143]\n"
"s_waitcnt vmcnt(18)\n"
"v_mfma_f32_16x16x32_bf16 v[176:179], a[128:131], v[192:195], v[176:179]\n"
"buffer_load_dwordx4 v[72:75], v239, s[20:23], s48 offen offset:2048 sc1\n"
"v_mfma_f32_16x16x32_bf16 v[176:179], a[144:147], v[196:199], v[176:179]\n"
"v_mfma_f32_16x16x32_bf16 v[176:179], a[160:163], v[200:203], v[176:179]\n"
"v_mfma_f32_16x16x32_bf16 v[176:179], a[176:179], v[204:207], v[176:179]\n"
"buffer_load_dwordx4 v[76:79], v239, s[20:23], s48 offen offset:3072 sc1\n"
"v_mfma_f32_16x16x32_bf16 v[180:183], a[132:135], v[192:195], v[180:183]\n"
"v_mfma_f32_16x16x32_bf16 v[180:183], a[148:151], v[196:199], v[180:183]\n"
"v_mfma_f32_16x16x32_bf16 v[180:183], a[164:167], v[200:203], v[180:183]\n"
"buffer_load_dwordx4 v[80:83], v236, s[20:23], s49 offen offset:0 sc1\n"
"v_mfma_f32_16x16x32_bf16 v[180:183], a[180:183], v[204:207], v[180:183]\n"
"v_mfma_f32_16x16x32_bf16 v[184:187], a[136:139], v[192:195], v[184:187]\n"
"v_mfma_f32_16x16x32_bf16 v[184:187], a[152:155], v[196:199], v[184:187]\n"
"buffer_load_dwordx4 v[84:87], v236, s[20:23], s49 offen offset:1024 sc1\n"
"v_mfma_f32_16x16x32_bf16 v[184:187], a[168:171], v[200:203], v[184:187]\n"
"ds_write_b64 v244, v[176:177] offset:32768\n"
"v_mfma_f32_16x16x32_bf16 v[184:187], a[184:187], v[204:207], v[184:187]\n"
"v_mfma_f32_16x16x32_bf16 v[188:191], a[140:143], v[192:195], v[188:191]\n"
"ds_write_b64 v244, v[178:179] offset:33280\n"
"v_mfma_f32_16x16x32_bf16 v[188:191], a[156:159], v[196:199], v[188:191]\n"
"buffer_load_dwordx4 v[88:91], v236, s[20:23], s49 offen offset:2048 sc1\n"
"v_mfma_f32_16x16x32_bf16 v[188:191], a[172:175], v[200:203], v[188:191]\n"
"ds_write_b64 v244, v[180:181] offset:33792\n"
"v_mfma_f32_16x16x32_bf16 v[188:191], a[188:191], v[204:207], v[188:191]\n"
"s_waitcnt vmcnt(19)\n"
"v_mfma_f32_16x16x32_bf16 v[144:147], a[128:131], v[0:3], v[144:147]\n"
"ds_write_b64 v244, v[182:183] offset:34304\n"
"v_mfma_f32_16x16x32_bf16 v[144:147], a[144:147], v[4:7], v[144:147]\n"
"buffer_load_dwordx4 v[92:95], v236, s[20:23], s49 offen offset:3072 sc1\n"
"v_mfma_f32_16x16x32_bf16 v[144:147], a[160:163], v[8:11], v[144:147]\n"
"ds_write_b64 v244, v[184:185] offset:34816\n"
"v_mfma_f32_16x16x32_bf16 v[144:147], a[176:179], v[12:15], v[144:147]\n"
"v_mfma_f32_16x16x32_bf16 v[148:151], a[132:135], v[0:3], v[148:151]\n"
"ds_write_b64 v244, v[186:187] offset:35328\n"
"v_mfma_f32_16x16x32_bf16 v[148:151], a[148:151], v[4:7], v[148:151]\n"
"buffer_load_dwordx4 v[96:99], v237, s[20:23], s49 offen offset:0 sc1\n"
"v_mfma_f32_16x16x32_bf16 v[148:151], a[164:167], v[8:11], v[148:151]\n"
"ds_write_b64 v244, v[188:189] offset:35840\n"
"v_mfma_f32_16x16x32_bf16 v[148:151], a[180:183], v[12:15], v[148:151]\n"
"v_mfma_f32_16x16x32_bf16 v[152:155], a[136:139], v[0:3], v[152:155]\n"
"ds_write_b64 v244, v[190:191] offset:36352\n"
"v_mfma_f32_16x16x32_bf16 v[152:155], a[152:155], v[4:7], v[152:155]\n"
"buffer_load_dwordx4 v[100:103], v237, s[20:23], s49 offen offset:1024 sc1\n"
"v_mfma_f32_16x16x32_bf16 v[152:155], a[168:171], v[8:11], v[152:155]\n"
"ds_write_b64 v244, v[144:145] offset:0\n"
"v_mfma_f32_16x16x32_bf16 v[152:155], a[184:187], v[12:15], v[152:155]\n"
"v_mfma_f32_16x16x32_bf16 v[156:159], a[140:143], v[0:3], v[156:159]\n"
"ds_write_b64 v244, v[146:147] offset:512\n"
"v_mfma_f32_16x16x32_bf16 v[156:159], a[156:159], v[4:7], v[156:159]\n"
"buffer_load_dwordx4 v[104:107], v237, s[20:23], s49 offen offset:2048 sc1\n"
"v_mfma_f32_16x16x32_bf16 v[156:159], a[172:175], v[8:11], v[156:159]\n"
"ds_write_b64 v244, v[148:149] offset:1024\n"
"v_mfma_f32_16x16x32_bf16 v[156:159], a[188:191], v[12:15], v[156:159]\n"
"v_mfma_f32_16x16x32_bf16 v[160:163], a[192:195], v[192:195], v[160:163]\n"
"ds_write_b64 v244, v[150:151] offset:1536\n"
"v_mfma_f32_16x16x32_bf16 v[160:163], a[208:211], v[196:199], v[160:163]\n"
"buffer_load_dwordx4 v[108:111], v237, s[20:23], s49 offen offset:3072 sc1\n"
"s_waitcnt lgkmcnt(15)\n"
"v_mfma_f32_16x16x32_bf16 v[160:163], a[224:227], v[200:203], v[160:163]\n"
"ds_write_b64 v244, v[152:153] offset:2048\n"
"v_mfma_f32_16x16x32_bf16 v[160:163], a[240:243], v[204:207], v[160:163]\n"
"v_bfe_i32 v240, v248, 4, 1\n"
"v_mfma_f32_16x16x32_bf16 v[164:167], a[196:199], v[192:195], v[164:167]\n"
"ds_write_b64 v244, v[154:155] offset:2560\n"
"v_mfma_f32_16x16x32_bf16 v[164:167], a[212:215], v[196:199], v[164:167]\n"
"v_bfi_b32 v240, v240, v243, v242\n"
"v_mfma_f32_16x16x32_bf16 v[164:167], a[228:231], v[200:203], v[164:167]\n"
"s_mov_b32 s42, 0xffffff00\n"
"ds_write_b64 v244, v[156:157] offset:3072\n"
"v_mfma_f32_16x16x32_bf16 v[164:167], a[244:247], v[204:207], v[164:167]\n"
"v_bfe_u32 v224, v240, 0, 8\n"
"v_mfma_f32_16x16x32_bf16 v[168:171], a[200:203], v[192:195], v[168:171]\n"
"ds_write_b64 v244, v[158:159] offset:3584\n"
"v_mfma_f32_16x16x32_bf16 v[168:171], a[216:219], v[196:199], v[168:171]\n"
"buffer_load_dwordx4 v[112:115], v238, s[20:23], s49 offen offset:0 sc1\n"
"v_mfma_f32_16x16x32_bf16 v[168:171], a[232:235], v[200:203], v[168:171]\n"
"ds_write_b64 v244, v[160:161] offset:4096\n"
"s_waitcnt lgkmcnt(15)\n"
"v_mfma_f32_16x16x32_bf16 v[168:171], a[248:251], v[204:207], v[168:171]\n"
"v_bfe_u32 v225, v240, 8, 8\n"
"v_mfma_f32_16x16x32_bf16 v[172:175], a[204:207], v[192:195], v[172:175]\n"
"ds_write_b64 v244, v[162:163] offset:4608\n"
"v_mfma_f32_16x16x32_bf16 v[172:175], a[220:223], v[196:199], v[172:175]\n"
"buffer_load_dwordx4 v[116:119], v238, s[20:23], s49 offen offset:1024 sc1\n"
"v_mfma_f32_16x16x32_bf16 v[172:175], a[236:239], v[200:203], v[172:175]\n"
"ds_write_b64 v244, v[164:165] offset:5120\n"
"v_mfma_f32_16x16x32_bf16 v[172:175], a[252:255], v[204:207], v[172:175]\n"
"v_bfe_u32 v226, v240, 16, 8\n"
"v_mfma_f32_16x16x32_bf16 v[128:131], a[192:195], v[0:3], v[128:131]\n"
"ds_write_b64 v244, v[166:167] offset:5632\n"
"v_mfma_f32_16x16x32_bf16 v[128:131], a[208:211], v[4:7], v[128:131]\n"
"buffer_load_dwordx4 v[120:123], v238, s[20:23], s49 offen offset:2048 sc1\n"
"v_mfma_f32_16x16x32_bf16 v[128:131], a[224:227], v[8:11], v[128:131]\n"
"ds_write_b64 v244, v[168:169] offset:6144\n"
"v_mfma_f32_16x16x32_bf16 v[128:131], a[240:243], v[12:15], v[128:131]\n"
"v_bfe_u32 v227, v240, 24, 8\n"
"v_mfma_f32_16x16x32_bf16 v[132:135], a[196:199], v[0:3], v[132:135]\n"
"ds_write_b64 v244, v[170:171] offset:6656\n"
"v_mfma_f32_16x16x32_bf16 v[132:135], a[212:215], v[4:7], v[132:135]\n"
"buffer_load_dwordx4 v[124:127], v238, s[20:23], s49 offen offset:3072 sc1\n"
"v_mfma_f32_16x16x32_bf16 v[132:135], a[228:231], v[8:11], v[132:135]\n"
"ds_write_b64 v244, v[172:173] offset:7168\n"
"v_mfma_f32_16x16x32_bf16 v[132:135], a[244:247], v[12:15], v[132:135]\n"
"v_mfma_f32_16x16x32_bf16 v[136:139], a[200:203], v[0:3], v[136:139]\n"
"ds_write_b64 v244, v[174:175] offset:7680\n"
"v_mfma_f32_16x16x32_bf16 v[136:139], a[216:219], v[4:7], v[136:139]\n"
"buffer_load_dwordx4 v[192:195], v239, s[20:23], s49 offen offset:0 sc1\n"
"v_mfma_f32_16x16x32_bf16 v[136:139], a[232:235], v[8:11], v[136:139]\n"
"v_mfma_f32_16x16x32_bf16 v[136:139], a[248:251], v[12:15], v[136:139]\n"
"v_xor_b32 v244, 65536, v244\n"
"v_mfma_f32_16x16x32_bf16 v[140:143], a[204:207], v[0:3], v[140:143]\n"
"s_waitcnt lgkmcnt(0)\n"
"s_barrier\n"
"v_mfma_f32_16x16x32_bf16 v[140:143], a[220:223], v[4:7], v[140:143]\n"
"ds_read2st64_b64 v[208:211], v245 offset0:0 offset1:1\n"
"v_mfma_f32_16x16x32_bf16 v[140:143], a[236:239], v[8:11], v[140:143]\n"
"v_mfma_f32_16x16x32_bf16 v[140:143], a[252:255], v[12:15], v[140:143]\n"
"s_waitcnt vmcnt(25)\n"
"buffer_load_dwordx4 v[196:199], v239, s[20:23], s49 offen offset:1024 sc1\n"
"v_mfma_f32_16x16x32_bf16 v[176:179], a[0:3], v[16:19], 0\n"
"ds_read2st64_b64 v[212:215], v246 offset0:8 offset1:9\n"
"v_mfma_f32_16x16x32_bf16 v[176:179], a[16:19], v[20:23], v[176:179]\n"
"v_mfma_f32_16x16x32_bf16 v[176:179], a[32:35], v[24:27], v[176:179]\n"
"buffer_load_dwordx4 v[200:203], v239, s[20:23], s49 offen offset:2048 sc1\n"
"v_mfma_f32_16x16x32_bf16 v[176:179], a[48:51], v[28:31], v[176:179]\n"
"ds_read2st64_b64 v[216:219], v247 offset0:64 offset1:65\n"
"v_mfma_f32_16x16x32_bf16 v[180:183], a[4:7], v[16:19], 0\n"
"s_waitcnt lgkmcnt(2)\n"
"v_add_f32 v128, v128, v208\n"
"v_add_f32 v129, v129, v209\n"
"v_mfma_f32_16x16x32_bf16 v[180:183], a[20:23], v[20:23], v[180:183]\n"
"v_add_f32 v130, v130, v210\n"
"v_add_f32 v131, v131, v211\n"
"v_mfma_f32_16x16x32_bf16 v[180:183], a[36:39], v[24:27], v[180:183]\n"
"ds_read2st64_b64 v[220:223], v245 offset0:2 offset1:3\n"
"v_mfma_f32_16x16x32_bf16 v[180:183], a[52:55], v[28:31], v[180:183]\n"
"s_waitcnt lgkmcnt(2)\n"
"v_add_f32 v128, v128, v212\n"
"v_add_f32 v129, v129, v213\n"
"v_mfma_f32_16x16x32_bf16 v[184:187], a[8:11], v[16:19], 0\n"
"v_add_f32 v130, v130, v214\n"
"v_add_f32 v131, v131, v215\n"
"v_mfma_f32_16x16x32_bf16 v[184:187], a[24:27], v[20:23], v[184:187]\n"
"ds_read2st64_b64 v[208:211], v246 offset0:10 offset1:11\n"
"v_mfma_f32_16x16x32_bf16 v[184:187], a[40:43], v[24:27], v[184:187]\n"
"s_waitcnt lgkmcnt(2)\n"
"v_add_f32 v128, v128, v216\n"
"v_add_f32 v129, v129, v217\n"
"v_mfma_f32_16x16x32_bf16 v[184:187], a[56:59], v[28:31], v[184:187]\n"
"v_add_f32 v130, v130, v218\n"
"v_add_f32 v131, v131, v219\n"
"v_mfma_f32_16x16x32_bf16 v[188:191], a[12:15], v[16:19], 0\n"
"ds_read2st64_b64 v[212:215], v247 offset0:66 offset1:67\n"
"v_and_or_b32 v128, v128, s42, v224\n"
"v_mfma_f32_16x16x32_bf16 v[188:191], a[28:31], v[20:23], v[188:191]\n"
"v_and_or_b32 v129, v129, s42, v225\n"
"s_waitcnt lgkmcnt(2)\n"
"v_add_f32 v132, v132, v220\n"
"v_add_f32 v133, v133, v221\n"
"v_mfma_f32_16x16x32_bf16 v[188:191], a[44:47], v[24:27], v[188:191]\n"
"v_and_or_b32 v130, v130, s42, v226\n"
"v_add_f32 v134, v134, v222\n"
"v_add_f32 v135, v135, v223\n"
"v_mfma_f32_16x16x32_bf16 v[188:191], a[60:63], v[28:31], v[188:191]\n"
"s_waitcnt vmcnt(23)\n"
"ds_read2st64_b64 v[216:219], v245 offset0:4 offset1:5\n"
"v_and_or_b32 v131, v131, s42, v227\n"
"v_mfma_f32_16x16x32_bf16 v[144:147], a[0:3], v[32:35], 0\n"
"buffer_store_dwordx4 v[128:131], v248, s[28:31], s44 offen offset:0 sc1\n"
"s_waitcnt lgkmcnt(2)\n"
"v_add_f32 v132, v132, v208\n"
"v_add_f32 v133, v133, v209\n"
"v_mfma_f32_16x16x32_bf16 v[144:147], a[16:19], v[36:39], v[144:147]\n"
"v_add_f32 v134, v134, v210\n"
"v_add_f32 v135, v135, v211\n"
"v_mfma_f32_16x16x32_bf16 v[144:147], a[32:35], v[40:43], v[144:147]\n"
"ds_read2st64_b64 v[220:223], v246 offset0:12 offset1:13\n"
"v_mfma_f32_16x16x32_bf16 v[144:147], a[48:51], v[44:47], v[144:147]\n"
"s_waitcnt lgkmcnt(2)\n"
"v_add_f32 v132, v132, v212\n"
"v_add_f32 v133, v133, v213\n"
"v_mfma_f32_16x16x32_bf16 v[148:151], a[4:7], v[32:35], 0\n"
"v_add_f32 v134, v134, v214\n"
"v_add_f32 v135, v135, v215\n"
"v_mfma_f32_16x16x32_bf16 v[148:151], a[20:23], v[36:39], v[148:151]\n"
"ds_read2st64_b64 v[208:211], v247 offset0:68 offset1:69\n"
"v_and_or_b32 v132, v132, s42, v224\n"
"v_mfma_f32_16x16x32_bf16 v[148:151], a[36:39], v[40:43], v[148:151]\n"
"v_and_or_b32 v133, v133, s42, v225\n"
"s_waitcnt lgkmcnt(2)\n"
"v_add_f32 v136, v136, v216\n"
"v_add_f32 v137, v137, v217\n"
"v_mfma_f32_16x16x32_bf16 v[148:151], a[52:55], v[44:47], v[148:151]\n"
"v_and_or_b32 v134, v134, s42, v226\n"
"v_add_f32 v138, v138, v218\n"
"v_add_f32 v139, v139, v219\n"
"v_mfma_f32_16x16x32_bf16 v[152:155], a[8:11], v[32:35], 0\n"
"ds_read2st64_b64 v[212:215], v245 offset0:6 offset1:7\n"
"v_and_or_b32 v135, v135, s42, v227\n"
"v_mfma_f32_16x16x32_bf16 v[152:155], a[24:27], v[36:39], v[152:155]\n"
"buffer_store_dwordx4 v[132:135], v248, s[28:31], s44 offen offset:1024 sc1\n"
"s_waitcnt lgkmcnt(2)\n"
"v_add_f32 v136, v136, v220\n"
"v_add_f32 v137, v137, v221\n"
"v_mfma_f32_16x16x32_bf16 v[152:155], a[40:43], v[40:43], v[152:155]\n"
"v_add_f32 v138, v138, v222\n"
"v_add_f32 v139, v139, v223\n"
"v_mfma_f32_16x16x32_bf16 v[152:155], a[56:59], v[44:47], v[152:155]\n"
"ds_read2st64_b64 v[216:219], v246 offset0:14 offset1:15\n"
"v_mfma_f32_16x16x32_bf16 v[156:159], a[12:15], v[32:35], 0\n"
"s_waitcnt lgkmcnt(2)\n"
"v_add_f32 v136, v136, v208\n"
"v_add_f32 v137, v137, v209\n"
"v_mfma_f32_16x16x32_bf16 v[156:159], a[28:31], v[36:39], v[156:159]\n"
"v_add_f32 v138, v138, v210\n"
"v_add_f32 v139, v139, v211\n"
"v_mfma_f32_16x16x32_bf16 v[156:159], a[44:47], v[40:43], v[156:159]\n"
"ds_read2st64_b64 v[220:223], v247 offset0:70 offset1:71\n"
"v_and_or_b32 v136, v136, s42, v224\n"
"v_mfma_f32_16x16x32_bf16 v[156:159], a[60:63], v[44:47], v[156:159]\n"
"v_and_or_b32 v137, v137, s42, v225\n"
"s_waitcnt lgkmcnt(2)\n"
"v_add_f32 v140, v140, v212\n"
"v_add_f32 v141, v141, v213\n"
"v_mfma_f32_16x16x32_bf16 v[160:163], a[64:67], v[16:19], 0\n"
"v_and_or_b32 v138, v138, s42, v226\n"
"v_add_f32 v142, v142, v214\n"
"v_add_f32 v143, v143, v215\n"
"v_mfma_f32_16x16x32_bf16 v[160:163], a[80:83], v[20:23], v[160:163]\n"
"v_and_or_b32 v139, v139, s42, v227\n"
"v_mfma_f32_16x16x32_bf16 v[160:163], a[96:99], v[24:27], v[160:163]\n"
"buffer_store_dwordx4 v[136:139], v248, s[28:31], s44 offen offset:2048 sc1\n"
"s_waitcnt lgkmcnt(1)\n"
"v_add_f32 v140, v140, v216\n"
"v_add_f32 v141, v141, v217\n"
"v_mfma_f32_16x16x32_bf16 v[160:163], a[112:115], v[28:31], v[160:163]\n"
"v_add_f32 v142, v142, v218\n"
"v_add_f32 v143, v143, v219\n"
"v_mfma_f32_16x16x32_bf16 v[164:167], a[68:71], v[16:19], 0\n"
"v_mfma_f32_16x16x32_bf16 v[164:167], a[84:87], v[20:23], v[164:167]\n"
"s_waitcnt lgkmcnt(0)\n"
"v_add_f32 v140, v140, v220\n"
"v_add_f32 v141, v141, v221\n"
"v_mfma_f32_16x16x32_bf16 v[164:167], a[100:103], v[24:27], v[164:167]\n"
"v_add_f32 v142, v142, v222\n"
"v_add_f32 v143, v143, v223\n"
"v_mfma_f32_16x16x32_bf16 v[164:167], a[116:119], v[28:31], v[164:167]\n"
"v_and_or_b32 v140, v140, s42, v224\n"
"v_and_or_b32 v141, v141, s42, v225\n"
"v_mfma_f32_16x16x32_bf16 v[168:171], a[72:75], v[16:19], 0\n"
"v_and_or_b32 v142, v142, s42, v226\n"
"v_and_or_b32 v143, v143, s42, v227\n"
"v_mfma_f32_16x16x32_bf16 v[168:171], a[88:91], v[20:23], v[168:171]\n"
"buffer_store_dwordx4 v[140:143], v248, s[28:31], s44 offen offset:3072 sc1\n"
"v_mfma_f32_16x16x32_bf16 v[168:171], a[104:107], v[24:27], v[168:171]\n"
"s_xor_b32 s44, s44, 69632\n"
"v_mfma_f32_16x16x32_bf16 v[168:171], a[120:123], v[28:31], v[168:171]\n"
"v_xor_b32 v245, 65536, v245\n"
"v_mfma_f32_16x16x32_bf16 v[172:175], a[76:79], v[16:19], 0\n"
"v_xor_b32 v246, 65536, v246\n"
"v_mfma_f32_16x16x32_bf16 v[172:175], a[92:95], v[20:23], v[172:175]\n"
"v_xor_b32 v247, 65536, v247\n"
"v_mfma_f32_16x16x32_bf16 v[172:175], a[108:111], v[24:27], v[172:175]\n"
"buffer_load_dwordx4 v[204:207], v239, s[20:23], s49 offen offset:3072 sc1\n"
"v_mfma_f32_16x16x32_bf16 v[172:175], a[124:127], v[28:31], v[172:175]\n"
"s_waitcnt vmcnt(24)\n"
"v_mfma_f32_16x16x32_bf16 v[176:179], a[128:131], v[48:51], v[176:179]\n"
"v_mfma_f32_16x16x32_bf16 v[176:179], a[144:147], v[52:55], v[176:179]\n"
"buffer_load_dwordx4 v[0:3], v236, s[20:23], s51 offen offset:0 sc1\n"
"v_mfma_f32_16x16x32_bf16 v[176:179], a[160:163], v[56:59], v[176:179]\n"
"v_mfma_f32_16x16x32_bf16 v[176:179], a[176:179], v[60:63], v[176:179]\n"
"v_mfma_f32_16x16x32_bf16 v[180:183], a[132:135], v[48:51], v[180:183]\n"
"buffer_load_dwordx4 v[4:7], v236, s[20:23], s51 offen offset:1024 sc1\n"
"v_mfma_f32_16x16x32_bf16 v[180:183], a[148:151], v[52:55], v[180:183]\n"
"v_mfma_f32_16x16x32_bf16 v[180:183], a[164:167], v[56:59], v[180:183]\n"
"v_mfma_f32_16x16x32_bf16 v[180:183], a[180:183], v[60:63], v[180:183]\n"
"buffer_load_dwordx4 v[8:11], v236, s[20:23], s51 offen offset:2048 sc1\n"
"v_mfma_f32_16x16x32_bf16 v[184:187], a[136:139], v[48:51], v[184:187]\n"
"v_mfma_f32_16x16x32_bf16 v[184:187], a[152:155], v[52:55], v[184:187]\n"
"v_mfma_f32_16x16x32_bf16 v[184:187], a[168:171], v[56:59], v[184:187]\n"
"ds_write_b64 v244, v[176:177] offset:32768\n"
"v_mfma_f32_16x16x32_bf16 v[184:187], a[184:187], v[60:63], v[184:187]\n"
"buffer_load_dwordx4 v[12:15], v236, s[20:23], s51 offen offset:3072 sc1\n"
"v_mfma_f32_16x16x32_bf16 v[188:191], a[140:143], v[48:51], v[188:191]\n"
"ds_write_b64 v244, v[178:179] offset:33280\n"
"v_mfma_f32_16x16x32_bf16 v[188:191], a[156:159], v[52:55], v[188:191]\n"
"v_mfma_f32_16x16x32_bf16 v[188:191], a[172:175], v[56:59], v[188:191]\n"
"ds_write_b64 v244, v[180:181] offset:33792\n"
"v_mfma_f32_16x16x32_bf16 v[188:191], a[188:191], v[60:63], v[188:191]\n"
"s_waitcnt vmcnt(24)\n"
"buffer_load_dwordx4 v[16:19], v237, s[20:23], s51 offen offset:0 sc1\n"
"v_mfma_f32_16x16x32_bf16 v[144:147], a[128:131], v[64:67], v[144:147]\n"
"ds_write_b64 v244, v[182:183] offset:34304\n"
"v_mfma_f32_16x16x32_bf16 v[144:147], a[144:147], v[68:71], v[144:147]\n"
"v_mfma_f32_16x16x32_bf16 v[144:147], a[160:163], v[72:75], v[144:147]\n"
"ds_write_b64 v244, v[184:185] offset:34816\n"
"v_mfma_f32_16x16x32_bf16 v[144:147], a[176:179], v[76:79], v[144:147]\n"
"buffer_load_dwordx4 v[20:23], v237, s[20:23], s51 offen offset:1024 sc1\n"
"v_mfma_f32_16x16x32_bf16 v[148:151], a[132:135], v[64:67], v[148:151]\n"
"ds_write_b64 v244, v[186:187] offset:35328\n"
"v_mfma_f32_16x16x32_bf16 v[148:151], a[148:151], v[68:71], v[148:151]\n"
"v_mfma_f32_16x16x32_bf16 v[148:151], a[164:167], v[72:75], v[148:151]\n"
"ds_write_b64 v244, v[188:189] offset:35840\n"
"v_mfma_f32_16x16x32_bf16 v[148:151], a[180:183], v[76:79], v[148:151]\n"
"buffer_load_dwordx4 v[24:27], v237, s[20:23], s51 offen offset:2048 sc1\n"
"v_mfma_f32_16x16x32_bf16 v[152:155], a[136:139], v[64:67], v[152:155]\n"
"s_add_u32 s57, s46, 4096\n"
"ds_write_b64 v244, v[190:191] offset:36352\n"
"v_mfma_f32_16x16x32_bf16 v[152:155], a[152:155], v[68:71], v[152:155]\n"
"v_mfma_f32_16x16x32_bf16 v[152:155], a[168:171], v[72:75], v[152:155]\n"
"s_add_u32 s59, s46, 8192\n"
"ds_write_b64 v244, v[144:145] offset:0\n"
"v_mfma_f32_16x16x32_bf16 v[152:155], a[184:187], v[76:79], v[152:155]\n"
"buffer_load_dwordx4 v[28:31], v237, s[20:23], s51 offen offset:3072 sc1\n"
"v_mfma_f32_16x16x32_bf16 v[156:159], a[140:143], v[64:67], v[156:159]\n"
"ds_write_b64 v244, v[146:147] offset:512\n"
"v_mfma_f32_16x16x32_bf16 v[156:159], a[156:159], v[68:71], v[156:159]\n"
"v_mfma_f32_16x16x32_bf16 v[156:159], a[172:175], v[72:75], v[156:159]\n"
"ds_write_b64 v244, v[148:149] offset:1024\n"
"v_mfma_f32_16x16x32_bf16 v[156:159], a[188:191], v[76:79], v[156:159]\n"
"v_mfma_f32_16x16x32_bf16 v[160:163], a[192:195], v[48:51], v[160:163]\n"
"ds_write_b64 v244, v[150:151] offset:1536\n"
"v_mfma_f32_16x16x32_bf16 v[160:163], a[208:211], v[52:55], v[160:163]\n"
"v_mfma_f32_16x16x32_bf16 v[160:163], a[224:227], v[56:59], v[160:163]\n"
"ds_write_b64 v244, v[152:153] offset:2048\n"
"v_mfma_f32_16x16x32_bf16 v[160:163], a[240:243], v[60:63], v[160:163]\n"
"v_mfma_f32_16x16x32_bf16 v[164:167], a[196:199], v[48:51], v[164:167]\n"
"ds_write_b64 v244, v[154:155] offset:2560\n"
"v_mfma_f32_16x16x32_bf16 v[164:167], a[212:215], v[52:55], v[164:167]\n"
"v_mfma_f32_16x16x32_bf16 v[164:167], a[228:231], v[56:59], v[164:167]\n"
"ds_write_b64 v244, v[156:157] offset:3072\n"
"v_mfma_f32_16x16x32_bf16 v[164:167], a[244:247], v[60:63], v[164:167]\n"
"v_mfma_f32_16x16x32_bf16 v[168:171], a[200:203], v[48:51], v[168:171]\n"
"ds_write_b64 v244, v[158:159] offset:3584\n"
"v_mfma_f32_16x16x32_bf16 v[168:171], a[216:219], v[52:55], v[168:171]\n"
"v_mfma_f32_16x16x32_bf16 v[168:171], a[232:235], v[56:59], v[168:171]\n"
"ds_write_b64 v244, v[160:161] offset:4096\n"
"v_mfma_f32_16x16x32_bf16 v[168:171], a[248:251], v[60:63], v[168:171]\n"
"v_mfma_f32_16x16x32_bf16 v[172:175], a[204:207], v[48:51], v[172:175]\n"
"ds_write_b64 v244, v[162:163] offset:4608\n"
"v_mfma_f32_16x16x32_bf16 v[172:175], a[220:223], v[52:55], v[172:175]\n"
"v_mfma_f32_16x16x32_bf16 v[172:175], a[236:239], v[56:59], v[172:175]\n"
"ds_write_b64 v244, v[164:165] offset:5120\n"
"v_mfma_f32_16x16x32_bf16 v[172:175], a[252:255], v[60:63], v[172:175]\n"
"v_mfma_f32_16x16x32_bf16 v[128:131], a[64:67], v[32:35], 0\n"
"ds_write_b64 v244, v[166:167] offset:5632\n"
"v_mfma_f32_16x16x32_bf16 v[128:131], a[80:83], v[36:39], v[128:131]\n"
"buffer_load_dwordx4 v[48:51], v238, s[20:23], s51 offen offset:0 sc1\n"
"v_mfma_f32_16x16x32_bf16 v[128:131], a[96:99], v[40:43], v[128:131]\n"
"ds_write_b64 v244, v[168:169] offset:6144\n"
"v_mfma_f32_16x16x32_bf16 v[128:131], a[112:115], v[44:47], v[128:131]\n"
"v_mfma_f32_16x16x32_bf16 v[128:131], a[192:195], v[64:67], v[128:131]\n"
"ds_write_b64 v244, v[170:171] offset:6656\n"
"v_mfma_f32_16x16x32_bf16 v[128:131], a[208:211], v[68:71], v[128:131]\n"
"buffer_load_dwordx4 v[52:55], v238, s[20:23], s51 offen offset:1024 sc1\n"
"v_mfma_f32_16x16x32_bf16 v[128:131], a[224:227], v[72:75], v[128:131]\n"
"ds_write_b64 v244, v[172:173] offset:7168\n"
"v_mfma_f32_16x16x32_bf16 v[128:131], a[240:243], v[76:79], v[128:131]\n"
"v_mfma_f32_16x16x32_bf16 v[132:135], a[68:71], v[32:35], 0\n"
"ds_write_b64 v244, v[174:175] offset:7680\n"
"v_mfma_f32_16x16x32_bf16 v[132:135], a[84:87], v[36:39], v[132:135]\n"
"buffer_load_dwordx4 v[56:59], v238, s[20:23], s51 offen offset:2048 sc1\n"
"v_mfma_f32_16x16x32_bf16 v[132:135], a[100:103], v[40:43], v[132:135]\n"
"v_mfma_f32_16x16x32_bf16 v[132:135], a[116:119], v[44:47], v[132:135]\n"
"v_xor_b32 v244, 65536, v244\n"
"v_mfma_f32_16x16x32_bf16 v[132:135], a[196:199], v[64:67], v[132:135]\n"
"s_waitcnt lgkmcnt(0)\n"
"s_barrier\n"
"v_mfma_f32_16x16x32_bf16 v[132:135], a[212:215], v[68:71], v[132:135]\n"
"ds_read2st64_b64 v[208:211], v245 offset0:0 offset1:1\n"
"v_mfma_f32_16x16x32_bf16 v[132:135], a[228:231], v[72:75], v[132:135]\n"
"v_mfma_f32_16x16x32_bf16 v[132:135], a[244:247], v[76:79], v[132:135]\n"
"buffer_load_dwordx4 v[60:63], v238, s[20:23], s51 offen offset:3072 sc1\n"
"v_mfma_f32_16x16x32_bf16 v[136:139], a[72:75], v[32:35], 0\n"
"ds_read2st64_b64 v[212:215], v246 offset0:8 offset1:9\n"
"v_mfma_f32_16x16x32_bf16 v[136:139], a[88:91], v[36:39], v[136:139]\n"
"v_mfma_f32_16x16x32_bf16 v[136:139], a[104:107], v[40:43], v[136:139]\n"
"v_mfma_f32_16x16x32_bf16 v[136:139], a[120:123], v[44:47], v[136:139]\n"
"ds_read2st64_b64 v[216:219], v247 offset0:64 offset1:65\n"
"v_mfma_f32_16x16x32_bf16 v[136:139], a[200:203], v[64:67], v[136:139]\n"
"s_waitcnt lgkmcnt(2)\n"
"v_add_f32 v128, v128, v208\n"
"v_add_f32 v129, v129, v209\n"
"v_mfma_f32_16x16x32_bf16 v[136:139], a[216:219], v[68:71], v[136:139]\n"
"v_add_f32 v130, v130, v210\n"
"v_add_f32 v131, v131, v211\n"
"v_mfma_f32_16x16x32_bf16 v[136:139], a[232:235], v[72:75], v[136:139]\n"
"ds_read2st64_b64 v[220:223], v245 offset0:2 offset1:3\n"
"v_mfma_f32_16x16x32_bf16 v[136:139], a[248:251], v[76:79], v[136:139]\n"
"s_waitcnt lgkmcnt(2)\n"
"v_add_f32 v128, v128, v212\n"
"v_add_f32 v129, v129, v213\n"
"v_mfma_f32_16x16x32_bf16 v[140:143], a[76:79], v[32:35], 0\n"
"v_add_f32 v130, v130, v214\n"
"v_add_f32 v131, v131, v215\n"
"v_mfma_f32_16x16x32_bf16 v[140:143], a[92:95], v[36:39], v[140:143]\n"
"ds_read2st64_b64 v[208:211], v246 offset0:10 offset1:11\n"
"v_mfma_f32_16x16x32_bf16 v[140:143], a[108:111], v[40:43], v[140:143]\n"
"s_waitcnt lgkmcnt(2)\n"
"v_add_f32 v128, v128, v216\n"
"v_add_f32 v129, v129, v217\n"
"v_mfma_f32_16x16x32_bf16 v[140:143], a[124:127], v[44:47], v[140:143]\n"
"v_add_f32 v130, v130, v218\n"
"v_add_f32 v131, v131, v219\n"
"v_mfma_f32_16x16x32_bf16 v[140:143], a[204:207], v[64:67], v[140:143]\n"
"ds_read2st64_b64 v[212:215], v247 offset0:66 offset1:67\n"
"v_and_or_b32 v128, v128, s42, v224\n"
"v_mfma_f32_16x16x32_bf16 v[140:143], a[220:223], v[68:71], v[140:143]\n"
"v_and_or_b32 v129, v129, s42, v225\n"
"s_waitcnt lgkmcnt(2)\n"
"v_add_f32 v132, v132, v220\n"
"v_add_f32 v133, v133, v221\n"
"v_mfma_f32_16x16x32_bf16 v[140:143], a[236:239], v[72:75], v[140:143]\n"
"v_and_or_b32 v130, v130, s42, v226\n"
"v_add_f32 v134, v134, v222\n"
"v_add_f32 v135, v135, v223\n"
"v_mfma_f32_16x16x32_bf16 v[140:143], a[252:255], v[76:79], v[140:143]\n"
"s_waitcnt vmcnt(28)\n"
"ds_read2st64_b64 v[216:219], v245 offset0:4 offset1:5\n"
"v_and_or_b32 v131, v131, s42, v227\n"
"v_mfma_f32_16x16x32_bf16 v[176:179], a[0:3], v[80:83], 0\n"
"buffer_store_dwordx4 v[128:131], v248, s[28:31], s44 offen offset:0 sc1\n"
"s_waitcnt lgkmcnt(2)\n"
"v_add_f32 v132, v132, v208\n"
"v_add_f32 v133, v133, v209\n"
"v_mfma_f32_16x16x32_bf16 v[176:179], a[16:19], v[84:87], v[176:179]\n"
"v_add_f32 v134, v134, v210\n"
"v_add_f32 v135, v135, v211\n"
"v_mfma_f32_16x16x32_bf16 v[176:179], a[32:35], v[88:91], v[176:179]\n"
"ds_read2st64_b64 v[220:223], v246 offset0:12 offset1:13\n"
"v_mfma_f32_16x16x32_bf16 v[176:179], a[48:51], v[92:95], v[176:179]\n"
"s_waitcnt lgkmcnt(2)\n"
"v_add_f32 v132, v132, v212\n"
"v_add_f32 v133, v133, v213\n"
"v_mfma_f32_16x16x32_bf16 v[180:183], a[4:7], v[80:83], 0\n"
"v_add_f32 v134, v134, v214\n"
"v_add_f32 v135, v135, v215\n"
"v_mfma_f32_16x16x32_bf16 v[180:183], a[20:23], v[84:87], v[180:183]\n"
"ds_read2st64_b64 v[208:211], v247 offset0:68 offset1:69\n"
"v_and_or_b32 v132, v132, s42, v224\n"
"v_mfma_f32_16x16x32_bf16 v[180:183], a[36:39], v[88:91], v[180:183]\n"
"v_and_or_b32 v133, v133, s42, v225\n"
"s_waitcnt lgkmcnt(2)\n"
"v_add_f32 v136, v136, v216\n"
"v_add_f32 v137, v137, v217\n"
"v_mfma_f32_16x16x32_bf16 v[180:183], a[52:55], v[92:95], v[180:183]\n"
"v_and_or_b32 v134, v134, s42, v226\n"
"v_add_f32 v138, v138, v218\n"
"v_add_f32 v139, v139, v219\n"
"v_mfma_f32_16x16x32_bf16 v[184:187], a[8:11], v[80:83], 0\n"
"ds_read2st64_b64 v[212:215], v245 offset0:6 offset1:7\n"
"v_and_or_b32 v135, v135, s42, v227\n"
"v_mfma_f32_16x16x32_bf16 v[184:187], a[24:27], v[84:87], v[184:187]\n"
"buffer_store_dwordx4 v[132:135], v248, s[28:31], s44 offen offset:1024 sc1\n"
"s_waitcnt lgkmcnt(2)\n"
"v_add_f32 v136, v136, v220\n"
"v_add_f32 v137, v137, v221\n"
"v_mfma_f32_16x16x32_bf16 v[184:187], a[40:43], v[88:91], v[184:187]\n"
"v_add_f32 v138, v138, v222\n"
"v_add_f32 v139, v139, v223\n"
"v_mfma_f32_16x16x32_bf16 v[184:187], a[56:59], v[92:95], v[184:187]\n"
"ds_read2st64_b64 v[216:219], v246 offset0:14 offset1:15\n"
"v_mfma_f32_16x16x32_bf16 v[188:191], a[12:15], v[80:83], 0\n"
"s_waitcnt lgkmcnt(2)\n"
"v_add_f32 v136, v136, v208\n"
"v_add_f32 v137, v137, v209\n"
"v_mfma_f32_16x16x32_bf16 v[188:191], a[28:31], v[84:87], v[188:191]\n"
"v_add_f32 v138, v138, v210\n"
"v_add_f32 v139, v139, v211\n"
"v_mfma_f32_16x16x32_bf16 v[188:191], a[44:47], v[88:91], v[188:191]\n"
"ds_read2st64_b64 v[220:223], v247 offset0:70 offset1:71\n"
"v_and_or_b32 v136, v136, s42, v224\n"
"v_mfma_f32_16x16x32_bf16 v[188:191], a[60:63], v[92:95], v[188:191]\n"
"s_waitcnt vmcnt(26)\n"
"v_and_or_b32 v137, v137, s42, v225\n"
"s_waitcnt lgkmcnt(2)\n"
"v_add_f32 v140, v140, v212\n"
"v_add_f32 v141, v141, v213\n"
"v_mfma_f32_16x16x32_bf16 v[144:147], a[0:3], v[96:99], 0\n"
"v_and_or_b32 v138, v138, s42, v226\n"
"v_add_f32 v142, v142, v214\n"
"v_add_f32 v143, v143, v215\n"
"v_mfma_f32_16x16x32_bf16 v[144:147], a[16:19], v[100:103], v[144:147]\n"
"v_and_or_b32 v139, v139, s42, v227\n"
"v_mfma_f32_16x16x32_bf16 v[144:147], a[32:35], v[104:107], v[144:147]\n"
"buffer_store_dwordx4 v[136:139], v248, s[28:31], s44 offen offset:2048 sc1\n"
"s_waitcnt lgkmcnt(1)\n"
"v_add_f32 v140, v140, v216\n"
"v_add_f32 v141, v141, v217\n"
"v_mfma_f32_16x16x32_bf16 v[144:147], a[48:51], v[108:111], v[144:147]\n"
"v_add_f32 v142, v142, v218\n"
"v_add_f32 v143, v143, v219\n"
"v_mfma_f32_16x16x32_bf16 v[148:151], a[4:7], v[96:99], 0\n"
"v_mfma_f32_16x16x32_bf16 v[148:151], a[20:23], v[100:103], v[148:151]\n"
"s_waitcnt lgkmcnt(0)\n"
"v_add_f32 v140, v140, v220\n"
"v_add_f32 v141, v141, v221\n"
"v_mfma_f32_16x16x32_bf16 v[148:151], a[36:39], v[104:107], v[148:151]\n"
"v_add_f32 v142, v142, v222\n"
"v_add_f32 v143, v143, v223\n"
"v_mfma_f32_16x16x32_bf16 v[148:151], a[52:55], v[108:111], v[148:151]\n"
"v_and_or_b32 v140, v140, s42, v224\n"
"v_and_or_b32 v141, v141, s42, v225\n"
"v_mfma_f32_16x16x32_bf16 v[152:155], a[8:11], v[96:99], 0\n"
"v_and_or_b32 v142, v142, s42, v226\n"
"v_and_or_b32 v143, v143, s42, v227\n"
"v_mfma_f32_16x16x32_bf16 v[152:155], a[24:27], v[100:103], v[152:155]\n"
"buffer_store_dwordx4 v[140:143], v248, s[28:31], s44 offen offset:3072 sc1\n"
"v_mfma_f32_16x16x32_bf16 v[152:155], a[40:43], v[104:107], v[152:155]\n"
"s_xor_b32 s44, s44, 208896\n"
"v_mfma_f32_16x16x32_bf16 v[152:155], a[56:59], v[108:111], v[152:155]\n"
"v_xor_b32 v245, 65536, v245\n"
"v_mfma_f32_16x16x32_bf16 v[156:159], a[12:15], v[96:99], 0\n"
"v_xor_b32 v246, 65536, v246\n"
"v_mfma_f32_16x16x32_bf16 v[156:159], a[28:31], v[100:103], v[156:159]\n"
"v_xor_b32 v247, 65536, v247\n"
"v_mfma_f32_16x16x32_bf16 v[156:159], a[44:47], v[104:107], v[156:159]\n"
"buffer_load_dwordx4 v[32:35], v239, s[20:23], s51 offen offset:0 sc1\n"
"v_mfma_f32_16x16x32_bf16 v[156:159], a[60:63], v[108:111], v[156:159]\n"
"v_mfma_f32_16x16x32_bf16 v[160:163], a[64:67], v[80:83], 0\n"
"v_mfma_f32_16x16x32_bf16 v[160:163], a[80:83], v[84:87], v[160:163]\n"
"buffer_load_dwordx4 v[36:39], v239, s[20:23], s51 offen offset:1024 sc1\n"
"v_mfma_f32_16x16x32_bf16 v[160:163], a[96:99], v[88:91], v[160:163]\n"
"v_mfma_f32_16x16x32_bf16 v[160:163], a[112:115], v[92:95], v[160:163]\n"
"v_mfma_f32_16x16x32_bf16 v[164:167], a[68:71], v[80:83], 0\n"
"buffer_load_dwordx4 v[40:43], v239, s[20:23], s51 offen offset:2048 sc1\n"
"v_mfma_f32_16x16x32_bf16 v[164:167], a[84:87], v[84:87], v[164:167]\n"
"v_mfma_f32_16x16x32_bf16 v[164:167], a[100:103], v[88:91], v[164:167]\n"
"v_mfma_f32_16x16x32_bf16 v[164:167], a[116:119], v[92:95], v[164:167]\n"
"buffer_load_dwordx4 v[44:47], v239, s[20:23], s51 offen offset:3072 sc1\n"
"v_mfma_f32_16x16x32_bf16 v[168:171], a[72:75], v[80:83], 0\n"
"v_mfma_f32_16x16x32_bf16 v[168:171], a[88:91], v[84:87], v[168:171]\n"
"v_mfma_f32_16x16x32_bf16 v[168:171], a[104:107], v[88:91], v[168:171]\n"
"v_mfma_f32_16x16x32_bf16 v[168:171], a[120:123], v[92:95], v[168:171]\n"
"v_mfma_f32_16x16x32_bf16 v[172:175], a[76:79], v[80:83], 0\n"
"v_mfma_f32_16x16x32_bf16 v[172:175], a[92:95], v[84:87], v[172:175]\n"
"v_mfma_f32_16x16x32_bf16 v[172:175], a[108:111], v[88:91], v[172:175]\n"
"v_mfma_f32_16x16x32_bf16 v[172:175], a[124:127], v[92:95], v[172:175]\n"
"s_waitcnt vmcnt(28)\n"
"v_mfma_f32_16x16x32_bf16 v[176:179], a[128:131], v[112:115], v[176:179]\n"
"v_mfma_f32_16x16x32_bf16 v[176:179], a[144:147], v[116:119], v[176:179]\n"
"v_mfma_f32_16x16x32_bf16 v[176:179], a[160:163], v[120:123], v[176:179]\n"
"v_mfma_f32_16x16x32_bf16 v[176:179], a[176:179], v[124:127], v[176:179]\n"
"v_mfma_f32_16x16x32_bf16 v[180:183], a[132:135], v[112:115], v[180:183]\n"
"v_mfma_f32_16x16x32_bf16 v[180:183], a[148:151], v[116:119], v[180:183]\n"
"v_mfma_f32_16x16x32_bf16 v[180:183], a[164:167], v[120:123], v[180:183]\n"
"v_mfma_f32_16x16x32_bf16 v[180:183], a[180:183], v[124:127], v[180:183]\n"
"v_mfma_f32_16x16x32_bf16 v[184:187], a[136:139], v[112:115], v[184:187]\n"
"v_mfma_f32_16x16x32_bf16 v[184:187], a[152:155], v[116:119], v[184:187]\n"
"v_mfma_f32_16x16x32_bf16 v[184:187], a[168:171], v[120:123], v[184:187]\n"
"s_nop 0\n"
"ds_write_b64 v244, v[176:177] offset:32768\n"
"v_mfma_f32_16x16x32_bf16 v[184:187], a[184:187], v[124:127], v[184:187]\n"
"v_mfma_f32_16x16x32_bf16 v[188:191], a[140:143], v[112:115], v[188:191]\n"
"ds_write_b64 v244, v[178:179] offset:33280\n"
"v_mfma_f32_16x16x32_bf16 v[188:191], a[156:159], v[116:119], v[188:191]\n"
"v_mfma_f32_16x16x32_bf16 v[188:191], a[172:175], v[120:123], v[188:191]\n"
"ds_write_b64 v244, v[180:181] offset:33792\n"
"v_mfma_f32_16x16x32_bf16 v[188:191], a[188:191], v[124:127], v[188:191]\n"
"s_waitcnt vmcnt(20)\n"
"v_mfma_f32_16x16x32_bf16 v[144:147], a[128:131], v[192:195], v[144:147]\n"
"ds_write_b64 v244, v[182:183] offset:34304\n"
"v_mfma_f32_16x16x32_bf16 v[144:147], a[144:147], v[196:199], v[144:147]\n"
"v_mfma_f32_16x16x32_bf16 v[144:147], a[160:163], v[200:203], v[144:147]\n"
"ds_write_b64 v244, v[184:185] offset:34816\n"
"v_mfma_f32_16x16x32_bf16 v[144:147], a[176:179], v[204:207], v[144:147]\n"
"v_mfma_f32_16x16x32_bf16 v[148:151], a[132:135], v[192:195], v[148:151]\n"
"ds_write_b64 v244, v[186:187] offset:35328\n"
"v_mfma_f32_16x16x32_bf16 v[148:151], a[148:151], v[196:199], v[148:151]\n"
"v_mfma_f32_16x16x32_bf16 v[148:151], a[164:167], v[200:203], v[148:151]\n"
"ds_write_b64 v244, v[188:189] offset:35840\n"
"v_mfma_f32_16x16x32_bf16 v[148:151], a[180:183], v[204:207], v[148:151]\n"
"v_mfma_f32_16x16x32_bf16 v[152:155], a[136:139], v[192:195], v[152:155]\n"
"ds_write_b64 v244, v[190:191] offset:36352\n"
"v_mfma_f32_16x16x32_bf16 v[152:155], a[152:155], v[196:199], v[152:155]\n"
"v_mfma_f32_16x16x32_bf16 v[152:155], a[168:171], v[200:203], v[152:155]\n"
"ds_write_b64 v244, v[144:145] offset:0\n"
"v_mfma_f32_16x16x32_bf16 v[152:155], a[184:187], v[204:207], v[152:155]\n"
"v_mfma_f32_16x16x32_bf16 v[156:159], a[140:143], v[192:195], v[156:159]\n"
"ds_write_b64 v244, v[146:147] offset:512\n"
"v_mfma_f32_16x16x32_bf16 v[156:159], a[156:159], v[196:199], v[156:159]\n"
"v_mfma_f32_16x16x32_bf16 v[156:159], a[172:175], v[200:203], v[156:159]\n"
"ds_write_b64 v244, v[148:149] offset:1024\n"
"v_mfma_f32_16x16x32_bf16 v[156:159], a[188:191], v[204:207], v[156:159]\n"
"v_mfma_f32_16x16x32_bf16 v[160:163], a[192:195], v[112:115], v[160:163]\n"
"ds_write_b64 v244, v[150:151] offset:1536\n"
"v_mfma_f32_16x16x32_bf16 v[160:163], a[208:211], v[116:119], v[160:163]\n"
"v_mfma_f32_16x16x32_bf16 v[160:163], a[224:227], v[120:123], v[160:163]\n"
"ds_write_b64 v244, v[152:153] offset:2048\n"
"v_mfma_f32_16x16x32_bf16 v[160:163], a[240:243], v[124:127], v[160:163]\n"
"v_mfma_f32_16x16x32_bf16 v[164:167], a[196:199], v[112:115], v[164:167]\n"
"ds_write_b64 v244, v[154:155] offset:2560\n"
"v_mfma_f32_16x16x32_bf16 v[164:167], a[212:215], v[116:119], v[164:167]\n"
"v_mfma_f32_16x16x32_bf16 v[164:167], a[228:231], v[120:123], v[164:167]\n"
"ds_write_b64 v244, v[156:157] offset:3072\n"
"v_mfma_f32_16x16x32_bf16 v[164:167], a[244:247], v[124:127], v[164:167]\n"
"v_mfma_f32_16x16x32_bf16 v[168:171], a[200:203], v[112:115], v[168:171]\n"
"ds_write_b64 v244, v[158:159] offset:3584\n"
"v_mfma_f32_16x16x32_bf16 v[168:171], a[216:219], v[116:119], v[168:171]\n"
"buffer_load_dwordx4 v[64:67], v248, s[28:31], s46 offen offset:0 sc1\n"
"v_mfma_f32_16x16x32_bf16 v[168:171], a[232:235], v[120:123], v[168:171]\n"
"ds_write_b64 v244, v[160:161] offset:4096\n"
"v_mfma_f32_16x16x32_bf16 v[168:171], a[248:251], v[124:127], v[168:171]\n"
"v_mfma_f32_16x16x32_bf16 v[172:175], a[204:207], v[112:115], v[172:175]\n"
"ds_write_b64 v244, v[162:163] offset:4608\n"
"buffer_load_dwordx4 v[68:71], v248, s[28:31], s46 offen offset:1024 sc1\n"
"v_mfma_f32_16x16x32_bf16 v[172:175], a[220:223], v[116:119], v[172:175]\n"
"v_mfma_f32_16x16x32_bf16 v[172:175], a[236:239], v[120:123], v[172:175]\n"
"ds_write_b64 v244, v[164:165] offset:5120\n"
"v_mfma_f32_16x16x32_bf16 v[172:175], a[252:255], v[124:127], v[172:175]\n"
"buffer_load_dwordx4 v[72:75], v248, s[28:31], s46 offen offset:2048 sc1\n"
"v_mfma_f32_16x16x32_bf16 v[128:131], a[64:67], v[96:99], 0\n"
"ds_write_b64 v244, v[166:167] offset:5632\n"
"v_mfma_f32_16x16x32_bf16 v[128:131], a[80:83], v[100:103], v[128:131]\n"
"v_mfma_f32_16x16x32_bf16 v[128:131], a[96:99], v[104:107], v[128:131]\n"
"ds_write_b64 v244, v[168:169] offset:6144\n"
"buffer_load_dwordx4 v[76:79], v248, s[28:31], s46 offen offset:3072 sc1\n"
"v_mfma_f32_16x16x32_bf16 v[128:131], a[112:115], v[108:111], v[128:131]\n"
"v_mfma_f32_16x16x32_bf16 v[128:131], a[192:195], v[192:195], v[128:131]\n"
"ds_write_b64 v244, v[170:171] offset:6656\n"
"v_mfma_f32_16x16x32_bf16 v[128:131], a[208:211], v[196:199], v[128:131]\n"
"v_mfma_f32_16x16x32_bf16 v[128:131], a[224:227], v[200:203], v[128:131]\n"
"ds_write_b64 v244, v[172:173] offset:7168\n"
"v_mfma_f32_16x16x32_bf16 v[128:131], a[240:243], v[204:207], v[128:131]\n"
"v_mfma_f32_16x16x32_bf16 v[132:135], a[68:71], v[96:99], 0\n"
"ds_write_b64 v244, v[174:175] offset:7680\n"
"v_mfma_f32_16x16x32_bf16 v[132:135], a[84:87], v[100:103], v[132:135]\n"
"v_mfma_f32_16x16x32_bf16 v[132:135], a[100:103], v[104:107], v[132:135]\n"
"v_mfma_f32_16x16x32_bf16 v[132:135], a[116:119], v[108:111], v[132:135]\n"
"v_xor_b32 v244, 65536, v244\n"
"v_mfma_f32_16x16x32_bf16 v[132:135], a[196:199], v[192:195], v[132:135]\n"
"s_waitcnt lgkmcnt(0)\n"
"s_barrier\n"
"v_mfma_f32_16x16x32_bf16 v[132:135], a[212:215], v[196:199], v[132:135]\n"
"ds_read2st64_b64 v[208:211], v245 offset0:0 offset1:1\n"
"v_mfma_f32_16x16x32_bf16 v[132:135], a[228:231], v[200:203], v[132:135]\n"
"v_mfma_f32_16x16x32_bf16 v[132:135], a[244:247], v[204:207], v[132:135]\n"
"ds_read2st64_b64 v[212:215], v246 offset0:8 offset1:9\n"
"v_mfma_f32_16x16x32_bf16 v[136:139], a[72:75], v[96:99], 0\n"
"v_mfma_f32_16x16x32_bf16 v[136:139], a[88:91], v[100:103], v[136:139]\n"
"ds_read2st64_b64 v[216:219], v247 offset0:64 offset1:65\n"
"v_mfma_f32_16x16x32_bf16 v[136:139], a[104:107], v[104:107], v[136:139]\n"
"v_mfma_f32_16x16x32_bf16 v[136:139], a[120:123], v[108:111], v[136:139]\n"
"ds_read2st64_b64 v[220:223], v245 offset0:2 offset1:3\n"
"s_waitcnt lgkmcnt(3)\n"
"v_add_f32 v128, v128, v208\n"
"v_add_f32 v129, v129, v209\n"
"v_mfma_f32_16x16x32_bf16 v[136:139], a[200:203], v[192:195], v[136:139]\n"
"v_add_f32 v130, v130, v210\n"
"v_add_f32 v131, v131, v211\n"
"v_mfma_f32_16x16x32_bf16 v[136:139], a[216:219], v[196:199], v[136:139]\n"
"ds_read2st64_b64 v[208:211], v246 offset0:10 offset1:11\n"
"s_waitcnt lgkmcnt(3)\n"
"v_add_f32 v128, v128, v212\n"
"v_add_f32 v129, v129, v213\n"
"v_mfma_f32_16x16x32_bf16 v[136:139], a[232:235], v[200:203], v[136:139]\n"
"v_add_f32 v130, v130, v214\n"
"v_add_f32 v131, v131, v215\n"
"v_mfma_f32_16x16x32_bf16 v[136:139], a[248:251], v[204:207], v[136:139]\n"
"ds_read2st64_b64 v[212:215], v247 offset0:66 offset1:67\n"
"s_waitcnt lgkmcnt(3)\n"
"v_add_f32 v128, v128, v216\n"
"v_add_f32 v129, v129, v217\n"
"v_mfma_f32_16x16x32_bf16 v[140:143], a[76:79], v[96:99], 0\n"
"v_add_f32 v130, v130, v218\n"
"v_add_f32 v131, v131, v219\n"
"v_mfma_f32_16x16x32_bf16 v[140:143], a[92:95], v[100:103], v[140:143]\n"
"ds_read2st64_b64 v[216:219], v245 offset0:4 offset1:5\n"
"v_and_or_b32 v128, v128, s42, v224\n"
"s_waitcnt lgkmcnt(3)\n"
"v_add_f32 v132, v132, v220\n"
"v_add_f32 v133, v133, v221\n"
"v_mfma_f32_16x16x32_bf16 v[140:143], a[108:111], v[104:107], v[140:143]\n"
"v_and_or_b32 v129, v129, s42, v225\n"
"v_add_f32 v134, v134, v222\n"
"v_add_f32 v135, v135, v223\n"
"v_mfma_f32_16x16x32_bf16 v[140:143], a[124:127], v[108:111], v[140:143]\n"
"ds_read2st64_b64 v[220:223], v246 offset0:12 offset1:13\n"
"v_and_or_b32 v130, v130, s42, v226\n"
"s_waitcnt lgkmcnt(3)\n"
"v_add_f32 v132, v132, v208\n"
"v_add_f32 v133, v133, v209\n"
"v_mfma_f32_16x16x32_bf16 v[140:143], a[204:207], v[192:195], v[140:143]\n"
"v_and_or_b32 v131, v131, s42, v227\n"
"v_add_f32 v134, v134, v210\n"
"v_add_f32 v135, v135, v211\n"
"v_mfma_f32_16x16x32_bf16 v[140:143], a[220:223], v[196:199], v[140:143]\n"
"buffer_store_dwordx4 v[128:131], v248, s[28:31], s44 offen offset:0 sc1\n"
"ds_read2st64_b64 v[208:211], v247 offset0:68 offset1:69\n"
"s_waitcnt lgkmcnt(3)\n"
"v_add_f32 v132, v132, v212\n"
"v_add_f32 v133, v133, v213\n"
"v_mfma_f32_16x16x32_bf16 v[140:143], a[236:239], v[200:203], v[140:143]\n"
"v_add_f32 v134, v134, v214\n"
"v_add_f32 v135, v135, v215\n"
"v_mfma_f32_16x16x32_bf16 v[140:143], a[252:255], v[204:207], v[140:143]\n"
"s_waitcnt vmcnt(21)\n"
"ds_read2st64_b64 v[212:215], v245 offset0:6 offset1:7\n"
"v_and_or_b32 v132, v132, s42, v224\n"
"s_waitcnt lgkmcnt(3)\n"
"v_add_f32 v136, v136, v216\n"
"v_add_f32 v137, v137, v217\n"
"v_mfma_f32_16x16x32_bf16 v[176:179], a[0:3], v[0:3], 0\n"
"v_and_or_b32 v133, v133, s42, v225\n"
"v_add_f32 v138, v138, v218\n"
"v_add_f32 v139, v139, v219\n"
"v_mfma_f32_16x16x32_bf16 v[176:179], a[16:19], v[4:7], v[176:179]\n"
"ds_read2st64_b64 v[216:219], v246 offset0:14 offset1:15\n"
"v_and_or_b32 v134, v134, s42, v226\n"
"s_waitcnt lgkmcnt(3)\n"
"v_add_f32 v136, v136, v220\n"
"v_add_f32 v137, v137, v221\n"
"v_mfma_f32_16x16x32_bf16 v[176:179], a[32:35], v[8:11], v[176:179]\n"
"v_and_or_b32 v135, v135, s42, v227\n"
"v_add_f32 v138, v138, v222\n"
"v_add_f32 v139, v139, v223\n"
"v_mfma_f32_16x16x32_bf16 v[176:179], a[48:51], v[12:15], v[176:179]\n"
"buffer_store_dwordx4 v[132:135], v248, s[28:31], s44 offen offset:1024 sc1\n"
"ds_read2st64_b64 v[220:223], v247 offset0:70 offset1:71\n"
"s_waitcnt lgkmcnt(3)\n"
"v_add_f32 v136, v136, v208\n"
"v_add_f32 v137, v137, v209\n"
"v_mfma_f32_16x16x32_bf16 v[180:183], a[4:7], v[0:3], 0\n"
"v_add_f32 v138, v138, v210\n"
"v_add_f32 v139, v139, v211\n"
"v_mfma_f32_16x16x32_bf16 v[180:183], a[20:23], v[4:7], v[180:183]\n"
"v_and_or_b32 v136, v136, s42, v224\n"
"s_waitcnt lgkmcnt(2)\n"
"v_add_f32 v140, v140, v212\n"
"v_add_f32 v141, v141, v213\n"
"v_mfma_f32_16x16x32_bf16 v[180:183], a[36:39], v[8:11], v[180:183]\n"
"v_and_or_b32 v137, v137, s42, v225\n"
"v_add_f32 v142, v142, v214\n"
"v_add_f32 v143, v143, v215\n"
"v_mfma_f32_16x16x32_bf16 v[180:183], a[52:55], v[12:15], v[180:183]\n"
"v_and_or_b32 v138, v138, s42, v226\n"
"s_waitcnt lgkmcnt(1)\n"
"v_add_f32 v140, v140, v216\n"
"v_add_f32 v141, v141, v217\n"
"v_mfma_f32_16x16x32_bf16 v[184:187], a[8:11], v[0:3], 0\n"
"v_and_or_b32 v139, v139, s42, v227\n"
"v_add_f32 v142, v142, v218\n"
"v_add_f32 v143, v143, v219\n"
"v_mfma_f32_16x16x32_bf16 v[184:187], a[24:27], v[4:7], v[184:187]\n"
"buffer_store_dwordx4 v[136:139], v248, s[28:31], s44 offen offset:2048 sc1\n"
"s_waitcnt lgkmcnt(0)\n"
"v_add_f32 v140, v140, v220\n"
"v_add_f32 v141, v141, v221\n"
"v_mfma_f32_16x16x32_bf16 v[184:187], a[40:43], v[8:11], v[184:187]\n"
"v_add_f32 v142, v142, v222\n"
"v_add_f32 v143, v143, v223\n"
"v_mfma_f32_16x16x32_bf16 v[184:187], a[56:59], v[12:15], v[184:187]\n"
"v_and_or_b32 v140, v140, s42, v224\n"
"v_and_or_b32 v141, v141, s42, v225\n"
"v_mfma_f32_16x16x32_bf16 v[188:191], a[12:15], v[0:3], 0\n"
"v_and_or_b32 v142, v142, s42, v226\n"
"v_and_or_b32 v143, v143, s42, v227\n"
"v_mfma_f32_16x16x32_bf16 v[188:191], a[28:31], v[4:7], v[188:191]\n"
"buffer_store_dwordx4 v[140:143], v248, s[28:31], s44 offen offset:3072 sc1\n"
"v_mfma_f32_16x16x32_bf16 v[188:191], a[44:47], v[8:11], v[188:191]\n"
"v_mfma_f32_16x16x32_bf16 v[188:191], a[60:63], v[12:15], v[188:191]\n"
"s_waitcnt vmcnt(20)\n"
"v_xor_b32 v245, 65536, v245\n"
"v_mfma_f32_16x16x32_bf16 v[144:147], a[0:3], v[16:19], 0\n"
"v_xor_b32 v246, 65536, v246\n"
"v_mfma_f32_16x16x32_bf16 v[144:147], a[16:19], v[20:23], v[144:147]\n"
"v_xor_b32 v247, 65536, v247\n"
"v_mfma_f32_16x16x32_bf16 v[144:147], a[32:35], v[24:27], v[144:147]\n"
"v_mfma_f32_16x16x32_bf16 v[144:147], a[48:51], v[28:31], v[144:147]\n"
"v_mfma_f32_16x16x32_bf16 v[148:151], a[4:7], v[16:19], 0\n"
"v_mfma_f32_16x16x32_bf16 v[148:151], a[20:23], v[20:23], v[148:151]\n"
"v_mfma_f32_16x16x32_bf16 v[148:151], a[36:39], v[24:27], v[148:151]\n"
"v_mfma_f32_16x16x32_bf16 v[148:151], a[52:55], v[28:31], v[148:151]\n"
"v_mfma_f32_16x16x32_bf16 v[152:155], a[8:11], v[16:19], 0\n"
"v_mfma_f32_16x16x32_bf16 v[152:155], a[24:27], v[20:23], v[152:155]\n"
"v_mfma_f32_16x16x32_bf16 v[152:155], a[40:43], v[24:27], v[152:155]\n"
"v_mfma_f32_16x16x32_bf16 v[152:155], a[56:59], v[28:31], v[152:155]\n"
"v_mfma_f32_16x16x32_bf16 v[156:159], a[12:15], v[16:19], 0\n"
"v_mfma_f32_16x16x32_bf16 v[156:159], a[28:31], v[20:23], v[156:159]\n"
"v_mfma_f32_16x16x32_bf16 v[156:159], a[44:47], v[24:27], v[156:159]\n"
"v_mfma_f32_16x16x32_bf16 v[156:159], a[60:63], v[28:31], v[156:159]\n"
"v_mfma_f32_16x16x32_bf16 v[160:163], a[64:67], v[0:3], 0\n"
"v_mfma_f32_16x16x32_bf16 v[160:163], a[80:83], v[4:7], v[160:163]\n"
"v_mfma_f32_16x16x32_bf16 v[160:163], a[96:99], v[8:11], v[160:163]\n"
"v_mfma_f32_16x16x32_bf16 v[160:163], a[112:115], v[12:15], v[160:163]\n"
"v_mfma_f32_16x16x32_bf16 v[164:167], a[68:71], v[0:3], 0\n"
"v_mfma_f32_16x16x32_bf16 v[164:167], a[84:87], v[4:7], v[164:167]\n"
"v_mfma_f32_16x16x32_bf16 v[164:167], a[100:103], v[8:11], v[164:167]\n"
"v_mfma_f32_16x16x32_bf16 v[164:167], a[116:119], v[12:15], v[164:167]\n"
"v_mfma_f32_16x16x32_bf16 v[168:171], a[72:75], v[0:3], 0\n"
"v_mfma_f32_16x16x32_bf16 v[168:171], a[88:91], v[4:7], v[168:171]\n"
"v_mfma_f32_16x16x32_bf16 v[168:171], a[104:107], v[8:11], v[168:171]\n"
"v_mfma_f32_16x16x32_bf16 v[168:171], a[120:123], v[12:15], v[168:171]\n"
"v_mfma_f32_16x16x32_bf16 v[172:175], a[76:79], v[0:3], 0\n"
"v_mfma_f32_16x16x32_bf16 v[172:175], a[92:95], v[4:7], v[172:175]\n"
"v_mfma_f32_16x16x32_bf16 v[172:175], a[108:111], v[8:11], v[172:175]\n"
"v_mfma_f32_16x16x32_bf16 v[172:175], a[124:127], v[12:15], v[172:175]\n"
"s_waitcnt vmcnt(16)\n"
"v_mfma_f32_16x16x32_bf16 v[176:179], a[128:131], v[48:51], v[176:179]\n"
"v_mfma_f32_16x16x32_bf16 v[176:179], a[144:147], v[52:55], v[176:179]\n"
"v_mfma_f32_16x16x32_bf16 v[176:179], a[160:163], v[56:59], v[176:179]\n"
"v_mfma_f32_16x16x32_bf16 v[176:179], a[176:179], v[60:63], v[176:179]\n"
"v_mfma_f32_16x16x32_bf16 v[180:183], a[132:135], v[48:51], v[180:183]\n"
"v_mfma_f32_16x16x32_bf16 v[180:183], a[148:151], v[52:55], v[180:183]\n"
"v_mfma_f32_16x16x32_bf16 v[180:183], a[164:167], v[56:59], v[180:183]\n"
"v_mfma_f32_16x16x32_bf16 v[180:183], a[180:183], v[60:63], v[180:183]\n"
"v_mfma_f32_16x16x32_bf16 v[184:187], a[136:139], v[48:51], v[184:187]\n"
"s_waitcnt vmcnt(4)\n"
"buffer_load_dwordx4 v[80:83], v248, s[28:31], s57 offen offset:0 sc1\n"
"v_mfma_f32_16x16x32_bf16 v[184:187], a[152:155], v[52:55], v[184:187]\n"
"v_xor_b32 v64, v64, v224\n"
"v_xor_b32 v65, v65, v225\n"
"v_mfma_f32_16x16x32_bf16 v[184:187], a[168:171], v[56:59], v[184:187]\n"
"ds_write_b64 v244, v[176:177] offset:32768\n"
"v_or3_b32 v250, v64, v65, v64\n"
"v_mfma_f32_16x16x32_bf16 v[184:187], a[184:187], v[60:63], v[184:187]\n"
"buffer_load_dwordx4 v[84:87], v248, s[28:31], s57 offen offset:1024 sc1\n"
"v_mfma_f32_16x16x32_bf16 v[188:191], a[140:143], v[48:51], v[188:191]\n"
"ds_write_b64 v244, v[178:179] offset:33280\n"
"v_xor_b32 v66, v66, v226\n"
"v_mfma_f32_16x16x32_bf16 v[188:191], a[156:159], v[52:55], v[188:191]\n"
"v_xor_b32 v67, v67, v227\n"
"v_or3_b32 v250, v66, v67, v250\n"
"v_mfma_f32_16x16x32_bf16 v[188:191], a[172:175], v[56:59], v[188:191]\n"
"ds_write_b64 v244, v[180:181] offset:33792\n"
"buffer_load_dwordx4 v[88:91], v248, s[28:31], s57 offen offset:2048 sc1\n"
"v_mfma_f32_16x16x32_bf16 v[188:191], a[188:191], v[60:63], v[188:191]\n"
"v_xor_b32 v68, v68, v224\n"
"v_xor_b32 v69, v69, v225\n"
"v_mfma_f32_16x16x32_bf16 v[144:147], a[128:131], v[32:35], v[144:147]\n"
"ds_write_b64 v244, v[182:183] offset:34304\n"
"v_or3_b32 v250, v68, v69, v250\n"
"v_mfma_f32_16x16x32_bf16 v[144:147], a[144:147], v[36:39], v[144:147]\n"
"buffer_load_dwordx4 v[92:95], v248, s[28:31], s57 offen offset:3072 sc1\n"
"v_mfma_f32_16x16x32_bf16 v[144:147], a[160:163], v[40:43], v[144:147]\n"
"ds_write_b64 v244, v[184:185] offset:34816\n"
"v_xor_b32 v70, v70, v226\n"
"v_mfma_f32_16x16x32_bf16 v[144:147], a[176:179], v[44:47], v[144:147]\n"
"v_xor_b32 v71, v71, v227\n"
"v_or3_b32 v250, v70, v71, v250\n"
"v_mfma_f32_16x16x32_bf16 v[148:151], a[132:135], v[32:35], v[148:151]\n"
"ds_write_b64 v244, v[186:187] offset:35328\n"
"v_xor_b32 v72, v72, v224\n"
"v_mfma_f32_16x16x32_bf16 v[148:151], a[148:151], v[36:39], v[148:151]\n"
"v_xor_b32 v73, v73, v225\n"
"v_or3_b32 v250, v72, v73, v250\n"
"v_mfma_f32_16x16x32_bf16 v[148:151], a[164:167], v[40:43], v[148:151]\n"
"ds_write_b64 v244, v[188:189] offset:35840\n"
"v_xor_b32 v74, v74, v226\n"
"v_mfma_f32_16x16x32_bf16 v[148:151], a[180:183], v[44:47], v[148:151]\n"
"v_xor_b32 v75, v75, v227\n"
"v_or3_b32 v250, v74, v75, v250\n"
"v_mfma_f32_16x16x32_bf16 v[152:155], a[136:139], v[32:35], v[152:155]\n"
"ds_write_b64 v244, v[190:191] offset:36352\n"
"v_xor_b32 v76, v76, v224\n"
"v_mfma_f32_16x16x32_bf16 v[152:155], a[152:155], v[36:39], v[152:155]\n"
"v_xor_b32 v77, v77, v225\n"
"v_or3_b32 v250, v76, v77, v250\n"
"v_mfma_f32_16x16x32_bf16 v[152:155], a[168:171], v[40:43], v[152:155]\n"
"ds_write_b64 v244, v[144:145] offset:0\n"
"v_xor_b32 v78, v78, v226\n"
"v_mfma_f32_16x16x32_bf16 v[152:155], a[184:187], v[44:47], v[152:155]\n"
"v_xor_b32 v79, v79, v227\n"
"v_or3_b32 v250, v78, v79, v250\n"
"v_mfma_f32_16x16x32_bf16 v[156:159], a[140:143], v[32:35], v[156:159]\n"
"ds_write_b64 v244, v[146:147] offset:512\n"
"v_lshlrev_b32 v250, 24, v250\n"
"v_mfma_f32_16x16x32_bf16 v[156:159], a[156:159], v[36:39], v[156:159]\n"
"v_cmp_ne_u32 vcc, 0, v250\n"
"v_mfma_f32_16x16x32_bf16 v[156:159], a[172:175], v[40:43], v[156:159]\n"
"ds_write_b64 v244, v[148:149] offset:1024\n"
"v_mfma_f32_16x16x32_bf16 v[156:159], a[188:191], v[44:47], v[156:159]\n"
"s_cbranch_vccnz .LslowA\n"
".LbackA:\n"
"v_mfma_f32_16x16x32_bf16 v[160:163], a[192:195], v[48:51], v[160:163]\n"
"ds_write_b64 v244, v[150:151] offset:1536\n"
"v_mfma_f32_16x16x32_bf16 v[160:163], a[208:211], v[52:55], v[160:163]\n"
"buffer_load_dwordx4 v[112:115], v248, s[28:31], s59 offen offset:0 sc1\n"
"v_mfma_f32_16x16x32_bf16 v[160:163], a[224:227], v[56:59], v[160:163]\n"
"ds_write_b64 v244, v[152:153] offset:2048\n"
"v_mfma_f32_16x16x32_bf16 v[160:163], a[240:243], v[60:63], v[160:163]\n"
"v_mfma_f32_16x16x32_bf16 v[164:167], a[196:199], v[48:51], v[164:167]\n"
"ds_write_b64 v244, v[154:155] offset:2560\n"
"buffer_load_dwordx4 v[116:119], v248, s[28:31], s59 offen offset:1024 sc1\n"
"v_mfma_f32_16x16x32_bf16 v[164:167], a[212:215], v[52:55], v[164:167]\n"
"v_mfma_f32_16x16x32_bf16 v[164:167], a[228:231], v[56:59], v[164:167]\n"
"ds_write_b64 v244, v[156:157] offset:3072\n"
"v_mfma_f32_16x16x32_bf16 v[164:167], a[244:247], v[60:63], v[164:167]\n"
"buffer_load_dwordx4 v[120:123], v248, s[28:31], s59 offen offset:2048 sc1\n"
"v_mfma_f32_16x16x32_bf16 v[168:171], a[200:203], v[48:51], v[168:171]\n"
"ds_write_b64 v244, v[158:159] offset:3584\n"
"v_mfma_f32_16x16x32_bf16 v[168:171], a[216:219], v[52:55], v[168:171]\n"
"v_mfma_f32_16x16x32_bf16 v[168:171], a[232:235], v[56:59], v[168:171]\n"
"ds_write_b64 v244, v[160:161] offset:4096\n"
"buffer_load_dwordx4 v[124:127], v248, s[28:31], s59 offen offset:3072 sc1\n"
"v_mfma_f32_16x16x32_bf16 v[168:171], a[248:251], v[60:63], v[168:171]\n"
"v_mfma_f32_16x16x32_bf16 v[172:175], a[204:207], v[48:51], v[172:175]\n"
"ds_write_b64 v244, v[162:163] offset:4608\n"
"v_mfma_f32_16x16x32_bf16 v[172:175], a[220:223], v[52:55], v[172:175]\n"
"v_mfma_f32_16x16x32_bf16 v[172:175], a[236:239], v[56:59], v[172:175]\n"
"ds_write_b64 v244, v[164:165] offset:5120\n"
"v_mfma_f32_16x16x32_bf16 v[172:175], a[252:255], v[60:63], v[172:175]\n"
"v_mfma_f32_16x16x32_bf16 v[128:131], a[64:67], v[16:19], v[64:67]\n"
"ds_write_b64 v244, v[166:167] offset:5632\n"
"v_mfma_f32_16x16x32_bf16 v[128:131], a[80:83], v[20:23], v[128:131]\n"
"v_mfma_f32_16x16x32_bf16 v[128:131], a[96:99], v[24:27], v[128:131]\n"
"ds_write_b64 v244, v[168:169] offset:6144\n"
"v_mfma_f32_16x16x32_bf16 v[128:131], a[112:115], v[28:31], v[128:131]\n"
"v_mfma_f32_16x16x32_bf16 v[128:131], a[192:195], v[32:35], v[128:131]\n"
"ds_write_b64 v244, v[170:171] offset:6656\n"
"v_mfma_f32_16x16x32_bf16 v[128:131], a[208:211], v[36:39], v[128:131]\n"
"v_mfma_f32_16x16x32_bf16 v[128:131], a[224:227], v[40:43], v[128:131]\n"
"ds_write_b64 v244, v[172:173] offset:7168\n"
"v_mfma_f32_16x16x32_bf16 v[128:131], a[240:243], v[44:47], v[128:131]\n"
"v_mfma_f32_16x16x32_bf16 v[132:135], a[68:71], v[16:19], v[68:71]\n"
"ds_write_b64 v244, v[174:175] offset:7680\n"
"v_mfma_f32_16x16x32_bf16 v[132:135], a[84:87], v[20:23], v[132:135]\n"
"v_mfma_f32_16x16x32_bf16 v[132:135], a[100:103], v[24:27], v[132:135]\n"
"v_mfma_f32_16x16x32_bf16 v[132:135], a[116:119], v[28:31], v[132:135]\n"
"v_mfma_f32_16x16x32_bf16 v[132:135], a[196:199], v[32:35], v[132:135]\n"
"s_waitcnt lgkmcnt(0)\n"
"s_barrier\n"
"v_mfma_f32_16x16x32_bf16 v[132:135], a[212:215], v[36:39], v[132:135]\n"
"ds_read2st64_b64 v[188:191], v247 offset0:70 offset1:71\n"
"v_mfma_f32_16x16x32_bf16 v[132:135], a[228:231], v[40:43], v[132:135]\n"
"ds_read2st64_b64 v[172:175], v246 offset0:14 offset1:15\n"
"v_mfma_f32_16x16x32_bf16 v[132:135], a[244:247], v[44:47], v[132:135]\n"
"ds_read2st64_b64 v[156:159], v245 offset0:6 offset1:7\n"
"v_mfma_f32_16x16x32_bf16 v[136:139], a[72:75], v[16:19], v[72:75]\n"
"ds_read2st64_b64 v[184:187], v247 offset0:68 offset1:69\n"
"v_mfma_f32_16x16x32_bf16 v[136:139], a[88:91], v[20:23], v[136:139]\n"
"ds_read2st64_b64 v[168:171], v246 offset0:12 offset1:13\n"
"v_mfma_f32_16x16x32_bf16 v[136:139], a[104:107], v[24:27], v[136:139]\n"
"ds_read2st64_b64 v[152:155], v245 offset0:4 offset1:5\n"
"v_mfma_f32_16x16x32_bf16 v[136:139], a[120:123], v[28:31], v[136:139]\n"
"ds_read2st64_b64 v[180:183], v247 offset0:66 offset1:67\n"
"s_waitcnt lgkmcnt(5)\n"
"v_add_f32 v188, v188, v172\n"
"v_add_f32 v189, v189, v173\n"
"v_mfma_f32_16x16x32_bf16 v[136:139], a[200:203], v[32:35], v[136:139]\n"
"ds_read2st64_b64 v[164:167], v246 offset0:10 offset1:11\n"
"v_add_f32 v190, v190, v174\n"
"v_add_f32 v191, v191, v175\n"
"v_mfma_f32_16x16x32_bf16 v[136:139], a[216:219], v[36:39], v[136:139]\n"
"ds_read2st64_b64 v[148:151], v245 offset0:2 offset1:3\n"
"s_waitcnt lgkmcnt(6)\n"
"v_add_f32 v188, v188, v156\n"
"v_add_f32 v189, v189, v157\n"
"v_mfma_f32_16x16x32_bf16 v[136:139], a[232:235], v[40:43], v[136:139]\n"
"ds_read2st64_b64 v[176:179], v247 offset0:64 offset1:65\n"
"v_add_f32 v190, v190, v158\n"
"v_add_f32 v191, v191, v159\n"
"v_mfma_f32_16x16x32_bf16 v[136:139], a[248:251], v[44:47], v[136:139]\n"
"ds_read2st64_b64 v[160:163], v246 offset0:8 offset1:9\n"
"s_waitcnt lgkmcnt(6)\n"
"v_add_f32 v184, v184, v168\n"
"v_add_f32 v185, v185, v169\n"
"v_mfma_f32_16x16x32_bf16 v[140:143], a[76:79], v[16:19], v[76:79]\n"
"ds_read2st64_b64 v[144:147], v245 offset0:0 offset1:1\n"
"v_add_f32 v186, v186, v170\n"
"v_add_f32 v187, v187, v171\n"
"v_mfma_f32_16x16x32_bf16 v[140:143], a[92:95], v[20:23], v[140:143]\n"
"s_waitcnt lgkmcnt(6)\n"
"v_add_f32 v184, v184, v152\n"
"v_add_f32 v185, v185, v153\n"
"v_mfma_f32_16x16x32_bf16 v[140:143], a[108:111], v[24:27], v[140:143]\n"
"v_add_f32 v186, v186, v154\n"
"v_add_f32 v187, v187, v155\n"
"v_mfma_f32_16x16x32_bf16 v[140:143], a[124:127], v[28:31], v[140:143]\n"
"s_waitcnt lgkmcnt(4)\n"
"v_add_f32 v180, v180, v164\n"
"v_add_f32 v181, v181, v165\n"
"v_mfma_f32_16x16x32_bf16 v[140:143], a[204:207], v[32:35], v[140:143]\n"
"v_add_f32 v182, v182, v166\n"
"v_add_f32 v183, v183, v167\n"
"v_mfma_f32_16x16x32_bf16 v[140:143], a[220:223], v[36:39], v[140:143]\n"
"s_waitcnt lgkmcnt(3)\n"
"v_add_f32 v180, v180, v148\n"
"v_add_f32 v181, v181, v149\n"
"v_mfma_f32_16x16x32_bf16 v[140:143], a[236:239], v[40:43], v[140:143]\n"
"v_add_f32 v182, v182, v150\n"
"v_add_f32 v183, v183, v151\n"
"v_mfma_f32_16x16x32_bf16 v[140:143], a[252:255], v[44:47], v[140:143]\n"
"s_waitcnt lgkmcnt(1)\n"
"v_add_f32 v176, v176, v160\n"
"v_add_f32 v177, v177, v161\n"
"v_pk_add_f32 v[178:179], v[178:179], v[162:163]\n"
"s_waitcnt lgkmcnt(0)\n"
"v_pk_add_f32 v[176:177], v[176:177], v[144:145]\n"
"v_pk_add_f32 v[178:179], v[178:179], v[146:147]\n"
"v_pk_add_f32 v[128:129], v[128:129], v[176:177]\n"
"v_pk_add_f32 v[130:131], v[130:131], v[178:179]\n"
"v_pk_add_f32 v[132:133], v[132:133], v[180:181]\n"
"v_pk_add_f32 v[134:135], v[134:135], v[182:183]\n"
"v_pk_add_f32 v[136:137], v[136:137], v[184:185]\n"
"v_pk_add_f32 v[138:139], v[138:139], v[186:187]\n"
"v_pk_add_f32 v[140:141], v[140:141], v[188:189]\n"
"v_pk_add_f32 v[142:143], v[142:143], v[190:191]\n"
"s_waitcnt vmcnt(4)\n"
"v_xor_b32 v80, v80, v224\n"
"v_xor_b32 v81, v81, v225\n"
"v_or3_b32 v250, v80, v81, v80\n"
"v_xor_b32 v82, v82, v226\n"
"v_xor_b32 v83, v83, v227\n"
"v_or3_b32 v250, v82, v83, v250\n"
"v_xor_b32 v84, v84, v224\n"
"v_xor_b32 v85, v85, v225\n"
"v_or3_b32 v250, v84, v85, v250\n"
"v_xor_b32 v86, v86, v226\n"
"v_xor_b32 v87, v87, v227\n"
"v_or3_b32 v250, v86, v87, v250\n"
"v_xor_b32 v88, v88, v224\n"
"v_xor_b32 v89, v89, v225\n"
"v_or3_b32 v250, v88, v89, v250\n"
"v_xor_b32 v90, v90, v226\n"
"v_xor_b32 v91, v91, v227\n"
"v_or3_b32 v250, v90, v91, v250\n"
"v_xor_b32 v92, v92, v224\n"
"v_xor_b32 v93, v93, v225\n"
"v_or3_b32 v250, v92, v93, v250\n"
"v_xor_b32 v94, v94, v226\n"
"v_xor_b32 v95, v95, v227\n"
"v_or3_b32 v250, v94, v95, v250\n"
"v_lshlrev_b32 v250, 24, v250\n"
"v_cmp_ne_u32 vcc, 0, v250\n"
"s_nop 1\n"
"s_cbranch_vccnz .LslowB\n"
".LbackB:\n"
"v_pk_add_f32 v[128:129], v[128:129], v[80:81]\n"
"v_pk_add_f32 v[130:131], v[130:131], v[82:83]\n"
"v_pk_add_f32 v[132:133], v[132:133], v[84:85]\n"
"v_pk_add_f32 v[134:135], v[134:135], v[86:87]\n"
"v_pk_add_f32 v[136:137], v[136:137], v[88:89]\n"
"v_pk_add_f32 v[138:139], v[138:139], v[90:91]\n"
"v_pk_add_f32 v[140:141], v[140:141], v[92:93]\n"
"v_pk_add_f32 v[142:143], v[142:143], v[94:95]\n"
"s_waitcnt vmcnt(0)\n"
"v_xor_b32 v112, v112, v224\n"
"v_xor_b32 v113, v113, v225\n"
"v_or3_b32 v250, v112, v113, v112\n"
"v_xor_b32 v114, v114, v226\n"
"v_xor_b32 v115, v115, v227\n"
"v_or3_b32 v250, v114, v115, v250\n"
"v_xor_b32 v116, v116, v224\n"
"v_xor_b32 v117, v117, v225\n"
"v_or3_b32 v250, v116, v117, v250\n"
"v_xor_b32 v118, v118, v226\n"
"v_xor_b32 v119, v119, v227\n"
"v_or3_b32 v250, v118, v119, v250\n"
"v_xor_b32 v120, v120, v224\n"
"v_xor_b32 v121, v121, v225\n"
"v_or3_b32 v250, v120, v121, v250\n"
"v_xor_b32 v122, v122, v226\n"
"v_xor_b32 v123, v123, v227\n"
"v_or3_b32 v250, v122, v123, v250\n"
"v_xor_b32 v124, v124, v224\n"
"v_xor_b32 v125, v125, v225\n"
"v_or3_b32 v250, v124, v125, v250\n"
"v_xor_b32 v126, v126, v226\n"
"v_xor_b32 v127, v127, v227\n"
"v_or3_b32 v250, v126, v127, v250\n"
"v_lshlrev_b32 v250, 24, v250\n"
"v_cmp_ne_u32 vcc, 0, v250\n"
"s_nop 1\n"
"s_cbranch_vccnz .LslowC\n"
".LbackC:\n"
"v_pk_add_f32 v[128:129], v[128:129], v[112:113]\n"
"v_pk_add_f32 v[130:131], v[130:131], v[114:115]\n"
"v_cvt_pk_bf16_f32 v128, v128, v129\n"
"v_cvt_pk_bf16_f32 v129, v130, v131\n"
"v_pk_add_f32 v[132:133], v[132:133], v[116:117]\n"
"v_pk_add_f32 v[134:135], v[134:135], v[118:119]\n"
"v_cvt_pk_bf16_f32 v130, v132, v133\n"
"v_cvt_pk_bf16_f32 v131, v134, v135\n"
"s_nop 0\n"
"v_permlane16_swap_b32 v128, v130\n"
"v_permlane16_swap_b32 v129, v131\n"
"buffer_store_dwordx4 v[128:131], v241, s[24:27], 0 offen offset:0 sc1\n"
"v_pk_add_f32 v[136:137], v[136:137], v[120:121]\n"
"v_pk_add_f32 v[138:139], v[138:139], v[122:123]\n"
"v_cvt_pk_bf16_f32 v136, v136, v137\n"
"v_cvt_pk_bf16_f32 v137, v138, v139\n"
"v_pk_add_f32 v[140:141], v[140:141], v[124:125]\n"
"v_pk_add_f32 v[142:143], v[142:143], v[126:127]\n"
"v_cvt_pk_bf16_f32 v138, v140, v141\n"
"v_cvt_pk_bf16_f32 v139, v142, v143\n"
"s_nop 0\n"
"v_permlane16_swap_b32 v136, v138\n"
"v_permlane16_swap_b32 v137, v139\n"
"buffer_store_dwordx4 v[136:139], v241, s[24:27], 0 offen offset:64 sc1\n"
"s_branch .Lend\n"
".Lfin:\n"
"s_waitcnt vmcnt(0)\n"
"s_branch .Lend\n"
".Lidle:\n"
"s_waitcnt lgkmcnt(0)\n"
"s_cmp_lg_u32 %2, 255\n"
"s_cbranch_scc1 .Lfin\n"
"s_lshl_b32 s52, s40, 13\n"
"v_lshl_add_u32 v240, v248, 3, s52\n"
"s_mov_b32 s52, 0x200000\n"
".Lpolli:\n"
"s_sleep 32\n"
"global_load_dwordx2 v[242:243], v240, s[62:63] sc1\n"
"s_sub_u32 s52, s52, 1\n"
"s_cbranch_scc1 .Lpolledi\n"
"s_waitcnt vmcnt(0)\n"
"v_cmp_ne_u64 vcc, s[74:75], v[242:243]\n"
"s_nop 1\n"
"s_cbranch_vccnz .Lpolli\n"
".Lpolledi:\n"
"s_waitcnt vmcnt(0)\n"
"s_barrier\n"
"s_cmp_lg_u32 s40, 0\n"
"s_cbranch_scc1 .Lfin\n"
"v_mov_b32 v240, 32768\n"
"v_mov_b32 v242, s74\n"
"v_mov_b32 v243, s75\n"
"s_mov_b64 exec, 1\n"
"global_store_dwordx2 v240, v[242:243], s[62:63] sc1\n"
"s_mov_b64 exec, -1\n"
"s_branch .Lfin\n"
".Lslow:\n"
"s_mov_b32 s52, 0x200000\n"
".Lpollq:\n"
"s_sleep 8\n"
"global_load_dwordx2 v[250:251], v249, s[62:63] sc1\n"
"s_sub_u32 s52, s52, 1\n"
"s_cbranch_scc1 .Lpolledq\n"
"s_waitcnt vmcnt(0)\n"
"v_cmp_ne_u64 vcc, s[74:75], v[250:251]\n"
"s_nop 1\n"
"s_cbranch_vccnz .Lpollq\n"
".Lpolledq:\n"
"s_waitcnt vmcnt(0)\n"
"s_branch .Lpolledp\n"
".LslowA:\n"
"s_mov_b32 s52, 0x100000\n"
".LspA:\n"
"buffer_load_dwordx4 v[64:67], v248, s[28:31], s46 offen offset:0 sc1\n"
"buffer_load_dwordx4 v[68:71], v248, s[28:31], s46 offen offset:1024 sc1\n"
"buffer_load_dwordx4 v[72:75], v248, s[28:31], s46 offen offset:2048 sc1\n"
"buffer_load_dwordx4 v[76:79], v248, s[28:31], s46 offen offset:3072 sc1\n"
"s_waitcnt vmcnt(0)\n"
"v_xor_b32 v64, v64, v224\n"
"v_xor_b32 v65, v65, v225\n"
"v_or3_b32 v250, v64, v65, v64\n"
"v_xor_b32 v66, v66, v226\n"
"v_xor_b32 v67, v67, v227\n"
"v_or3_b32 v250, v66, v67, v250\n"
"v_xor_b32 v68, v68, v224\n"
"v_xor_b32 v69, v69, v225\n"
"v_or3_b32 v250, v68, v69, v250\n"
"v_xor_b32 v70, v70, v226\n"
"v_xor_b32 v71, v71, v227\n"
"v_or3_b32 v250, v70, v71, v250\n"
"v_xor_b32 v72, v72, v224\n"
"v_xor_b32 v73, v73, v225\n"
"v_or3_b32 v250, v72, v73, v250\n"
"v_xor_b32 v74, v74, v226\n"
"v_xor_b32 v75, v75, v227\n"
"v_or3_b32 v250, v74, v75, v250\n"
"v_xor_b32 v76, v76, v224\n"
"v_xor_b32 v77, v77, v225\n"
"v_or3_b32 v250, v76, v77, v250\n"
"v_xor_b32 v78, v78, v226\n"
"v_xor_b32 v79, v79, v227\n"
"v_or3_b32 v250, v78, v79, v250\n"
"v_lshlrev_b32 v250, 24, v250\n"
"v_cmp_ne_u32 vcc, 0, v250\n"
"s_sub_u32 s52, s52, 1\n"
"s_cbranch_scc1 .LbackA\n"
"s_cbranch_vccz .LbackA\n"
"s_sleep 8\n"
"s_branch .LspA\n"
".LslowB:\n"
"s_mov_b32 s52, 0x100000\n"
".LspB:\n"
"buffer_load_dwordx4 v[80:83], v248, s[28:31], s57 offen offset:0 sc1\n"
"buffer_load_dwordx4 v[84:87], v248, s[28:31], s57 offen offset:1024 sc1\n"
"buffer_load_dwordx4 v[88:91], v248, s[28:31], s57 offen offset:2048 sc1\n"
"buffer_load_dwordx4 v[92:95], v248, s[28:31], s57 offen offset:3072 sc1\n"
"s_waitcnt vmcnt(0)\n"
"v_xor_b32 v80, v80, v224\n"
"v_xor_b32 v81, v81, v225\n"
"v_or3_b32 v250, v80, v81, v80\n"
"v_xor_b32 v82, v82, v226\n"
"v_xor_b32 v83, v83, v227\n"
"v_or3_b32 v250, v82, v83, v250\n"
"v_xor_b32 v84, v84, v224\n"
"v_xor_b32 v85, v85, v225\n"
"v_or3_b32 v250, v84, v85, v250\n"
"v_xor_b32 v86, v86, v226\n"
"v_xor_b32 v87, v87, v227\n"
"v_or3_b32 v250, v86, v87, v250\n"
"v_xor_b32 v88, v88, v224\n"
"v_xor_b32 v89, v89, v225\n"
"v_or3_b32 v250, v88, v89, v250\n"
"v_xor_b32 v90, v90, v226\n"
"v_xor_b32 v91, v91, v227\n"
"v_or3_b32 v250, v90, v91, v250\n"
"v_xor_b32 v92, v92, v224\n"
"v_xor_b32 v93, v93, v225\n"
"v_or3_b32 v250, v92, v93, v250\n"
"v_xor_b32 v94, v94, v226\n"
"v_xor_b32 v95, v95, v227\n"
"v_or3_b32 v250, v94, v95, v250\n"
"v_lshlrev_b32 v250, 24, v250\n"
"v_cmp_ne_u32 vcc, 0, v250\n"
"s_sub_u32 s52, s52, 1\n"
"s_cbranch_scc1 .LbackB\n"
"s_cbranch_vccz .LbackB\n"
"s_sleep 8\n"
"s_branch .LspB\n"
".LslowC:\n"
"s_mov_b32 s52, 0x100000\n"
".LspC:\n"
"buffer_load_dwordx4 v[112:115], v248, s[28:31], s59 offen offset:0 sc1\n"
"buffer_load_dwordx4 v[116:119], v248, s[28:31], s59 offen offset:1024 sc1\n"
"buffer_load_dwordx4 v[120:123], v248, s[28:31], s59 offen offset:2048 sc1\n"
"buffer_load_dwordx4 v[124:127], v248, s[28:31], s59 offen offset:3072 sc1\n"
"s_waitcnt vmcnt(0)\n"
"v_xor_b32 v112, v112, v224\n"
"v_xor_b32 v113, v113, v225\n"
"v_or3_b32 v250, v112, v113, v112\n"
"v_xor_b32 v114, v114, v226\n"
"v_xor_b32 v115, v115, v227\n"
"v_or3_b32 v250, v114, v115, v250\n"
"v_xor_b32 v116, v116, v224\n"
"v_xor_b32 v117, v117, v225\n"
"v_or3_b32 v250, v116, v117, v250\n"
"v_xor_b32 v118, v118, v226\n"
"v_xor_b32 v119, v119, v227\n"
"v_or3_b32 v250, v118, v119, v250\n"
"v_xor_b32 v120, v120, v224\n"
"v_xor_b32 v121, v121, v225\n"
"v_or3_b32 v250, v120, v121, v250\n"
"v_xor_b32 v122, v122, v226\n"
"v_xor_b32 v123, v123, v227\n"
"v_or3_b32 v250, v122, v123, v250\n"
"v_xor_b32 v124, v124, v224\n"
"v_xor_b32 v125, v125, v225\n"
"v_or3_b32 v250, v124, v125, v250\n"
"v_xor_b32 v126, v126, v226\n"
"v_xor_b32 v127, v127, v227\n"
"v_or3_b32 v250, v126, v127, v250\n"
"v_lshlrev_b32 v250, 24, v250\n"
"v_cmp_ne_u32 vcc, 0, v250\n"
"s_sub_u32 s52, s52, 1\n"
"s_cbranch_scc1 .LbackC\n"
"s_cbranch_vccz .LbackC\n"
"s_branch .LspC\n"
".Lpa0:\n"
"s_memrealtime s[94:95]\n"
"s_waitcnt lgkmcnt(0)\n"
"s_sub_u32 s94, s94, s98\n"
"s_cmp_lt_i32 s94, 0\n"
"s_cbranch_scc1 .Lpa0\n"
"s_branch .Lpa0x\n"
".Lpa1:\n"
"s_memrealtime s[94:95]\n"
"s_waitcnt lgkmcnt(0)\n"
"s_sub_u32 s94, s94, s98\n"
"s_cmp_lt_i32 s94, 0\n"
"s_cbranch_scc1 .Lpa1\n"
"s_branch .Lpa1x\n"
".Lpa4:\n"
"s_memrealtime s[94:95]\n"
"s_waitcnt lgkmcnt(0)\n"
"s_sub_u32 s94, s94, s98\n"
"s_cmp_lt_i32 s94, 0\n"
"s_cbranch_scc1 .Lpa4\n"
"s_branch .Lpa4x\n"
".Lpb0:\n"
"s_memrealtime s[94:95]\n"
"s_waitcnt lgkmcnt(0)\n"
"s_sub_u32 s94, s94, s98\n"
"s_cmp_lt_i32 s94, 0\n"
"s_cbranch_scc1 .Lpb0\n"
"s_branch .Lpb0x\n"
".Lpb1:\n"
"s_memrealtime s[94:95]\n"
"s_waitcnt lgkmcnt(0)\n"
"s_sub_u32 s94, s94, s98\n"
"s_cmp_lt_i32 s94, 0\n"
"s_cbranch_scc1 .Lpb1\n"
"s_branch .Lpb1x\n"
".Lpc3:\n"
"s_memrealtime s[94:95]\n"
"s_waitcnt lgkmcnt(0)\n"
"s_sub_u32 s94, s94, s98\n"
"s_cmp_lt_i32 s94, 0\n"
"s_cbranch_scc1 .Lpc3\n"
"s_branch .Lpc3x\n"
".Lpc4:\n"
"s_memrealtime s[94:95]\n"
"s_waitcnt lgkmcnt(0)\n"
"s_sub_u32 s94, s94, s98\n"
"s_cmp_lt_i32 s94, 0\n"
"s_cbranch_scc1 .Lpc4\n"
"s_branch .Lpc4x\n"
".Lstub_0:\n"
"s_call_b64 s[48:49], .Lrare_0_0\n"
"s_branch .Lback_0\n"
".Lstub_1:\n"
"s_call_b64 s[48:49], .Lrare_1_1\n"
"s_branch .Lback_1\n"
".Lstub_2:\n"
"s_call_b64 s[48:49], .Lrare_2_0\n"
"s_branch .Lback_2\n"
".Lstub_3:\n"
"s_call_b64 s[48:49], .Lrare_3_1\n"
"s_branch .Lback_3\n"
".Lstub_4:\n"
"s_call_b64 s[48:49], .Lrare_4_0\n"
"s_branch .Lback_4\n"
".Lstub_5:\n"
"s_call_b64 s[48:49], .Lrare_5_1\n"
"s_branch .Lback_5\n"
".Lstub_6:\n"
"s_call_b64 s[48:49], .Lrare_0_0\n"
"s_branch .Lback_6\n"
".Lstub_7:\n"
"s_call_b64 s[48:49], .Lrare_1_1\n"
"s_branch .Lback_7\n"
".Lstub_8:\n"
"s_call_b64 s[48:49], .Lrare_2_0\n"
"s_branch .Lback_8\n"
".Lstub_9:\n"
"s_call_b64 s[48:49], .Lrare_3_1\n"
"s_branch .Lback_9\n"
".Lstub_10:\n"
"s_call_b64 s[48:49], .Lrare_4_0\n"
"s_branch .Lback_10\n"
".Lstub_11:\n"
"s_call_b64 s[48:49], .Lrare_5_1\n"
"s_branch .Lback_11\n"
".Lstub_12:\n"
"s_call_b64 s[48:49], .Lrare_0_0\n"
"s_branch .Lback_12\n"
".Lstub_13:\n"
"s_call_b64 s[48:49], .Lrare_1_1\n"
"s_branch .Lback_13\n"
".Lstub_14:\n"
"s_call_b64 s[48:49], .Lrare_2_0\n"
"s_branch .Lback_14\n"
".Lstub_15:\n"
"s_call_b64 s[48:49], .Lrare_3_1\n"
"s_branch .Lback_15\n"
".Lstub_16:\n"
"s_call_b64 s[48:49], .Lrare_4_0\n"
"s_branch .Lback_16\n"
".Lstub_17:\n"
"s_call_b64 s[48:49], .Lrare_5_1\n"
"s_branch .Lback_17\n"
".Lstub_18:\n"
"s_call_b64 s[48:49], .Lrare_0_0\n"
"s_branch .Lback_18\n"
".Lstub_19:\n"
"s_call_b64 s[48:49], .Lrare_1_1\n"
"s_branch .Lback_19\n"
".Lstub_20:\n"
"s_call_b64 s[48:49], .Lrare_2_0\n"
"s_branch .Lback_20\n"
".Lstub_21:\n"
"s_call_b64 s[48:49], .Lrare_3_1\n"
"s_branch .Lback_21\n"
".Lstub_22:\n"
"s_call_b64 s[48:49], .Lrare_4_0\n"
"s_branch .Lback_22\n"
".Lstub_23:\n"
"s_call_b64 s[48:49], .Lrare_5_1\n"
"s_branch .Lback_23\n"
".Lrare_0_0:\n"
"v_max3_f32 v228, v184, v185, v186\n"
"v_max3_f32 v228, v228, v187, v188\n"
"v_max3_f32 v228, v228, v189, v190\n"
"v_max3_f32 v228, v228, v191, v192\n"
"v_max3_f32 v228, v228, v193, v194\n"
"v_max3_f32 v228, v228, v195, v196\n"
"v_max3_f32 v228, v228, v197, v198\n"
"v_max_f32 v228, v228, v199\n"
"v_mov_b32 v229, v228\n"
"s_nop 1\n"
"v_permlane32_swap_b32 v228, v229\n"
"s_nop 1\n"
"v_max_f32 v228, v228, v229\n"
"v_mov_b32 v229, v228\n"
"s_nop 1\n"
"v_permlane16_swap_b32 v228, v229\n"
"s_nop 1\n"
"v_max_f32 v228, v228, v229\n"
"v_ceil_f32 v228, v228\n"
"v_max_f32 v228, s43, v228\n"
"v_exp_f32 v229, -v228\n"
"s_nop 0\n"
"v_min_f32 v229, 1.0, v229\n"
"v_sub_f32 v160, v160, v228\n"
"v_sub_f32 v161, v161, v228\n"
"v_sub_f32 v162, v162, v228\n"
"v_sub_f32 v163, v163, v228\n"
"v_mul_f32 v240, v240, v229\n"
"v_sub_f32 v230, v184, v228\n"
"v_sub_f32 v231, v185, v228\n"
"v_exp_f32 v230, v230\n"
"v_exp_f32 v231, v231\n"
"s_nop 0\n"
"v_cvt_pk_bf16_f32 v216, v230, v231\n"
"v_sub_f32 v230, v186, v228\n"
"v_sub_f32 v231, v187, v228\n"
"v_exp_f32 v230, v230\n"
"v_exp_f32 v231, v231\n"
"s_nop 0\n"
"v_cvt_pk_bf16_f32 v217, v230, v231\n"
"v_sub_f32 v230, v188, v228\n"
"v_sub_f32 v231, v189, v228\n"
"v_exp_f32 v230, v230\n"
"v_exp_f32 v231, v231\n"
"s_nop 0\n"
"v_cvt_pk_bf16_f32 v218, v230, v231\n"
"v_sub_f32 v230, v190, v228\n"
"v_sub_f32 v231, v191, v228\n"
"v_exp_f32 v230, v230\n"
"v_exp_f32 v231, v231\n"
"s_nop 0\n"
"v_cvt_pk_bf16_f32 v219, v230, v231\n"
"v_sub_f32 v230, v192, v228\n"
"v_sub_f32 v231, v193, v228\n"
"v_exp_f32 v230, v230\n"
"v_exp_f32 v231, v231\n"
"s_nop 0\n"
"v_cvt_pk_bf16_f32 v220, v230, v231\n"
"v_sub_f32 v230, v194, v228\n"
"v_sub_f32 v231, v195, v228\n"
"v_exp_f32 v230, v230\n"
"v_exp_f32 v231, v231\n"
"s_nop 0\n"
"v_cvt_pk_bf16_f32 v221, v230, v231\n"
"v_sub_f32 v230, v196, v228\n"
"v_sub_f32 v231, v197, v228\n"
"v_exp_f32 v230, v230\n"
"v_exp_f32 v231, v231\n"
"s_nop 0\n"
"v_cvt_pk_bf16_f32 v222, v230, v231\n"
"v_sub_f32 v230, v198, v228\n"
"v_sub_f32 v231, v199, v228\n"
"v_exp_f32 v230, v230\n"
"v_exp_f32 v231, v231\n"
"s_nop 0\n"
"v_cvt_pk_bf16_f32 v223, v230, v231\n"
"v_mbcnt_lo_u32_b32 v230, -1, 0\n"
"v_mbcnt_hi_u32_b32 v230, -1, v230\n"
"v_and_b32 v230, 48, v230\n"
"ds_bpermute_b32 v184, v230, v229 offset:0\n"
"ds_bpermute_b32 v185, v230, v229 offset:4\n"
"ds_bpermute_b32 v186, v230, v229 offset:8\n"
"ds_bpermute_b32 v187, v230, v229 offset:12\n"
"s_waitcnt lgkmcnt(0)\n"
"v_accvgpr_read_b32 v188, a0\n"
"v_accvgpr_read_b32 v189, a1\n"
"v_accvgpr_read_b32 v190, a2\n"
"v_accvgpr_read_b32 v191, a3\n"
"v_accvgpr_read_b32 v192, a4\n"
"v_accvgpr_read_b32 v193, a5\n"
"v_accvgpr_read_b32 v194, a6\n"
"v_accvgpr_read_b32 v195, a7\n"
"v_mul_f32 v188, v188, v184\n"
"v_mul_f32 v189, v189, v185\n"
"v_mul_f32 v190, v190, v186\n"
"v_mul_f32 v191, v191, v187\n"
"v_mul_f32 v192, v192, v184\n"
"v_mul_f32 v193, v193, v185\n"
"v_mul_f32 v194, v194, v186\n"
"v_mul_f32 v195, v195, v187\n"
"v_accvgpr_write_b32 a0, v188\n"
"v_accvgpr_write_b32 a1, v189\n"
"v_accvgpr_write_b32 a2, v190\n"
"v_accvgpr_write_b32 a3, v191\n"
"v_accvgpr_write_b32 a4, v192\n"
"v_accvgpr_write_b32 a5, v193\n"
"v_accvgpr_write_b32 a6, v194\n"
"v_accvgpr_write_b32 a7, v195\n"
"v_accvgpr_read_b32 v188, a8\n"
"v_accvgpr_read_b32 v189, a9\n"
"v_accvgpr_read_b32 v190, a10\n"
"v_accvgpr_read_b32 v191, a11\n"
"v_accvgpr_read_b32 v192, a12\n"
"v_accvgpr_read_b32 v193, a13\n"
"v_accvgpr_read_b32 v194, a14\n"
"v_accvgpr_read_b32 v195, a15\n"
"v_mul_f32 v188, v188, v184\n"
"v_mul_f32 v189, v189, v185\n"
"v_mul_f32 v190, v190, v186\n"
"v_mul_f32 v191, v191, v187\n"
"v_mul_f32 v192, v192, v184\n"
"v_mul_f32 v193, v193, v185\n"
"v_mul_f32 v194, v194, v186\n"
"v_mul_f32 v195, v195, v187\n"
"v_accvgpr_write_b32 a8, v188\n"
"v_accvgpr_write_b32 a9, v189\n"
"v_accvgpr_write_b32 a10, v190\n"
"v_accvgpr_write_b32 a11, v191\n"
"v_accvgpr_write_b32 a12, v192\n"
"v_accvgpr_write_b32 a13, v193\n"
"v_accvgpr_write_b32 a14, v194\n"
"v_accvgpr_write_b32 a15, v195\n"
"v_accvgpr_read_b32 v188, a16\n"
"v_accvgpr_read_b32 v189, a17\n"
"v_accvgpr_read_b32 v190, a18\n"
"v_accvgpr_read_b32 v191, a19\n"
"v_accvgpr_read_b32 v192, a20\n"
"v_accvgpr_read_b32 v193, a21\n"
"v_accvgpr_read_b32 v194, a22\n"
"v_accvgpr_read_b32 v195, a23\n"
"v_mul_f32 v188, v188, v184\n"
"v_mul_f32 v189, v189, v185\n"
"v_mul_f32 v190, v190, v186\n"
"v_mul_f32 v191, v191, v187\n"
"v_mul_f32 v192, v192, v184\n"
"v_mul_f32 v193, v193, v185\n"
"v_mul_f32 v194, v194, v186\n"
"v_mul_f32 v195, v195, v187\n"
"v_accvgpr_write_b32 a16, v188\n"
"v_accvgpr_write_b32 a17, v189\n"
"v_accvgpr_write_b32 a18, v190\n"
"v_accvgpr_write_b32 a19, v191\n"
"v_accvgpr_write_b32 a20, v192\n"
"v_accvgpr_write_b32 a21, v193\n"
"v_accvgpr_write_b32 a22, v194\n"
"v_accvgpr_write_b32 a23, v195\n"
"v_accvgpr_read_b32 v188, a24\n"
"v_accvgpr_read_b32 v189, a25\n"
"v_accvgpr_read_b32 v190, a26\n"
"v_accvgpr_read_b32 v191, a27\n"
"v_accvgpr_read_b32 v192, a28\n"
"v_accvgpr_read_b32 v193, a29\n"
"v_accvgpr_read_b32 v194, a30\n"
"v_accvgpr_read_b32 v195, a31\n"
"v_mul_f32 v188, v188, v184\n"
"v_mul_f32 v189, v189, v185\n"
"v_mul_f32 v190, v190, v186\n"
"v_mul_f32 v191, v191, v187\n"
"v_mul_f32 v192, v192, v184\n"
"v_mul_f32 v193, v193, v185\n"
"v_mul_f32 v194, v194, v186\n"
"v_mul_f32 v195, v195, v187\n"
"v_accvgpr_write_b32 a24, v188\n"
"v_accvgpr_write_b32 a25, v189\n"
"v_accvgpr_write_b32 a26, v190\n"
"v_accvgpr_write_b32 a27, v191\n"
"v_accvgpr_write_b32 a28, v192\n"
"v_accvgpr_write_b32 a29, v193\n"
"v_accvgpr_write_b32 a30, v194\n"
"v_accvgpr_write_b32 a31, v195\n"
"s_nop 1\n"
"v_mfma_f32_16x16x32_bf16 v[228:231], v[236:239], v[216:219], 0\n"
"v_mfma_f32_16x16x32_bf16 v[228:231], v[236:239], v[220:223], v[228:231]\n"
"s_nop 7\n"
"s_setpc_b64 s[48:49]\n"
".Lrare_1_1:\n"
"v_max3_f32 v220, v200, v201, v202\n"
"v_max3_f32 v220, v220, v203, v204\n"
"v_max3_f32 v220, v220, v205, v206\n"
"v_max3_f32 v220, v220, v207, v208\n"
"v_max3_f32 v220, v220, v209, v210\n"
"v_max3_f32 v220, v220, v211, v212\n"
"v_max3_f32 v220, v220, v213, v214\n"
"v_max_f32 v220, v220, v215\n"
"v_mov_b32 v221, v220\n"
"s_nop 1\n"
"v_permlane32_swap_b32 v220, v221\n"
"s_nop 1\n"
"v_max_f32 v220, v220, v221\n"
"v_mov_b32 v221, v220\n"
"s_nop 1\n"
"v_permlane16_swap_b32 v220, v221\n"
"s_nop 1\n"
"v_max_f32 v220, v220, v221\n"
"v_ceil_f32 v220, v220\n"
"v_max_f32 v220, s43, v220\n"
"v_exp_f32 v221, -v220\n"
"s_nop 0\n"
"v_min_f32 v221, 1.0, v221\n"
"v_sub_f32 v164, v164, v220\n"
"v_sub_f32 v165, v165, v220\n"
"v_sub_f32 v166, v166, v220\n"
"v_sub_f32 v167, v167, v220\n"
"v_mul_f32 v241, v241, v221\n"
"v_sub_f32 v222, v200, v220\n"
"v_sub_f32 v223, v201, v220\n"
"v_exp_f32 v222, v222\n"
"v_exp_f32 v223, v223\n"
"s_nop 0\n"
"v_cvt_pk_bf16_f32 v224, v222, v223\n"
"v_sub_f32 v222, v202, v220\n"
"v_sub_f32 v223, v203, v220\n"
"v_exp_f32 v222, v222\n"
"v_exp_f32 v223, v223\n"
"s_nop 0\n"
"v_cvt_pk_bf16_f32 v225, v222, v223\n"
"v_sub_f32 v222, v204, v220\n"
"v_sub_f32 v223, v205, v220\n"
"v_exp_f32 v222, v222\n"
"v_exp_f32 v223, v223\n"
"s_nop 0\n"
"v_cvt_pk_bf16_f32 v226, v222, v223\n"
"v_sub_f32 v222, v206, v220\n"
"v_sub_f32 v223, v207, v220\n"
"v_exp_f32 v222, v222\n"
"v_exp_f32 v223, v223\n"
"s_nop 0\n"
"v_cvt_pk_bf16_f32 v227, v222, v223\n"
"v_sub_f32 v222, v208, v220\n"
"v_sub_f32 v223, v209, v220\n"
"v_exp_f32 v222, v222\n"
"v_exp_f32 v223, v223\n"
"s_nop 0\n"
"v_cvt_pk_bf16_f32 v228, v222, v223\n"
"v_sub_f32 v222, v210, v220\n"
"v_sub_f32 v223, v211, v220\n"
"v_exp_f32 v222, v222\n"
"v_exp_f32 v223, v223\n"
"s_nop 0\n"
"v_cvt_pk_bf16_f32 v229, v222, v223\n"
"v_sub_f32 v222, v212, v220\n"
"v_sub_f32 v223, v213, v220\n"
"v_exp_f32 v222, v222\n"
"v_exp_f32 v223, v223\n"
"s_nop 0\n"
"v_cvt_pk_bf16_f32 v230, v222, v223\n"
"v_sub_f32 v222, v214, v220\n"
"v_sub_f32 v223, v215, v220\n"
"v_exp_f32 v222, v222\n"
"v_exp_f32 v223, v223\n"
"s_nop 0\n"
"v_cvt_pk_bf16_f32 v231, v222, v223\n"
"v_mbcnt_lo_u32_b32 v222, -1, 0\n"
"v_mbcnt_hi_u32_b32 v222, -1, v222\n"
"v_and_b32 v222, 48, v222\n"
"ds_bpermute_b32 v200, v222, v221 offset:0\n"
"ds_bpermute_b32 v201, v222, v221 offset:4\n"
"ds_bpermute_b32 v202, v222, v221 offset:8\n"
"ds_bpermute_b32 v203, v222, v221 offset:12\n"
"s_waitcnt lgkmcnt(0)\n"
"v_mul_f32 v0, v0, v200\n"
"v_mul_f32 v1, v1, v201\n"
"v_mul_f32 v2, v2, v202\n"
"v_mul_f32 v3, v3, v203\n"
"v_mul_f32 v4, v4, v200\n"
"v_mul_f32 v5, v5, v201\n"
"v_mul_f32 v6, v6, v202\n"
"v_mul_f32 v7, v7, v203\n"
"v_mul_f32 v8, v8, v200\n"
"v_mul_f32 v9, v9, v201\n"
"v_mul_f32 v10, v10, v202\n"
"v_mul_f32 v11, v11, v203\n"
"v_mul_f32 v12, v12, v200\n"
"v_mul_f32 v13, v13, v201\n"
"v_mul_f32 v14, v14, v202\n"
"v_mul_f32 v15, v15, v203\n"
"v_mul_f32 v16, v16, v200\n"
"v_mul_f32 v17, v17, v201\n"
"v_mul_f32 v18, v18, v202\n"
"v_mul_f32 v19, v19, v203\n"
"v_mul_f32 v20, v20, v200\n"
"v_mul_f32 v21, v21, v201\n"
"v_mul_f32 v22, v22, v202\n"
"v_mul_f32 v23, v23, v203\n"
"v_mul_f32 v24, v24, v200\n"
"v_mul_f32 v25, v25, v201\n"
"v_mul_f32 v26, v26, v202\n"
"v_mul_f32 v27, v27, v203\n"
"v_mul_f32 v28, v28, v200\n"
"v_mul_f32 v29, v29, v201\n"
"v_mul_f32 v30, v30, v202\n"
"v_mul_f32 v31, v31, v203\n"
"s_nop 1\n"
"v_mfma_f32_16x16x32_bf16 v[220:223], v[236:239], v[224:227], 0\n"
"v_mfma_f32_16x16x32_bf16 v[220:223], v[236:239], v[228:231], v[220:223]\n"
"s_nop 7\n"
"s_setpc_b64 s[48:49]\n"
".Lrare_2_0:\n"
"v_max3_f32 v228, v184, v185, v186\n"
"v_max3_f32 v228, v228, v187, v188\n"
"v_max3_f32 v228, v228, v189, v190\n"
"v_max3_f32 v228, v228, v191, v192\n"
"v_max3_f32 v228, v228, v193, v194\n"
"v_max3_f32 v228, v228, v195, v196\n"
"v_max3_f32 v228, v228, v197, v198\n"
"v_max_f32 v228, v228, v199\n"
"v_mov_b32 v229, v228\n"
"s_nop 1\n"
"v_permlane32_swap_b32 v228, v229\n"
"s_nop 1\n"
"v_max_f32 v228, v228, v229\n"
"v_mov_b32 v229, v228\n"
"s_nop 1\n"
"v_permlane16_swap_b32 v228, v229\n"
"s_nop 1\n"
"v_max_f32 v228, v228, v229\n"
"v_ceil_f32 v228, v228\n"
"v_max_f32 v228, s43, v228\n"
"v_exp_f32 v229, -v228\n"
"s_nop 0\n"
"v_min_f32 v229, 1.0, v229\n"
"v_sub_f32 v168, v168, v228\n"
"v_sub_f32 v169, v169, v228\n"
"v_sub_f32 v170, v170, v228\n"
"v_sub_f32 v171, v171, v228\n"
"v_mul_f32 v242, v242, v229\n"
"v_sub_f32 v230, v184, v228\n"
"v_sub_f32 v231, v185, v228\n"
"v_exp_f32 v230, v230\n"
"v_exp_f32 v231, v231\n"
"s_nop 0\n"
"v_cvt_pk_bf16_f32 v216, v230, v231\n"
"v_sub_f32 v230, v186, v228\n"
"v_sub_f32 v231, v187, v228\n"
"v_exp_f32 v230, v230\n"
"v_exp_f32 v231, v231\n"
"s_nop 0\n"
"v_cvt_pk_bf16_f32 v217, v230, v231\n"
"v_sub_f32 v230, v188, v228\n"
"v_sub_f32 v231, v189, v228\n"
"v_exp_f32 v230, v230\n"
"v_exp_f32 v231, v231\n"
"s_nop 0\n"
"v_cvt_pk_bf16_f32 v218, v230, v231\n"
"v_sub_f32 v230, v190, v228\n"
"v_sub_f32 v231, v191, v228\n"
"v_exp_f32 v230, v230\n"
"v_exp_f32 v231, v231\n"
"s_nop 0\n"
"v_cvt_pk_bf16_f32 v219, v230, v231\n"
"v_sub_f32 v230, v192, v228\n"
"v_sub_f32 v231, v193, v228\n"
"v_exp_f32 v230, v230\n"
"v_exp_f32 v231, v231\n"
"s_nop 0\n"
"v_cvt_pk_bf16_f32 v220, v230, v231\n"
"v_sub_f32 v230, v194, v228\n"
"v_sub_f32 v231, v195, v228\n"
"v_exp_f32 v230, v230\n"
"v_exp_f32 v231, v231\n"
"s_nop 0\n"
"v_cvt_pk_bf16_f32 v221, v230, v231\n"
"v_sub_f32 v230, v196, v228\n"
"v_sub_f32 v231, v197, v228\n"
"v_exp_f32 v230, v230\n"
"v_exp_f32 v231, v231\n"
"s_nop 0\n"
"v_cvt_pk_bf16_f32 v222, v230, v231\n"
"v_sub_f32 v230, v198, v228\n"
"v_sub_f32 v231, v199, v228\n"
"v_exp_f32 v230, v230\n"
"v_exp_f32 v231, v231\n"
"s_nop 0\n"
"v_cvt_pk_bf16_f32 v223, v230, v231\n"
"v_mbcnt_lo_u32_b32 v230, -1, 0\n"
"v_mbcnt_hi_u32_b32 v230, -1, v230\n"
"v_and_b32 v230, 48, v230\n"
"ds_bpermute_b32 v184, v230, v229 offset:0\n"
"ds_bpermute_b32 v185, v230, v229 offset:4\n"
"ds_bpermute_b32 v186, v230, v229 offset:8\n"
"ds_bpermute_b32 v187, v230, v229 offset:12\n"
"s_waitcnt lgkmcnt(0)\n"
"v_mul_f32 v32, v32, v184\n"
"v_mul_f32 v33, v33, v185\n"
"v_mul_f32 v34, v34, v186\n"
"v_mul_f32 v35, v35, v187\n"
"v_mul_f32 v36, v36, v184\n"
"v_mul_f32 v37, v37, v185\n"
"v_mul_f32 v38, v38, v186\n"
"v_mul_f32 v39, v39, v187\n"
"v_mul_f32 v40, v40, v184\n"
"v_mul_f32 v41, v41, v185\n"
"v_mul_f32 v42, v42, v186\n"
"v_mul_f32 v43, v43, v187\n"
"v_mul_f32 v44, v44, v184\n"
"v_mul_f32 v45, v45, v185\n"
"v_mul_f32 v46, v46, v186\n"
"v_mul_f32 v47, v47, v187\n"
"v_mul_f32 v48, v48, v184\n"
"v_mul_f32 v49, v49, v185\n"
"v_mul_f32 v50, v50, v186\n"
"v_mul_f32 v51, v51, v187\n"
"v_mul_f32 v52, v52, v184\n"
"v_mul_f32 v53, v53, v185\n"
"v_mul_f32 v54, v54, v186\n"
"v_mul_f32 v55, v55, v187\n"
"v_mul_f32 v56, v56, v184\n"
"v_mul_f32 v57, v57, v185\n"
"v_mul_f32 v58, v58, v186\n"
"v_mul_f32 v59, v59, v187\n"
"v_mul_f32 v60, v60, v184\n"
"v_mul_f32 v61, v61, v185\n"
"v_mul_f32 v62, v62, v186\n"
"v_mul_f32 v63, v63, v187\n"
"s_nop 1\n"
"v_mfma_f32_16x16x32_bf16 v[228:231], v[236:239], v[216:219], 0\n"
"v_mfma_f32_16x16x32_bf16 v[228:231], v[236:239], v[220:223], v[228:231]\n"
"s_nop 7\n"
"s_setpc_b64 s[48:49]\n"
".Lrare_3_1:\n"
"v_max3_f32 v220, v200, v201, v202\n"
"v_max3_f32 v220, v220, v203, v204\n"
"v_max3_f32 v220, v220, v205, v206\n"
"v_max3_f32 v220, v220, v207, v208\n"
"v_max3_f32 v220, v220, v209, v210\n"
"v_max3_f32 v220, v220, v211, v212\n"
"v_max3_f32 v220, v220, v213, v214\n"
"v_max_f32 v220, v220, v215\n"
"v_mov_b32 v221, v220\n"
"s_nop 1\n"
"v_permlane32_swap_b32 v220, v221\n"
"s_nop 1\n"
"v_max_f32 v220, v220, v221\n"
"v_mov_b32 v221, v220\n"
"s_nop 1\n"
"v_permlane16_swap_b32 v220, v221\n"
"s_nop 1\n"
"v_max_f32 v220, v220, v221\n"
"v_ceil_f32 v220, v220\n"
"v_max_f32 v220, s43, v220\n"
"v_exp_f32 v221, -v220\n"
"s_nop 0\n"
"v_min_f32 v221, 1.0, v221\n"
"v_sub_f32 v172, v172, v220\n"
"v_sub_f32 v173, v173, v220\n"
"v_sub_f32 v174, v174, v220\n"
"v_sub_f32 v175, v175, v220\n"
"v_mul_f32 v243, v243, v221\n"
"v_sub_f32 v222, v200, v220\n"
"v_sub_f32 v223, v201, v220\n"
"v_exp_f32 v222, v222\n"
"v_exp_f32 v223, v223\n"
"s_nop 0\n"
"v_cvt_pk_bf16_f32 v224, v222, v223\n"
"v_sub_f32 v222, v202, v220\n"
"v_sub_f32 v223, v203, v220\n"
"v_exp_f32 v222, v222\n"
"v_exp_f32 v223, v223\n"
"s_nop 0\n"
"v_cvt_pk_bf16_f32 v225, v222, v223\n"
"v_sub_f32 v222, v204, v220\n"
"v_sub_f32 v223, v205, v220\n"
"v_exp_f32 v222, v222\n"
"v_exp_f32 v223, v223\n"
"s_nop 0\n"
"v_cvt_pk_bf16_f32 v226, v222, v223\n"
"v_sub_f32 v222, v206, v220\n"
"v_sub_f32 v223, v207, v220\n"
"v_exp_f32 v222, v222\n"
"v_exp_f32 v223, v223\n"
"s_nop 0\n"
"v_cvt_pk_bf16_f32 v227, v222, v223\n"
"v_sub_f32 v222, v208, v220\n"
"v_sub_f32 v223, v209, v220\n"
"v_exp_f32 v222, v222\n"
"v_exp_f32 v223, v223\n"
"s_nop 0\n"
"v_cvt_pk_bf16_f32 v228, v222, v223\n"
"v_sub_f32 v222, v210, v220\n"
"v_sub_f32 v223, v211, v220\n"
"v_exp_f32 v222, v222\n"
"v_exp_f32 v223, v223\n"
"s_nop 0\n"
"v_cvt_pk_bf16_f32 v229, v222, v223\n"
"v_sub_f32 v222, v212, v220\n"
"v_sub_f32 v223, v213, v220\n"
"v_exp_f32 v222, v222\n"
"v_exp_f32 v223, v223\n"
"s_nop 0\n"
"v_cvt_pk_bf16_f32 v230, v222, v223\n"
"v_sub_f32 v222, v214, v220\n"
"v_sub_f32 v223, v215, v220\n"
"v_exp_f32 v222, v222\n"
"v_exp_f32 v223, v223\n"
"s_nop 0\n"
"v_cvt_pk_bf16_f32 v231, v222, v223\n"
"v_mbcnt_lo_u32_b32 v222, -1, 0\n"
"v_mbcnt_hi_u32_b32 v222, -1, v222\n"
"v_and_b32 v222, 48, v222\n"
"ds_bpermute_b32 v200, v222, v221 offset:0\n"
"ds_bpermute_b32 v201, v222, v221 offset:4\n"
"ds_bpermute_b32 v202, v222, v221 offset:8\n"
"ds_bpermute_b32 v203, v222, v221 offset:12\n"
"s_waitcnt lgkmcnt(0)\n"
"v_mul_f32 v64, v64, v200\n"
"v_mul_f32 v65, v65, v201\n"
"v_mul_f32 v66, v66, v202\n"
"v_mul_f32 v67, v67, v203\n"
"v_mul_f32 v68, v68, v200\n"
"v_mul_f32 v69, v69, v201\n"
"v_mul_f32 v70, v70, v202\n"
"v_mul_f32 v71, v71, v203\n"
"v_mul_f32 v72, v72, v200\n"
"v_mul_f32 v73, v73, v201\n"
"v_mul_f32 v74, v74, v202\n"
"v_mul_f32 v75, v75, v203\n"
"v_mul_f32 v76, v76, v200\n"
"v_mul_f32 v77, v77, v201\n"
"v_mul_f32 v78, v78, v202\n"
"v_mul_f32 v79, v79, v203\n"
"v_mul_f32 v80, v80, v200\n"
"v_mul_f32 v81, v81, v201\n"
"v_mul_f32 v82, v82, v202\n"
"v_mul_f32 v83, v83, v203\n"
"v_mul_f32 v84, v84, v200\n"
"v_mul_f32 v85, v85, v201\n"
"v_mul_f32 v86, v86, v202\n"
"v_mul_f32 v87, v87, v203\n"
"v_mul_f32 v88, v88, v200\n"
"v_mul_f32 v89, v89, v201\n"
"v_mul_f32 v90, v90, v202\n"
"v_mul_f32 v91, v91, v203\n"
"v_mul_f32 v92, v92, v200\n"
"v_mul_f32 v93, v93, v201\n"
"v_mul_f32 v94, v94, v202\n"
"v_mul_f32 v95, v95, v203\n"
"s_nop 1\n"
"v_mfma_f32_16x16x32_bf16 v[220:223], v[236:239], v[224:227], 0\n"
"v_mfma_f32_16x16x32_bf16 v[220:223], v[236:239], v[228:231], v[220:223]\n"
"s_nop 7\n"
"s_setpc_b64 s[48:49]\n"
".Lrare_4_0:\n"
"v_max3_f32 v228, v184, v185, v186\n"
"v_max3_f32 v228, v228, v187, v188\n"
"v_max3_f32 v228, v228, v189, v190\n"
"v_max3_f32 v228, v228, v191, v192\n"
"v_max3_f32 v228, v228, v193, v194\n"
"v_max3_f32 v228, v228, v195, v196\n"
"v_max3_f32 v228, v228, v197, v198\n"
"v_max_f32 v228, v228, v199\n"
"v_mov_b32 v229, v228\n"
"s_nop 1\n"
"v_permlane32_swap_b32 v228, v229\n"
"s_nop 1\n"
"v_max_f32 v228, v228, v229\n"
"v_mov_b32 v229, v228\n"
"s_nop 1\n"
"v_permlane16_swap_b32 v228, v229\n"
"s_nop 1\n"
"v_max_f32 v228, v228, v229\n"
"v_ceil_f32 v228, v228\n"
"v_max_f32 v228, s43, v228\n"
"v_exp_f32 v229, -v228\n"
"s_nop 0\n"
"v_min_f32 v229, 1.0, v229\n"
"v_sub_f32 v176, v176, v228\n"
"v_sub_f32 v177, v177, v228\n"
"v_sub_f32 v178, v178, v228\n"
"v_sub_f32 v179, v179, v228\n"
"v_mul_f32 v244, v244, v229\n"
"v_sub_f32 v230, v184, v228\n"
"v_sub_f32 v231, v185, v228\n"
"v_exp_f32 v230, v230\n"
"v_exp_f32 v231, v231\n"
"s_nop 0\n"
"v_cvt_pk_bf16_f32 v216, v230, v231\n"
"v_sub_f32 v230, v186, v228\n"
"v_sub_f32 v231, v187, v228\n"
"v_exp_f32 v230, v230\n"
"v_exp_f32 v231, v231\n"
"s_nop 0\n"
"v_cvt_pk_bf16_f32 v217, v230, v231\n"
"v_sub_f32 v230, v188, v228\n"
"v_sub_f32 v231, v189, v228\n"
"v_exp_f32 v230, v230\n"
"v_exp_f32 v231, v231\n"
"s_nop 0\n"
"v_cvt_pk_bf16_f32 v218, v230, v231\n"
"v_sub_f32 v230, v190, v228\n"
"v_sub_f32 v231, v191, v228\n"
"v_exp_f32 v230, v230\n"
"v_exp_f32 v231, v231\n"
"s_nop 0\n"
"v_cvt_pk_bf16_f32 v219, v230, v231\n"
"v_sub_f32 v230, v192, v228\n"
"v_sub_f32 v231, v193, v228\n"
"v_exp_f32 v230, v230\n"
"v_exp_f32 v231, v231\n"
"s_nop 0\n"
"v_cvt_pk_bf16_f32 v220, v230, v231\n"
"v_sub_f32 v230, v194, v228\n"
"v_sub_f32 v231, v195, v228\n"
"v_exp_f32 v230, v230\n"
"v_exp_f32 v231, v231\n"
"s_nop 0\n"
"v_cvt_pk_bf16_f32 v221, v230, v231\n"
"v_sub_f32 v230, v196, v228\n"
"v_sub_f32 v231, v197, v228\n"
"v_exp_f32 v230, v230\n"
"v_exp_f32 v231, v231\n"
"s_nop 0\n"
"v_cvt_pk_bf16_f32 v222, v230, v231\n"
"v_sub_f32 v230, v198, v228\n"
"v_sub_f32 v231, v199, v228\n"
"v_exp_f32 v230, v230\n"
"v_exp_f32 v231, v231\n"
"s_nop 0\n"
"v_cvt_pk_bf16_f32 v223, v230, v231\n"
"v_mbcnt_lo_u32_b32 v230, -1, 0\n"
"v_mbcnt_hi_u32_b32 v230, -1, v230\n"
"v_and_b32 v230, 48, v230\n"
"ds_bpermute_b32 v184, v230, v229 offset:0\n"
"ds_bpermute_b32 v185, v230, v229 offset:4\n"
"ds_bpermute_b32 v186, v230, v229 offset:8\n"
"ds_bpermute_b32 v187, v230, v229 offset:12\n"
"s_waitcnt lgkmcnt(0)\n"
"v_mul_f32 v96, v96, v184\n"
"v_mul_f32 v97, v97, v185\n"
"v_mul_f32 v98, v98, v186\n"
"v_mul_f32 v99, v99, v187\n"
"v_mul_f32 v100, v100, v184\n"
"v_mul_f32 v101, v101, v185\n"
"v_mul_f32 v102, v102, v186\n"
"v_mul_f32 v103, v103, v187\n"
"v_mul_f32 v104, v104, v184\n"
"v_mul_f32 v105, v105, v185\n"
"v_mul_f32 v106, v106, v186\n"
"v_mul_f32 v107, v107, v187\n"
"v_mul_f32 v108, v108, v184\n"
"v_mul_f32 v109, v109, v185\n"
"v_mul_f32 v110, v110, v186\n"
"v_mul_f32 v111, v111, v187\n"
"v_mul_f32 v112, v112, v184\n"
"v_mul_f32 v113, v113, v185\n"
"v_mul_f32 v114, v114, v186\n"
"v_mul_f32 v115, v115, v187\n"
"v_mul_f32 v116, v116, v184\n"
"v_mul_f32 v117, v117, v185\n"
"v_mul_f32 v118, v118, v186\n"
"v_mul_f32 v119, v119, v187\n"
"v_mul_f32 v120, v120, v184\n"
"v_mul_f32 v121, v121, v185\n"
"v_mul_f32 v122, v122, v186\n"
"v_mul_f32 v123, v123, v187\n"
"v_mul_f32 v124, v124, v184\n"
"v_mul_f32 v125, v125, v185\n"
"v_mul_f32 v126, v126, v186\n"
"v_mul_f32 v127, v127, v187\n"
"s_nop 1\n"
"v_mfma_f32_16x16x32_bf16 v[228:231], v[236:239], v[216:219], 0\n"
"v_mfma_f32_16x16x32_bf16 v[228:231], v[236:239], v[220:223], v[228:231]\n"
"s_nop 7\n"
"s_setpc_b64 s[48:49]\n"
".Lrare_5_1:\n"
"v_max3_f32 v220, v200, v201, v202\n"
"v_max3_f32 v220, v220, v203, v204\n"
"v_max3_f32 v220, v220, v205, v206\n"
"v_max3_f32 v220, v220, v207, v208\n"
"v_max3_f32 v220, v220, v209, v210\n"
"v_max3_f32 v220, v220, v211, v212\n"
"v_max3_f32 v220, v220, v213, v214\n"
"v_max_f32 v220, v220, v215\n"
"v_mov_b32 v221, v220\n"
"s_nop 1\n"
"v_permlane32_swap_b32 v220, v221\n"
"s_nop 1\n"
"v_max_f32 v220, v220, v221\n"
"v_mov_b32 v221, v220\n"
"s_nop 1\n"
"v_permlane16_swap_b32 v220, v221\n"
"s_nop 1\n"
"v_max_f32 v220, v220, v221\n"
"v_ceil_f32 v220, v220\n"
"v_max_f32 v220, s43, v220\n"
"v_exp_f32 v221, -v220\n"
"s_nop 0\n"
"v_min_f32 v221, 1.0, v221\n"
"v_sub_f32 v180, v180, v220\n"
"v_sub_f32 v181, v181, v220\n"
"v_sub_f32 v182, v182, v220\n"
"v_sub_f32 v183, v183, v220\n"
"v_mul_f32 v245, v245, v221\n"
"v_sub_f32 v222, v200, v220\n"
"v_sub_f32 v223, v201, v220\n"
"v_exp_f32 v222, v222\n"
"v_exp_f32 v223, v223\n"
"s_nop 0\n"
"v_cvt_pk_bf16_f32 v224, v222, v223\n"
"v_sub_f32 v222, v202, v220\n"
"v_sub_f32 v223, v203, v220\n"
"v_exp_f32 v222, v222\n"
"v_exp_f32 v223, v223\n"
"s_nop 0\n"
"v_cvt_pk_bf16_f32 v225, v222, v223\n"
"v_sub_f32 v222, v204, v220\n"
"v_sub_f32 v223, v205, v220\n"
"v_exp_f32 v222, v222\n"
"v_exp_f32 v223, v223\n"
"s_nop 0\n"
"v_cvt_pk_bf16_f32 v226, v222, v223\n"
"v_sub_f32 v222, v206, v220\n"
"v_sub_f32 v223, v207, v220\n"
"v_exp_f32 v222, v222\n"
"v_exp_f32 v223, v223\n"
"s_nop 0\n"
"v_cvt_pk_bf16_f32 v227, v222, v223\n"
"v_sub_f32 v222, v208, v220\n"
"v_sub_f32 v223, v209, v220\n"
"v_exp_f32 v222, v222\n"
"v_exp_f32 v223, v223\n"
"s_nop 0\n"
"v_cvt_pk_bf16_f32 v228, v222, v223\n"
"v_sub_f32 v222, v210, v220\n"
"v_sub_f32 v223, v211, v220\n"
"v_exp_f32 v222, v222\n"
"v_exp_f32 v223, v223\n"
"s_nop 0\n"
"v_cvt_pk_bf16_f32 v229, v222, v223\n"
"v_sub_f32 v222, v212, v220\n"
"v_sub_f32 v223, v213, v220\n"
"v_exp_f32 v222, v222\n"
"v_exp_f32 v223, v223\n"
"s_nop 0\n"
"v_cvt_pk_bf16_f32 v230, v222, v223\n"
"v_sub_f32 v222, v214, v220\n"
"v_sub_f32 v223, v215, v220\n"
"v_exp_f32 v222, v222\n"
"v_exp_f32 v223, v223\n"
"s_nop 0\n"
"v_cvt_pk_bf16_f32 v231, v222, v223\n"
"v_mbcnt_lo_u32_b32 v222, -1, 0\n"
"v_mbcnt_hi_u32_b32 v222, -1, v222\n"
"v_and_b32 v222, 48, v222\n"
"ds_bpermute_b32 v200, v222, v221 offset:0\n"
"ds_bpermute_b32 v201, v222, v221 offset:4\n"
"ds_bpermute_b32 v202, v222, v221 offset:8\n"
"ds_bpermute_b32 v203, v222, v221 offset:12\n"
"s_waitcnt lgkmcnt(0)\n"
"v_mul_f32 v128, v128, v200\n"
"v_mul_f32 v129, v129, v201\n"
"v_mul_f32 v130, v130, v202\n"
"v_mul_f32 v131, v131, v203\n"
"v_mul_f32 v132, v132, v200\n"
"v_mul_f32 v133, v133, v201\n"
"v_mul_f32 v134, v134, v202\n"
"v_mul_f32 v135, v135, v203\n"
"v_mul_f32 v136, v136, v200\n"
"v_mul_f32 v137, v137, v201\n"
"v_mul_f32 v138, v138, v202\n"
"v_mul_f32 v139, v139, v203\n"
"v_mul_f32 v140, v140, v200\n"
"v_mul_f32 v141, v141, v201\n"
"v_mul_f32 v142, v142, v202\n"
"v_mul_f32 v143, v143, v203\n"
"v_mul_f32 v144, v144, v200\n"
"v_mul_f32 v145, v145, v201\n"
"v_mul_f32 v146, v146, v202\n"
"v_mul_f32 v147, v147, v203\n"
"v_mul_f32 v148, v148, v200\n"
"v_mul_f32 v149, v149, v201\n"
"v_mul_f32 v150, v150, v202\n"
"v_mul_f32 v151, v151, v203\n"
"v_mul_f32 v152, v152, v200\n"
"v_mul_f32 v153, v153, v201\n"
"v_mul_f32 v154, v154, v202\n"
"v_mul_f32 v155, v155, v203\n"
"v_mul_f32 v156, v156, v200\n"
"v_mul_f32 v157, v157, v201\n"
"v_mul_f32 v158, v158, v202\n"
"v_mul_f32 v159, v159, v203\n"
"s_nop 1\n"
"v_mfma_f32_16x16x32_bf16 v[220:223], v[236:239], v[224:227], 0\n"
"v_mfma_f32_16x16x32_bf16 v[220:223], v[236:239], v[228:231], v[220:223]\n"
"s_nop 7\n"
"s_setpc_b64 s[48:49]\n"
".Lend:\n"
  :: "s"(ka), "s"(wid), "s"(bid) : "v0", "v1", "v2", "v3", "v4", "v5", "v6", "v7", "v8", "v9", "v10", "v11", "v12", "v13", "v14", "v15", "v16", "v17", "v18", "v19", "v20", "v21", "v22", "v23", "v24", "v25", "v26", "v27", "v28", "v29", "v30", "v31", "v32", "v33", "v34", "v35", "v36", "v37", "v38", "v39", "v40", "v41", "v42", "v43", "v44", "v45", "v46", "v47", "v48", "v49", "v50", "v51", "v52", "v53", "v54", "v55", "v56", "v57", "v58", "v59", "v60", "v61", "v62", "v63", "v64", "v65", "v66", "v67", "v68", "v69", "v70", "v71", "v72", "v73", "v74", "v75", "v76", "v77", "v78", "v79", "v80", "v81", "v82", "v83", "v84", "v85", "v86", "v87", "v88", "v89", "v90", "v91", "v92", "v93", "v94", "v95", "v96", "v97", "v98", "v99", "v100", "v101", "v102", "v103", "v104", "v105", "v106", "v107", "v108", "v109", "v110", "v111", "v112", "v113", "v114", "v115", "v116", "v117", "v118", "v119", "v120", "v121", "v122", "v123", "v124", "v125", "v126", "v127", "v128", "v129", "v130", "v131", "v132", "v133", "v134", "v135", "v136", "v137", "v138", "v139", "v140", "v141", "v142", "v143", "v144", "v145", "v146", "v147", "v148", "v149", "v150", "v151", "v152", "v153", "v154", "v155", "v156", "v157", "v158", "v159", "v160", "v161", "v162", "v163", "v164", "v165", "v166", "v167", "v168", "v169", "v170", "v171", "v172", "v173", "v174", "v175", "v176", "v177", "v178", "v179", "v180", "v181", "v182", "v183", "v184", "v185", "v186", "v187", "v188", "v189", "v190", "v191", "v192", "v193", "v194", "v195", "v196", "v197", "v198", "v199", "v200", "v201", "v202", "v203", "v204", "v205", "v206", "v207", "v208", "v209", "v210", "v211", "v212", "v213", "v214", "v215", "v216", "v217", "v218", "v219", "v220", "v221", "v222", "v223", "v224", "v225", "v226", "v227", "v228", "v229", "v230", "v231", "v232", "v233", "v234", "v235", "v236", "v237", "v238", "v239", "v240", "v241", "v242", "v243", "v244", "v245", "v246", "v247", "v248", "v249", "v250", "v251", "v252", "v253", "v254", "v255", "a0", "a1", "a2", "a3", "a4", "a5", "a6", "a7", "a8", "a9", "a10", "a11", "a12", "a13", "a14", "a15", "a16", "a17", "a18", "a19", "a20", "a21", "a22", "a23", "a24", "a25", "a26", "a27", "a28", "a29", "a30", "a31", "a32", "a33", "a34", "a35", "a36", "a37", "a38", "a39", "a40", "a41", "a42", "a43", "a44", "a45", "a46", "a47", "a48", "a49", "a50", "a51", "a52", "a53", "a54", "a55", "a56", "a57", "a58", "a59", "a60", "a61", "a62", "a63", "a64", "a65", "a66", "a67", "a68", "a69", "a70", "a71", "a72", "a73", "a74", "a75", "a76", "a77", "a78", "a79", "a80", "a81", "a82", "a83", "a84", "a85", "a86", "a87", "a88", "a89", "a90", "a91", "a92", "a93", "a94", "a95", "a96", "a97", "a98", "a99", "a100", "a101", "a102", "a103", "a104", "a105", "a106", "a107", "a108", "a109", "a110", "a111", "a112", "a113", "a114", "a115", "a116", "a117", "a118", "a119", "a120", "a121", "a122", "a123", "a124", "a125", "a126", "a127", "a128", "a129", "a130", "a131", "a132", "a133", "a134", "a135", "a136", "a137", "a138", "a139", "a140", "a141", "a142", "a143", "a144", "a145", "a146", "a147", "a148", "a149", "a150", "a151", "a152", "a153", "a154", "a155", "a156", "a157", "a158", "a159", "a160", "a161", "a162", "a163", "a164", "a165", "a166", "a167", "a168", "a169", "a170", "a171", "a172", "a173", "a174", "a175", "a176", "a177", "a178", "a179", "a180", "a181", "a182", "a183", "a184", "a185", "a186", "a187", "a188", "a189", "a190", "a191", "a192", "a193", "a194", "a195", "a196", "a197", "a198", "a199", "a200", "a201", "a202", "a203", "a204", "a205", "a206", "a207", "a208", "a209", "a210", "a211", "a212", "a213", "a214", "a215", "a216", "a217", "a218", "a219", "a220", "a221", "a222", "a223", "a224", "a225", "a226", "a227", "a228", "a229", "a230", "a231", "a232", "a233", "a234", "a235", "a236", "a237", "a238", "a239", "a240", "a241", "a242", "a243", "a244", "a245", "a246", "a247", "a248", "a249", "a250", "a251", "a252", "a253", "a254", "a255", "s20", "s21", "s22", "s23", "s24", "s25", "s26", "s27", "s28", "s29", "s30", "s31", "s36", "s37", "s38", "s39", "s40", "s41", "s42", "s43", "s44", "s45", "s46", "s47", "s48", "s49", "s50", "s51", "s52", "s53", "s54", "s55", "s56", "s57", "s58", "s59", "s60", "s61", "s62", "s63", "s64", "s65", "s66", "s67", "s68", "s69", "s70", "s71", "s72", "s73", "s74", "s75", "s76", "s77", "s78", "s79", "s80", "s81", "s82", "s83", "s84", "s85", "s86", "s87", "s88", "s89", "s90", "s91", "s92", "s93", "s94", "s95", "s96", "s97", "s98", "s99", "vcc", "scc", "memory");
}

extern "C" void kernel_launch(void* const* d_in, const int* in_sizes, int n_in, void* d_out, int out_size, void* d_ws, size_t ws_size, hipStream_t stream){
  (void)hipFuncSetAttribute((const void*)attn_k, hipFuncAttributeMaxDynamicSharedMemorySize, 163840);
  attn_k<<<256,256,163840,stream>>>(d_in[0],d_in[1],d_in[2],d_in[3],d_in[4],d_out,d_ws);
}
```
